```python
import torch

def _softmax(x, axis=-1):
    return torch.softmax(x, dim=axis)
def _gelu(x):
    return torch.nn.functional.gelu(x, approximate="tanh")
def _silu(x):
    return torch.nn.functional.silu(x)
def _sigmoid(x):
    return torch.sigmoid(x)
def _layernorm(x, axis=-1, eps=1e-5):
    m = x.mean(dim=axis, keepdim=True)
    v = x.var(dim=axis, keepdim=True, unbiased=False)
    return (x - m) / torch.sqrt(v + eps)
def _scatter_add(tgt, idx, src, axis=0):
    return tgt.clone().index_add_(axis, idx, src)
def _segment_sum(src, seg, n):
    out = torch.zeros((n,) + src.shape[1:], dtype=src.dtype)
    return out.index_add_(0, seg, src)
def _pad(x, pw):
    flat = tuple(v for pair in reversed(pw) for v in pair)
    return torch.nn.functional.pad(x, flat)

kernel_name = 'standalone__gqa_softmax__near_ridge'

def setup_inputs():

    _w = (lambda a: (lambda t: t.to(torch.bfloat16) if t.is_floating_point() else t)(torch.as_tensor(a)))
    B, Hq, Hkv, Sq, Sk, D = 8, 32, 32, 384, 512, 192
    return {
        "q": _w((lambda *s: torch.randn(*s, dtype=torch.bfloat16) * 0.8)(B, Hq, Sq, D)),
        "k": _w((lambda *s: torch.randn(*s, dtype=torch.bfloat16) * 0.8)(B, Hkv, Sk, D)),
        "v": _w((lambda *s: torch.randn(*s, dtype=torch.bfloat16) * 0.05)(B, Hkv, Sk, D)),
    }

def reference(q, k, v):
    B, Hq, Sq, D = q.shape
    Hkv = k.shape[1]
    rep = Hq // Hkv
    k = (lambda x, n, axis: torch.repeat_interleave(x, n, dim=axis))(k, rep, axis=1)
    v = (lambda x, n, axis: torch.repeat_interleave(x, n, dim=axis))(v, rep, axis=1)
    scale = D ** -0.5
    scores = torch.einsum("bhqd,bhkd->bhqk", q, k) * scale
    p = _softmax(scores, axis=-1)
    return torch.einsum("bhqk,bhkd->bhqd", p, v)
```

```cpp
#include <hip/hip_runtime.h>
#include <hip/hip_bf16.h>
typedef float    f32x16 __attribute__((ext_vector_type(16)));
typedef float    f32x2  __attribute__((ext_vector_type(2)));
typedef __bf16   bf16x8 __attribute__((ext_vector_type(8)));
typedef __bf16   bf16x2 __attribute__((ext_vector_type(2)));
typedef short    s16x4  __attribute__((ext_vector_type(4)));
typedef unsigned u32x4  __attribute__((ext_vector_type(4)));
typedef unsigned u32x2  __attribute__((ext_vector_type(2)));
#define DI __device__ __forceinline__
constexpr int NB = 8, NH = 32, SQ = 384, SK = 512, D = 192;
constexpr float CSC = 0.07216878364870322f * 1.4426950408889634f;

DI unsigned cvtpk(float lo, float hi) { f32x2 v = {lo, hi}; bf16x2 b = __builtin_convertvector(v, bf16x2); return __builtin_bit_cast(unsigned, b); }

DI void slow_rows(const unsigned short* __restrict__ q, const unsigned short* __restrict__ k, const unsigned short* __restrict__ v,
                  unsigned short* __restrict__ o, int bh, int row0, char* lds, int tid) {
  const int lane = tid & 63, w = tid >> 6, r = lane & 31, h = lane >> 5;
  const unsigned short* qp = q + ((size_t)bh * SQ + row0 + 32 * w + r) * D;
  bf16x8 qf[12];
#pragma unroll
  for (int s = 0; s < 12; ++s) qf[s] = *(const bf16x8*)(qp + 16 * s + 8 * h);
  f32x16 acc[6];
#pragma unroll
  for (int db = 0; db < 6; ++db)
#pragma unroll
    for (int i = 0; i < 16; ++i) acc[db][i] = 0.f;
  float m = -INFINITY, l = 0.f;
  const char* kb = (const char*)(k + (size_t)bh * SK * D);
  const char* vb = (const char*)(v + (size_t)bh * SK * D);
  for (int t = 0; t < SK / 32; ++t) {
    __syncthreads();
#pragma unroll
    for (int i = 0; i < 3; ++i) {
      const int n = tid + 256 * i, row = n / 24, c = n % 24;
      const u32x4 kv = *(const u32x4*)(kb + (size_t)(32 * t + row) * 384 + 16 * c);
      const u32x4 vv = *(const u32x4*)(vb + (size_t)(32 * t + row) * 384 + 16 * c);
      const int sub = 3072 * (row >> 3) + 512 * (c >> 2) + 64 * (row & 7);
      *(u32x4*)(lds + sub + 16 * ((c & 3) ^ ((row >> 2) & 3))) = kv;
      *(u32x4*)(lds + 12288 + sub + 16 * (c & 3)) = vv;
    }
    __syncthreads();
    f32x16 s;
#pragma unroll
    for (int i = 0; i < 16; ++i) s[i] = 0.f;
#pragma unroll
    for (int st = 0; st < 12; ++st) {
      const int c = 2 * st + h;
      const bf16x8 kf = *(const bf16x8*)(lds + 3072 * (r >> 3) + 512 * (c >> 2) + 64 * (r & 7) + 16 * ((c & 3) ^ ((r >> 2) & 3)));
      s = __builtin_amdgcn_mfma_f32_32x32x16_bf16(kf, qf[st], s, 0, 0, 0);
    }
    float mx = s[0];
#pragma unroll
    for (int i = 1; i < 16; ++i) mx = fmaxf(mx, s[i]);
    mx = fmaxf(mx, __shfl_xor(mx, 32));
    const float mn = fmaxf(m, mx * CSC);
    const float f = __builtin_amdgcn_exp2f(m - mn);
    m = mn;
    float rs = 0.f;
#pragma unroll
    for (int i = 0; i < 16; ++i) { s[i] = __builtin_amdgcn_exp2f(__builtin_fmaf(s[i], CSC, -mn)); rs += s[i]; }
    rs += __shfl_xor(rs, 32);
    l = l * f + rs;
#pragma unroll
    for (int db = 0; db < 6; ++db)
#pragma unroll
      for (int i = 0; i < 16; ++i) acc[db][i] *= f;
    const int i16 = lane & 15, qq = i16 >> 2, p = i16 & 3, blk = (lane >> 4) & 1;
#pragma unroll
    for (int ks = 0; ks < 2; ++ks) {
      u32x4 pw;
#pragma unroll
      for (int e = 0; e < 4; ++e) pw[e] = cvtpk(s[8 * ks + 2 * e], s[8 * ks + 2 * e + 1]);
      const bf16x8 pf = __builtin_bit_cast(bf16x8, pw);
#pragma unroll
      for (int db = 0; db < 6; ++db) {
        typedef __attribute__((address_space(3))) s16x4* lp;
        const char* a0 = lds + 12288 + 3072 * (2 * ks) + 512 * db + 64 * (4 * h + qq) + 32 * blk + 8 * p;
        const s16x4 lo = __builtin_amdgcn_ds_read_tr16_b64_v4i16((lp)a0);
        const s16x4 hi = __builtin_amdgcn_ds_read_tr16_b64_v4i16((lp)(a0 + 3072));
        const bf16x8 vf = __builtin_bit_cast(bf16x8, __builtin_shufflevector(lo, hi, 0, 1, 2, 3, 4, 5, 6, 7));
        acc[db] = __builtin_amdgcn_mfma_f32_32x32x16_bf16(vf, pf, acc[db], 0, 0, 0);
      }
    }
  }
  const float il = 1.f / l;
  unsigned short* op = o + ((size_t)bh * SQ + row0 + 32 * w + r) * D;
#pragma unroll
  for (int db = 0; db < 6; ++db)
#pragma unroll
    for (int g = 0; g < 4; ++g) {
      u32x2 wv;
      wv[0] = cvtpk(acc[db][4 * g] * il, acc[db][4 * g + 1] * il);
      wv[1] = cvtpk(acc[db][4 * g + 2] * il, acc[db][4 * g + 3] * il);
      *(u32x2*)(op + 32 * db + 8 * g + 4 * h) = wv;
    }
}

__global__ __launch_bounds__(256) void attn_base(const unsigned short* __restrict__ q, const unsigned short* __restrict__ k,
                                                 const unsigned short* __restrict__ v, unsigned short* __restrict__ o) {
  __shared__ __attribute__((aligned(16))) char lds[24576];
  const int blk = blockIdx.x;
  const int xcd = blk & 7, idx = blk >> 3;
  const int bh = xcd * 32 + idx / 3, part = idx % 3;
  slow_rows(q, k, v, o, bh, part * 128, lds, threadIdx.x);
}

extern "C" void kernel_launch(void* const* d_in, const int* in_sizes, int n_in, void* d_out, int out_size, void* d_ws, size_t ws_size,
                              hipStream_t stream) {
  attn_base<<<NB * NH * 3, 256, 0, stream>>>((const unsigned short*)d_in[0], (const unsigned short*)d_in[1], (const unsigned short*)d_in[2],
                                             (unsigned short*)d_out);
}
```

```cpp
#include <hip/hip_runtime.h>
#include <hip/hip_bf16.h>

typedef float    f32x4  __attribute__((ext_vector_type(4)));
typedef float    f32x2  __attribute__((ext_vector_type(2)));
typedef __bf16   bf16x8 __attribute__((ext_vector_type(8)));
typedef __bf16   bf16x2 __attribute__((ext_vector_type(2)));
typedef short    s16x4  __attribute__((ext_vector_type(4)));
typedef unsigned u32x4  __attribute__((ext_vector_type(4)));
typedef unsigned u32x2  __attribute__((ext_vector_type(2)));

static __device__ __forceinline__ unsigned cvtpk(float lo, float hi) {
  f32x2 x = {lo, hi}; bf16x2 b = __builtin_convertvector(x, bf16x2); return __builtin_bit_cast(unsigned, b);
}

static __device__ __forceinline__ void rows_true_max(const char* __restrict__ q, const char* __restrict__ k, const char* __restrict__ v,
                                                     char* __restrict__ o, unsigned bh, unsigned row0, char* vt) {
  unsigned lane;
  asm volatile("v_mbcnt_lo_u32_b32 %0, -1, 0\n\tv_mbcnt_hi_u32_b32 %0, -1, %0" : "=v"(lane));
  const unsigned r16 = lane & 15u, g = lane >> 4;
  const float C = 0.07216878364870322f * 1.4426950408889634f;
  const char* qp = q + ((size_t)bh * 384 + row0 + r16) * 384 + 16 * g;
  bf16x8 qf[3][6];
#pragma unroll
  for (int i = 0; i < 3; ++i)
#pragma unroll
    for (int s = 0; s < 6; ++s) qf[i][s] = *(const bf16x8*)(qp + i * 6144 + 64 * s);
  f32x4 acc[3][12];
  float m[3], l[3];
#pragma unroll
  for (int i = 0; i < 3; ++i) {
    m[i] = -INFINITY; l[i] = 0.f;
#pragma unroll
    for (int c = 0; c < 12; ++c) acc[i][c] = f32x4{0.f, 0.f, 0.f, 0.f};
  }
  const char* kb = k + (size_t)bh * 196608;
  const char* vb = v + (size_t)bh * 196608;
#pragma unroll 1
  for (int t = 0; t < 16; ++t) {
#pragma unroll
    for (int j = 0; j < 12; ++j) *(u32x4*)(vt + 1024 * j + 16 * lane) = *(const u32x4*)(vb + 12288 * t + 1024 * j + 16 * lane);
    f32x4 s[2][3];
#pragma unroll
    for (int tau = 0; tau < 2; ++tau) {
      const char* kp = kb + (size_t)(32 * t + 8 * (r16 >> 2) + 4 * tau + (r16 & 3)) * 384 + 16 * g;
#pragma unroll
      for (int i = 0; i < 3; ++i) s[tau][i] = f32x4{0.f, 0.f, 0.f, 0.f};
#pragma unroll
      for (int st = 0; st < 6; ++st) {
        const bf16x8 kf = *(const bf16x8*)(kp + 64 * st);
#pragma unroll
        for (int i = 0; i < 3; ++i) s[tau][i] = __builtin_amdgcn_mfma_f32_16x16x32_bf16(kf, qf[i][st], s[tau][i], 0, 0, 0);
      }
    }
    bf16x8 pf[3];
#pragma unroll
    for (int i = 0; i < 3; ++i) {
      float mx = fmaxf(fmaxf(fmaxf(s[0][i][0], s[0][i][1]), fmaxf(s[0][i][2], s[0][i][3])),
                       fmaxf(fmaxf(s[1][i][0], s[1][i][1]), fmaxf(s[1][i][2], s[1][i][3])));
      mx = fmaxf(mx, __shfl_xor(mx, 16));
      mx = fmaxf(mx, __shfl_xor(mx, 32));
      const float mn = fmaxf(m[i], mx * C);
      const float f = __builtin_amdgcn_exp2f(m[i] - mn);
      m[i] = mn;
      float rs = 0.f;
      u32x4 pw;
#pragma unroll
      for (int tau = 0; tau < 2; ++tau) {
        float e[4];
#pragma unroll
        for (int r = 0; r < 4; ++r) { e[r] = __builtin_amdgcn_exp2f(__builtin_fmaf(s[tau][i][r], C, -mn)); rs += e[r]; }
        pw[2 * tau] = cvtpk(e[0], e[1]); pw[2 * tau + 1] = cvtpk(e[2], e[3]);
      }
      rs += __shfl_xor(rs, 16);
      rs += __shfl_xor(rs, 32);
      l[i] = l[i] * f + rs;
      pf[i] = __builtin_bit_cast(bf16x8, pw);
#pragma unroll
      for (int c = 0; c < 12; ++c) acc[i][c] *= f;
    }
    __builtin_amdgcn_wave_barrier();
    typedef __attribute__((address_space(3))) s16x4* lp;
    const char* vr = vt + (8 * g + ((lane >> 2) & 3u)) * 384 + 8 * (lane & 3u);
#pragma unroll
    for (int c = 0; c < 12; ++c) {
      const s16x4 lo = __builtin_amdgcn_ds_read_tr16_b64_v4i16((lp)(vr + 32 * c));
      const s16x4 hi = __builtin_amdgcn_ds_read_tr16_b64_v4i16((lp)(vr + 32 * c + 4 * 384));
      const bf16x8 vf = __builtin_bit_cast(bf16x8, __builtin_shufflevector(lo, hi, 0, 1, 2, 3, 4, 5, 6, 7));
#pragma unroll
      for (int i = 0; i < 3; ++i) acc[i][c] = __builtin_amdgcn_mfma_f32_16x16x32_bf16(vf, pf[i], acc[i][c], 0, 0, 0);
    }
    __builtin_amdgcn_wave_barrier();
  }
#pragma unroll
  for (int i = 0; i < 3; ++i) {
    const float il = 1.f / l[i];
    char* op = o + ((size_t)bh * 384 + row0 + 16 * i + r16) * 384 + 8 * g;
#pragma unroll
    for (int c = 0; c < 12; ++c) {
      u32x2 x;
      x[0] = cvtpk(acc[i][c][0] * il, acc[i][c][1] * il);
      x[1] = cvtpk(acc[i][c][2] * il, acc[i][c][3] * il);
      *(u32x2*)(op + 32 * c) = x;
    }
  }
}

__global__ __launch_bounds__(256, 1) void attn_fwd_d192(const char* __restrict__ q, const char* __restrict__ k, const char* __restrict__ v,
                                                        char* __restrict__ o) {
  extern __shared__ __attribute__((aligned(16))) char lds[];
  const unsigned b = blockIdx.x;
  const unsigned hb = (b & 7u) * 32u + ((b >> 4) << 1);
  const unsigned half = (b >> 3) & 1u;
  const unsigned idx = b >> 3;
  const unsigned w = __builtin_amdgcn_readfirstlane(threadIdx.x >> 6);
  unsigned bad;
  asm volatile(
"s_nop 4\n"
"v_mbcnt_lo_u32_b32 v255, -1, 0\n"
"v_mbcnt_hi_u32_b32 v255, -1, v255\n"
"s_mul_i32 s68, %5, 147456\n"
"s_mov_b64 s[44:45], %1\n"
"s_add_u32 s44, s44, s68\n"
"s_addc_u32 s45, s45, 0\n"
"s_and_b32 s45, s45, 0xffff\n"
"s_mov_b32 s46, 294912\n"
"s_mov_b32 s47, 0x20000\n"
"s_mov_b32 s60, 0\n"
"s_mov_b32 s61, 6144\n"
"s_mov_b32 s62, 12288\n"
"s_mul_i32 s69, %7, 73728\n"
"s_mul_i32 s68, %6, 18432\n"
"s_add_u32 s69, s69, s68\n"
"v_and_b32 v250, 15, v255\n"
"v_lshrrev_b32 v247, 4, v255\n"
"v_mul_u32_u24 v250, 384, v250\n"
"v_add_u32 v250, s69, v250\n"
"v_lshl_add_u32 v243, v247, 4, v250\n"
"s_nop 1\n"
"s_getpc_b64 s[66:67]\n"
"L_cs_%=:\n"
"s_lshl_b32 s68, %8, 2\n"
"s_add_u32 s68, s68, %6\n"
"s_lshl_b32 s68, s68, 9\n"
"v_and_b32 v246, 3, v255\n"
"v_lshlrev_b32 v246, 7, v246\n"
"v_add_u32 v246, s68, v246\n"
"v_min_u32 v246, L_ce_%=-L_cs_%=-4, v246\n"
"global_load_dword v249, v246, s[66:67]\n"
"s_mul_i32 s68, %5, 196608\n"
"s_mov_b64 s[36:37], %2\n"
"s_add_u32 s36, s36, s68\n"
"s_addc_u32 s37, s37, 0\n"
"s_and_b32 s37, s37, 0xffff\n"
"s_mov_b32 s38, 393216\n"
"s_mov_b32 s39, 0x20000\n"
"s_mul_i32 s52, %6, 40000\n"
"s_and_b32 s68, %6, 1\n"
"s_lshl_b32 s68, s68, 5\n"
"s_add_u32 s52, s52, s68\n"
"v_bfe_u32 v246, v255, 2, 3\n"
"v_lshrrev_b32 v248, 5, v255\n"
"v_mul_u32_u24 v246, 384, v246\n"
"v_and_b32 v251, 3, v255\n"
"v_lshl_add_u32 v246, v248, 6, v246\n"
"s_mul_i32 s68, %6, 3072\n"
"v_lshl_add_u32 v246, v251, 4, v246\n"
"v_add_u32 v242, s68, v246\n"
"s_mul_i32 s69, %7, 73728\n"
"s_mul_i32 s68, %6, 15360\n"
"s_add_u32 s69, s69, s68\n"
"v_add_u32 v216, s69, v242\n"
"v_bfe_u32 v248, v255, 3, 1\n"
"v_and_b32 v246, 7, v255\n"
"v_mul_u32_u24 v248, 3072, v248\n"
"v_lshl_add_u32 v248, v246, 6, v248\n"
"v_lshl_add_u32 v248, v247, 4, v248\n"
"v_add_u32 v217, s52, v248\n"
"s_mov_b32 s68, 0\n"
"s_add_u32 m0, s52, 15360\n"
"s_nop 0\n"
"buffer_load_dwordx4 v216, s[44:47], s68 offen offset:0 sc0 nt lds\n"
"s_add_u32 m0, s52, 16256\n"
"s_nop 0\n"
"buffer_load_dwordx4 v216, s[44:47], s68 offen offset:128 sc0 nt lds\n"
"s_add_u32 m0, s52, 17152\n"
"s_nop 0\n"
"buffer_load_dwordx4 v216, s[44:47], s68 offen offset:256 sc0 nt lds\n"
"s_mov_b32 s68, 3072\n"
"s_add_u32 m0, s52, 18432\n"
"s_nop 0\n"
"buffer_load_dwordx4 v216, s[44:47], s68 offen offset:0 sc0 nt lds\n"
"s_add_u32 m0, s52, 19328\n"
"s_nop 0\n"
"buffer_load_dwordx4 v216, s[44:47], s68 offen offset:128 sc0 nt lds\n"
"s_add_u32 m0, s52, 20224\n"
"s_nop 0\n"
"buffer_load_dwordx4 v216, s[44:47], s68 offen offset:256 sc0 nt lds\n"
"s_mov_b32 s68, 6144\n"
"s_add_u32 m0, s52, 27648\n"
"s_nop 0\n"
"buffer_load_dwordx4 v216, s[44:47], s68 offen offset:0 sc0 nt lds\n"
"s_add_u32 m0, s52, 28544\n"
"s_nop 0\n"
"buffer_load_dwordx4 v216, s[44:47], s68 offen offset:128 sc0 nt lds\n"
"s_add_u32 m0, s52, 29440\n"
"s_nop 0\n"
"buffer_load_dwordx4 v216, s[44:47], s68 offen offset:256 sc0 nt lds\n"
"s_mov_b32 s68, 9216\n"
"s_add_u32 m0, s52, 30720\n"
"s_nop 0\n"
"buffer_load_dwordx4 v216, s[44:47], s68 offen offset:0 sc0 nt lds\n"
"s_add_u32 m0, s52, 31616\n"
"s_nop 0\n"
"buffer_load_dwordx4 v216, s[44:47], s68 offen offset:128 sc0 nt lds\n"
"s_add_u32 m0, s52, 32512\n"
"s_nop 0\n"
"buffer_load_dwordx4 v216, s[44:47], s68 offen offset:256 sc0 nt lds\n"
"s_mov_b32 s68, 12288\n"
"s_add_u32 m0, s52, 33792\n"
"s_nop 0\n"
"buffer_load_dwordx4 v216, s[44:47], s68 offen offset:0 sc0 nt lds\n"
"s_add_u32 m0, s52, 34688\n"
"s_nop 0\n"
"buffer_load_dwordx4 v216, s[44:47], s68 offen offset:128 sc0 nt lds\n"
"s_add_u32 m0, s52, 35584\n"
"s_nop 0\n"
"buffer_load_dwordx4 v216, s[44:47], s68 offen offset:256 sc0 nt lds\n"
"s_mov_b32 s68, 15360\n"
"s_add_u32 m0, s52, 36864\n"
"s_nop 0\n"
"buffer_load_dwordx4 v216, s[44:47], s68 offen offset:0 sc0 nt lds\n"
"s_add_u32 m0, s52, 37760\n"
"s_nop 0\n"
"buffer_load_dwordx4 v216, s[44:47], s68 offen offset:128 sc0 nt lds\n"
"s_add_u32 m0, s52, 38656\n"
"s_nop 0\n"
"buffer_load_dwordx4 v216, s[44:47], s68 offen offset:256 sc0 nt lds\n"
"s_mov_b32 s53, 0\n"
"s_add_u32 m0, s52, 0\n"
"s_nop 0\n"
"buffer_load_dwordx4 v242, s[36:39], s53 offen offset:0 sc0 nt lds\n"
"s_add_u32 m0, s52, 896\n"
"s_nop 0\n"
"buffer_load_dwordx4 v242, s[36:39], s53 offen offset:128 sc0 nt lds\n"
"s_add_u32 m0, s52, 1792\n"
"s_nop 0\n"
"buffer_load_dwordx4 v242, s[36:39], s53 offen offset:256 sc0 nt lds\n"
"s_mov_b32 s54, 12288\n"
"s_add_u32 m0, s52, 3072\n"
"s_nop 0\n"
"buffer_load_dwordx4 v242, s[36:39], s54 offen offset:0 sc0 nt lds\n"
"s_add_u32 m0, s52, 3968\n"
"s_nop 0\n"
"buffer_load_dwordx4 v242, s[36:39], s54 offen offset:128 sc0 nt lds\n"
"s_add_u32 m0, s52, 4864\n"
"s_nop 0\n"
"buffer_load_dwordx4 v242, s[36:39], s54 offen offset:256 sc0 nt lds\n"
"s_mul_i32 s68, %5, 196608\n"
"s_mov_b64 s[40:41], %3\n"
"s_add_u32 s40, s40, s68\n"
"s_addc_u32 s41, s41, 0\n"
"s_and_b32 s41, s41, 0xffff\n"
"s_mov_b32 s42, 393216\n"
"s_mov_b32 s43, 0x20000\n"
"s_mul_i32 s68, %5, 147456\n"
"s_mov_b64 s[48:49], %4\n"
"s_add_u32 s48, s48, s68\n"
"s_addc_u32 s49, s49, 0\n"
"s_and_b32 s49, s49, 0xffff\n"
"s_mov_b32 s50, 294912\n"
"s_mov_b32 s51, 0x20000\n"
"s_mov_b32 s63, 147456\n"
"s_mov_b32 s64, 153600\n"
"s_mov_b32 s65, 159744\n"
"s_mov_b32 s55, 0x3dd53b95\n"
"s_mov_b32 s56, 0xffff0000\n"
"s_mov_b32 s57, 0x21800000\n"
"s_mov_b32 s58, 0x3c000000\n"
"v_and_b32 v248, 1, v247\n"
"v_lshrrev_b32 v244, 1, v247\n"
"v_lshlrev_b32 v248, 1, v248\n"
"v_or_b32 v248, v248, v244\n"
"v_lshl_add_u32 v244, v248, 4, v250\n"
"v_bfe_u32 v246, v255, 2, 2\n"
"v_mul_u32_u24 v240, 40000, v246\n"
"v_and_b32 v248, 1, v246\n"
"v_lshl_add_u32 v240, v248, 5, v240\n"
"v_lshl_add_u32 v240, v251, 6, v240\n"
"v_lshl_add_u32 v240, v247, 4, v240\n"
"v_mul_u32_u24 v241, 40000, v247\n"
"v_and_b32 v248, 1, v247\n"
"v_lshl_add_u32 v241, v248, 5, v241\n"
"v_lshl_add_u32 v241, v246, 6, v241\n"
"v_lshl_add_u32 v241, v251, 3, v241\n"
"v_and_b32 v248, 15, v255\n"
"v_mul_u32_u24 v248, 144, v248\n"
"v_lshl_add_u32 v248, v247, 3, v248\n"
"v_add_u32 v218, s52, v248\n"
"v_lshrrev_b32 v246, 3, v255\n"
"v_and_b32 v251, 7, v255\n"
"v_mul_u32_u24 v248, 144, v246\n"
"v_lshl_add_u32 v248, v251, 4, v248\n"
"v_add_u32 v219, s52, v248\n"
"s_mul_i32 s69, %7, 73728\n"
"s_mul_i32 s68, %6, 18432\n"
"s_add_u32 s69, s69, s68\n"
"v_mul_u32_u24 v248, 384, v246\n"
"v_lshl_add_u32 v248, v251, 4, v248\n"
"v_add_u32 v220, s69, v248\n"
"v_lshl_add_u32 v221, v247, 3, v218\n"
"v_and_b32 v251, 3, v255\n"
"v_mov_b32 v232, 0\n"
"v_mov_b32 v233, 0\n"
"v_mov_b32 v234, 0\n"
"v_mov_b32 v235, 0\n"
"v_mov_b32 v236, 0\n"
"v_mov_b32 v237, 0\n"
"v_mov_b32 v238, 0\n"
"v_mov_b32 v246, 0x3f803f80\n"
"v_accvgpr_write_b32 a252, v246\n"
"v_accvgpr_write_b32 a253, v246\n"
"v_accvgpr_write_b32 a254, v246\n"
"v_accvgpr_write_b32 a255, v246\n"
"s_waitcnt vmcnt(18)\n"
"ds_read_b128 a[0:3], v217 offset:15360\n"
"ds_read_b128 a[4:7], v217 offset:15872\n"
"ds_read_b128 a[8:11], v217 offset:16384\n"
"ds_read_b128 a[12:15], v217 offset:16896\n"
"ds_read_b128 a[16:19], v217 offset:17408\n"
"ds_read_b128 a[20:23], v217 offset:17920\n"
"s_mov_b32 s53, 24576\n"
"s_add_u32 m0, s52, 6144\n"
"s_nop 0\n"
"buffer_load_dwordx4 v242, s[36:39], s53 offen offset:0 sc0 nt lds\n"
"s_add_u32 m0, s52, 7040\n"
"s_nop 0\n"
"buffer_load_dwordx4 v242, s[36:39], s53 offen offset:128 sc0 nt lds\n"
"s_add_u32 m0, s52, 7936\n"
"s_nop 0\n"
"buffer_load_dwordx4 v242, s[36:39], s53 offen offset:256 sc0 nt lds\n"
"s_waitcnt vmcnt(15)\n"
"ds_read_b128 a[24:27], v217 offset:27648\n"
"ds_read_b128 a[28:31], v217 offset:28160\n"
"ds_read_b128 a[32:35], v217 offset:28672\n"
"ds_read_b128 a[36:39], v217 offset:29184\n"
"ds_read_b128 a[40:43], v217 offset:29696\n"
"ds_read_b128 a[44:47], v217 offset:30208\n"
"s_mov_b32 s54, 36864\n"
"s_add_u32 m0, s52, 9216\n"
"s_nop 0\n"
"buffer_load_dwordx4 v242, s[36:39], s54 offen offset:0 sc0 nt lds\n"
"s_add_u32 m0, s52, 10112\n"
"s_nop 0\n"
"buffer_load_dwordx4 v242, s[36:39], s54 offen offset:128 sc0 nt lds\n"
"s_add_u32 m0, s52, 11008\n"
"s_nop 0\n"
"buffer_load_dwordx4 v242, s[36:39], s54 offen offset:256 sc0 nt lds\n"
"s_waitcnt vmcnt(12)\n"
"ds_read_b128 a[48:51], v217 offset:33792\n"
"ds_read_b128 a[52:55], v217 offset:34304\n"
"ds_read_b128 a[56:59], v217 offset:34816\n"
"ds_read_b128 a[60:63], v217 offset:35328\n"
"ds_read_b128 a[64:67], v217 offset:35840\n"
"ds_read_b128 a[68:71], v217 offset:36352\n"
"s_waitcnt vmcnt(6)\n"
"s_waitcnt lgkmcnt(0)\n"
"s_barrier\n"
"ds_read_b128 a[144:147], v240 offset:0\n"
"ds_read_b128 a[148:151], v240 offset:512\n"
"ds_read_b128 a[152:155], v240 offset:1024\n"
"ds_read_b128 a[156:159], v240 offset:1536\n"
"s_waitcnt lgkmcnt(2)\n"
"v_mfma_f32_16x16x32_bf16 v[144:147], a[144:147], a[0:3], 0\n"
"s_mov_b32 s53, 49152\n"
"ds_read_b128 a[160:163], v240 offset:2048\n"
"ds_read_b128 a[164:167], v240 offset:2560\n"
"v_mfma_f32_16x16x32_bf16 v[144:147], a[148:151], a[4:7], v[144:147]\n"
"s_add_u32 m0, s52, 12288\n"
"v_mfma_f32_16x16x32_bf16 v[148:151], a[148:151], a[28:31], 0\n"
"buffer_load_dwordx4 v242, s[36:39], s53 offen offset:0 sc0 nt lds\n"
"v_mfma_f32_16x16x32_bf16 v[148:151], a[144:147], a[24:27], v[148:151]\n"
"s_add_u32 m0, s52, 13184\n"
"v_mfma_f32_16x16x32_bf16 v[152:155], a[144:147], a[48:51], 0\n"
"buffer_load_dwordx4 v242, s[36:39], s53 offen offset:128 sc0 nt lds\n"
"v_mfma_f32_16x16x32_bf16 v[152:155], a[148:151], a[52:55], v[152:155]\n"
"s_waitcnt lgkmcnt(2)\n"
"v_mfma_f32_16x16x32_bf16 v[144:147], a[152:155], a[8:11], v[144:147]\n"
"ds_read_b128 a[144:147], v240 offset:256\n"
"ds_read_b128 a[148:151], v240 offset:768\n"
"v_mfma_f32_16x16x32_bf16 v[144:147], a[156:159], a[12:15], v[144:147]\n"
"s_add_u32 m0, s52, 14080\n"
"v_mfma_f32_16x16x32_bf16 v[148:151], a[156:159], a[36:39], v[148:151]\n"
"s_mov_b32 s54, 61440\n"
"buffer_load_dwordx4 v242, s[36:39], s53 offen offset:256 sc0 nt lds\n"
"v_mfma_f32_16x16x32_bf16 v[148:151], a[152:155], a[32:35], v[148:151]\n"
"s_add_u32 m0, s52, 15360\n"
"v_mfma_f32_16x16x32_bf16 v[152:155], a[152:155], a[56:59], v[152:155]\n"
"buffer_load_dwordx4 v242, s[36:39], s54 offen offset:0 sc0 nt lds\n"
"v_mfma_f32_16x16x32_bf16 v[152:155], a[156:159], a[60:63], v[152:155]\n"
"s_waitcnt lgkmcnt(2)\n"
"v_mfma_f32_16x16x32_bf16 v[144:147], a[160:163], a[16:19], v[144:147]\n"
"ds_read_b128 a[152:155], v240 offset:1280\n"
"ds_read_b128 a[156:159], v240 offset:1792\n"
"v_mfma_f32_16x16x32_bf16 v[144:147], a[164:167], a[20:23], v[144:147]\n"
"s_add_u32 m0, s52, 16256\n"
"v_mfma_f32_16x16x32_bf16 v[148:151], a[164:167], a[44:47], v[148:151]\n"
"buffer_load_dwordx4 v242, s[36:39], s54 offen offset:128 sc0 nt lds\n"
"v_mfma_f32_16x16x32_bf16 v[148:151], a[160:163], a[40:43], v[148:151]\n"
"s_add_u32 m0, s52, 17152\n"
"v_mfma_f32_16x16x32_bf16 v[152:155], a[160:163], a[64:67], v[152:155]\n"
"buffer_load_dwordx4 v242, s[36:39], s54 offen offset:256 sc0 nt lds\n"
"v_mfma_f32_16x16x32_bf16 v[152:155], a[164:167], a[68:71], v[152:155]\n"
"s_waitcnt lgkmcnt(2)\n"
"v_mfma_f32_16x16x32_bf16 v[156:159], a[144:147], a[0:3], 0\n"
"s_mov_b32 s59, 0\n"
"ds_read_b128 a[160:163], v240 offset:2304\n"
"ds_read_b128 a[164:167], v240 offset:2816\n"
"v_mfma_f32_16x16x32_bf16 v[156:159], a[148:151], a[4:7], v[156:159]\n"
"s_add_u32 m0, s52, 21504\n"
"v_mfma_f32_16x16x32_bf16 v[160:163], a[148:151], a[28:31], 0\n"
"buffer_load_dwordx4 v242, s[40:43], s59 offen offset:0 sc0 nt lds\n"
"v_mfma_f32_16x16x32_bf16 v[160:163], a[144:147], a[24:27], v[160:163]\n"
"s_add_u32 m0, s52, 22400\n"
"v_mul_f32 v144, s55, v144\n"
"v_mul_f32 v145, s55, v145\n"
"v_mfma_f32_16x16x32_bf16 v[164:167], a[144:147], a[48:51], 0\n"
"buffer_load_dwordx4 v242, s[40:43], s59 offen offset:128 sc0 nt lds\n"
"v_mfma_f32_16x16x32_bf16 v[164:167], a[148:151], a[52:55], v[164:167]\n"
"s_waitcnt lgkmcnt(2)\n"
"v_mul_f32 v146, s55, v146\n"
"v_mul_f32 v147, s55, v147\n"
"v_mfma_f32_16x16x32_bf16 v[156:159], a[152:155], a[8:11], v[156:159]\n"
"ds_read_b128 a[144:147], v240 offset:3072\n"
"ds_read_b128 a[148:151], v240 offset:3584\n"
"v_mfma_f32_16x16x32_bf16 v[156:159], a[156:159], a[12:15], v[156:159]\n"
"s_add_u32 m0, s52, 23296\n"
"v_exp_f32 v144, v144\n"
"v_mfma_f32_16x16x32_bf16 v[160:163], a[156:159], a[36:39], v[160:163]\n"
"s_mov_b32 s70, 12288\n"
"buffer_load_dwordx4 v242, s[40:43], s59 offen offset:256 sc0 nt lds\n"
"v_mfma_f32_16x16x32_bf16 v[160:163], a[152:155], a[32:35], v[160:163]\n"
"s_add_u32 m0, s52, 24576\n"
"v_exp_f32 v145, v145\n"
"v_mfma_f32_16x16x32_bf16 v[164:167], a[152:155], a[56:59], v[164:167]\n"
"buffer_load_dwordx4 v242, s[40:43], s70 offen offset:0 sc0 nt lds\n"
"v_mfma_f32_16x16x32_bf16 v[164:167], a[156:159], a[60:63], v[164:167]\n"
"s_waitcnt lgkmcnt(2)\n"
"v_exp_f32 v146, v146\n"
"v_mfma_f32_16x16x32_bf16 v[156:159], a[160:163], a[16:19], v[156:159]\n"
"ds_read_b128 a[152:155], v240 offset:4096\n"
"ds_read_b128 a[156:159], v240 offset:4608\n"
"v_mfma_f32_16x16x32_bf16 v[156:159], a[164:167], a[20:23], v[156:159]\n"
"s_add_u32 m0, s52, 25472\n"
"v_exp_f32 v147, v147\n"
"v_mfma_f32_16x16x32_bf16 v[160:163], a[164:167], a[44:47], v[160:163]\n"
"buffer_load_dwordx4 v242, s[40:43], s70 offen offset:128 sc0 nt lds\n"
"v_mfma_f32_16x16x32_bf16 v[160:163], a[160:163], a[40:43], v[160:163]\n"
"s_add_u32 m0, s52, 26368\n"
"v_mul_f32 v148, s55, v148\n"
"v_mul_f32 v149, s55, v149\n"
"v_mfma_f32_16x16x32_bf16 v[164:167], a[160:163], a[64:67], v[164:167]\n"
"buffer_load_dwordx4 v242, s[40:43], s70 offen offset:256 sc0 nt lds\n"
"v_mfma_f32_16x16x32_bf16 v[164:167], a[164:167], a[68:71], v[164:167]\n"
"s_waitcnt lgkmcnt(2)\n"
"v_mul_f32 v150, s55, v150\n"
"v_mul_f32 v151, s55, v151\n"
"v_mfma_f32_16x16x32_bf16 v[168:171], a[144:147], a[0:3], 0\n"
"ds_read_b128 a[160:163], v240 offset:5120\n"
"ds_read_b128 a[164:167], v240 offset:5632\n"
"v_mfma_f32_16x16x32_bf16 v[168:171], a[148:151], a[4:7], v[168:171]\n"
"v_cvt_pk_bf16_f32 v192, v144, v145\n"
"v_cvt_pk_bf16_f32 v193, v146, v147\n"
"v_mfma_f32_16x16x32_bf16 v[172:175], a[148:151], a[28:31], 0\n"
"v_exp_f32 v148, v148\n"
"v_mfma_f32_16x16x32_bf16 v[172:175], a[144:147], a[24:27], v[172:175]\n"
"v_exp_f32 v149, v149\n"
"v_mfma_f32_16x16x32_bf16 v[176:179], a[144:147], a[48:51], 0\n"
"v_exp_f32 v150, v150\n"
"v_mfma_f32_16x16x32_bf16 v[176:179], a[148:151], a[52:55], v[176:179]\n"
"s_waitcnt lgkmcnt(2)\n"
"v_exp_f32 v151, v151\n"
"v_mfma_f32_16x16x32_bf16 v[168:171], a[152:155], a[8:11], v[168:171]\n"
"ds_read_b128 a[144:147], v240 offset:3328\n"
"ds_read_b128 a[148:151], v240 offset:3840\n"
"v_mfma_f32_16x16x32_bf16 v[168:171], a[156:159], a[12:15], v[168:171]\n"
"v_mul_f32 v152, s55, v152\n"
"v_mul_f32 v153, s55, v153\n"
"v_mfma_f32_16x16x32_bf16 v[172:175], a[156:159], a[36:39], v[172:175]\n"
"v_mul_f32 v154, s55, v154\n"
"v_mul_f32 v155, s55, v155\n"
"v_mfma_f32_16x16x32_bf16 v[172:175], a[152:155], a[32:35], v[172:175]\n"
"v_cvt_pk_bf16_f32 v196, v148, v149\n"
"v_cvt_pk_bf16_f32 v197, v150, v151\n"
"v_mfma_f32_16x16x32_bf16 v[176:179], a[152:155], a[56:59], v[176:179]\n"
"v_exp_f32 v152, v152\n"
"v_mfma_f32_16x16x32_bf16 v[176:179], a[156:159], a[60:63], v[176:179]\n"
"s_waitcnt lgkmcnt(2)\n"
"v_exp_f32 v153, v153\n"
"v_mfma_f32_16x16x32_bf16 v[168:171], a[160:163], a[16:19], v[168:171]\n"
"ds_read_b128 a[152:155], v240 offset:4352\n"
"ds_read_b128 a[156:159], v240 offset:4864\n"
"v_mfma_f32_16x16x32_bf16 v[168:171], a[164:167], a[20:23], v[168:171]\n"
"v_exp_f32 v154, v154\n"
"v_mfma_f32_16x16x32_bf16 v[172:175], a[164:167], a[44:47], v[172:175]\n"
"v_exp_f32 v155, v155\n"
"v_mfma_f32_16x16x32_bf16 v[172:175], a[160:163], a[40:43], v[172:175]\n"
"v_cvt_pk_bf16_f32 v200, v152, v153\n"
"v_cvt_pk_bf16_f32 v201, v154, v155\n"
"v_mfma_f32_16x16x32_bf16 v[176:179], a[160:163], a[64:67], v[176:179]\n"
"v_mul_f32 v156, s55, v156\n"
"v_mul_f32 v157, s55, v157\n"
"v_mfma_f32_16x16x32_bf16 v[176:179], a[164:167], a[68:71], v[176:179]\n"
"s_waitcnt lgkmcnt(2)\n"
"v_mul_f32 v158, s55, v158\n"
"v_mul_f32 v159, s55, v159\n"
"v_mfma_f32_16x16x32_bf16 v[180:183], a[144:147], a[0:3], 0\n"
"ds_read_b128 a[160:163], v240 offset:5376\n"
"ds_read_b128 a[164:167], v240 offset:5888\n"
"v_mfma_f32_16x16x32_bf16 v[180:183], a[148:151], a[4:7], v[180:183]\n"
"v_exp_f32 v156, v156\n"
"v_mfma_f32_16x16x32_bf16 v[184:187], a[148:151], a[28:31], 0\n"
"v_exp_f32 v157, v157\n"
"v_mfma_f32_16x16x32_bf16 v[184:187], a[144:147], a[24:27], v[184:187]\n"
"s_waitcnt vmcnt(12)\n"
"v_exp_f32 v158, v158\n"
"v_mfma_f32_16x16x32_bf16 v[188:191], a[144:147], a[48:51], 0\n"
"s_barrier\n"
"v_exp_f32 v159, v159\n"
"v_mfma_f32_16x16x32_bf16 v[188:191], a[148:151], a[52:55], v[188:191]\n"
"s_waitcnt lgkmcnt(2)\n"
"v_mul_f32 v160, s55, v160\n"
"v_mul_f32 v161, s55, v161\n"
"v_mfma_f32_16x16x32_bf16 v[180:183], a[152:155], a[8:11], v[180:183]\n"
"s_mov_b32 s53, 73728\n"
"ds_read_b128 a[144:147], v240 offset:6144\n"
"ds_read_b128 a[148:151], v240 offset:6656\n"
"v_mfma_f32_16x16x32_bf16 v[180:183], a[156:159], a[12:15], v[180:183]\n"
"s_add_u32 m0, s52, 18432\n"
"v_mul_f32 v162, s55, v162\n"
"v_mul_f32 v163, s55, v163\n"
"v_mfma_f32_16x16x32_bf16 v[184:187], a[156:159], a[36:39], v[184:187]\n"
"buffer_load_dwordx4 v242, s[36:39], s53 offen offset:0 sc0 nt lds\n"
"v_mfma_f32_16x16x32_bf16 v[184:187], a[152:155], a[32:35], v[184:187]\n"
"s_add_u32 m0, s52, 19328\n"
"v_cvt_pk_bf16_f32 v194, v156, v157\n"
"v_cvt_pk_bf16_f32 v195, v158, v159\n"
"v_mfma_f32_16x16x32_bf16 v[188:191], a[152:155], a[56:59], v[188:191]\n"
"buffer_load_dwordx4 v242, s[36:39], s53 offen offset:128 sc0 nt lds\n"
"v_mfma_f32_16x16x32_bf16 v[188:191], a[156:159], a[60:63], v[188:191]\n"
"s_waitcnt lgkmcnt(2)\n"
"v_exp_f32 v160, v160\n"
"v_mfma_f32_16x16x32_bf16 v[180:183], a[160:163], a[16:19], v[180:183]\n"
"ds_read_b128 a[152:155], v240 offset:7168\n"
"ds_read_b128 a[156:159], v240 offset:7680\n"
"v_mfma_f32_16x16x32_bf16 v[180:183], a[164:167], a[20:23], v[180:183]\n"
"s_add_u32 m0, s52, 20224\n"
"v_exp_f32 v161, v161\n"
"v_mfma_f32_16x16x32_bf16 v[184:187], a[164:167], a[44:47], v[184:187]\n"
"s_mov_b32 s54, 86016\n"
"buffer_load_dwordx4 v242, s[36:39], s53 offen offset:256 sc0 nt lds\n"
"v_mfma_f32_16x16x32_bf16 v[184:187], a[160:163], a[40:43], v[184:187]\n"
"s_add_u32 m0, s52, 0\n"
"v_exp_f32 v162, v162\n"
"v_mfma_f32_16x16x32_bf16 v[188:191], a[160:163], a[64:67], v[188:191]\n"
"buffer_load_dwordx4 v242, s[36:39], s54 offen offset:0 sc0 nt lds\n"
"v_mfma_f32_16x16x32_bf16 v[188:191], a[164:167], a[68:71], v[188:191]\n"
"s_waitcnt lgkmcnt(2)\n"
"v_exp_f32 v163, v163\n"
"v_mfma_f32_16x16x32_bf16 v[144:147], a[144:147], a[0:3], 0\n"
"ds_read_b128 a[160:163], v240 offset:8192\n"
"ds_read_b128 a[164:167], v240 offset:8704\n"
"v_mfma_f32_16x16x32_bf16 v[144:147], a[148:151], a[4:7], v[144:147]\n"
"s_add_u32 m0, s52, 896\n"
"v_mul_f32 v164, s55, v164\n"
"v_mul_f32 v165, s55, v165\n"
"v_mfma_f32_16x16x32_bf16 v[148:151], a[148:151], a[28:31], 0\n"
"buffer_load_dwordx4 v242, s[36:39], s54 offen offset:128 sc0 nt lds\n"
"v_mfma_f32_16x16x32_bf16 v[148:151], a[144:147], a[24:27], v[148:151]\n"
"s_add_u32 m0, s52, 1792\n"
"v_mul_f32 v166, s55, v166\n"
"v_mul_f32 v167, s55, v167\n"
"v_mfma_f32_16x16x32_bf16 v[152:155], a[144:147], a[48:51], 0\n"
"buffer_load_dwordx4 v242, s[36:39], s54 offen offset:256 sc0 nt lds\n"
"v_mfma_f32_16x16x32_bf16 v[152:155], a[148:151], a[52:55], v[152:155]\n"
"s_waitcnt lgkmcnt(2)\n"
"v_cvt_pk_bf16_f32 v198, v160, v161\n"
"v_cvt_pk_bf16_f32 v199, v162, v163\n"
"v_mfma_f32_16x16x32_bf16 v[144:147], a[152:155], a[8:11], v[144:147]\n"
"s_mov_b32 s59, 24576\n"
"ds_read_b128 a[144:147], v240 offset:6400\n"
"ds_read_b128 a[148:151], v240 offset:6912\n"
"v_mfma_f32_16x16x32_bf16 v[144:147], a[156:159], a[12:15], v[144:147]\n"
"s_add_u32 m0, s52, 27648\n"
"v_exp_f32 v164, v164\n"
"v_mfma_f32_16x16x32_bf16 v[148:151], a[156:159], a[36:39], v[148:151]\n"
"buffer_load_dwordx4 v242, s[40:43], s59 offen offset:0 sc0 nt lds\n"
"v_mfma_f32_16x16x32_bf16 v[148:151], a[152:155], a[32:35], v[148:151]\n"
"s_add_u32 m0, s52, 28544\n"
"v_exp_f32 v165, v165\n"
"v_mfma_f32_16x16x32_bf16 v[152:155], a[152:155], a[56:59], v[152:155]\n"
"buffer_load_dwordx4 v242, s[40:43], s59 offen offset:128 sc0 nt lds\n"
"v_mfma_f32_16x16x32_bf16 v[152:155], a[156:159], a[60:63], v[152:155]\n"
"s_waitcnt lgkmcnt(2)\n"
"v_exp_f32 v166, v166\n"
"v_mfma_f32_16x16x32_bf16 v[144:147], a[160:163], a[16:19], v[144:147]\n"
"ds_read_b128 a[152:155], v240 offset:7424\n"
"ds_read_b128 a[156:159], v240 offset:7936\n"
"v_mfma_f32_16x16x32_bf16 v[144:147], a[164:167], a[20:23], v[144:147]\n"
"s_add_u32 m0, s52, 29440\n"
"v_exp_f32 v167, v167\n"
"v_mfma_f32_16x16x32_bf16 v[148:151], a[164:167], a[44:47], v[148:151]\n"
"s_mov_b32 s70, 36864\n"
"buffer_load_dwordx4 v242, s[40:43], s59 offen offset:256 sc0 nt lds\n"
"v_mfma_f32_16x16x32_bf16 v[148:151], a[160:163], a[40:43], v[148:151]\n"
"s_add_u32 m0, s52, 30720\n"
"v_cvt_pk_bf16_f32 v202, v164, v165\n"
"v_cvt_pk_bf16_f32 v203, v166, v167\n"
"v_mfma_f32_16x16x32_bf16 v[152:155], a[160:163], a[64:67], v[152:155]\n"
"buffer_load_dwordx4 v242, s[40:43], s70 offen offset:0 sc0 nt lds\n"
"v_mfma_f32_16x16x32_bf16 v[152:155], a[164:167], a[68:71], v[152:155]\n"
"s_waitcnt lgkmcnt(2)\n"
"v_mul_f32 v168, s55, v168\n"
"v_mul_f32 v169, s55, v169\n"
"v_mfma_f32_16x16x32_bf16 v[156:159], a[144:147], a[0:3], 0\n"
"ds_read_b128 a[160:163], v240 offset:8448\n"
"ds_read_b128 a[164:167], v240 offset:8960\n"
"v_mfma_f32_16x16x32_bf16 v[156:159], a[148:151], a[4:7], v[156:159]\n"
"s_add_u32 m0, s52, 31616\n"
"v_mul_f32 v170, s55, v170\n"
"v_mul_f32 v171, s55, v171\n"
"v_mfma_f32_16x16x32_bf16 v[160:163], a[148:151], a[28:31], 0\n"
"buffer_load_dwordx4 v242, s[40:43], s70 offen offset:128 sc0 nt lds\n"
"v_mfma_f32_16x16x32_bf16 v[160:163], a[144:147], a[24:27], v[160:163]\n"
"s_add_u32 m0, s52, 32512\n"
"v_exp_f32 v168, v168\n"
"v_mfma_f32_16x16x32_bf16 v[164:167], a[144:147], a[48:51], 0\n"
"buffer_load_dwordx4 v242, s[40:43], s70 offen offset:256 sc0 nt lds\n"
"v_mfma_f32_16x16x32_bf16 v[164:167], a[148:151], a[52:55], v[164:167]\n"
"s_waitcnt lgkmcnt(2)\n"
"v_exp_f32 v169, v169\n"
"v_mfma_f32_16x16x32_bf16 v[156:159], a[152:155], a[8:11], v[156:159]\n"
"ds_read_b128 a[144:147], v240 offset:9216\n"
"ds_read_b128 a[148:151], v240 offset:9728\n"
"v_mfma_f32_16x16x32_bf16 v[156:159], a[156:159], a[12:15], v[156:159]\n"
"v_exp_f32 v170, v170\n"
"v_mfma_f32_16x16x32_bf16 v[160:163], a[156:159], a[36:39], v[160:163]\n"
"buffer_load_dwordx4 a[72:75], v220, s[44:47], s63 offen offset:0 sc0 nt\n"
"v_mfma_f32_16x16x32_bf16 v[160:163], a[152:155], a[32:35], v[160:163]\n"
"v_exp_f32 v171, v171\n"
"v_mfma_f32_16x16x32_bf16 v[164:167], a[152:155], a[56:59], v[164:167]\n"
"v_mul_f32 v172, s55, v172\n"
"v_mul_f32 v173, s55, v173\n"
"v_mfma_f32_16x16x32_bf16 v[164:167], a[156:159], a[60:63], v[164:167]\n"
"s_waitcnt lgkmcnt(2)\n"
"v_mul_f32 v174, s55, v174\n"
"v_mul_f32 v175, s55, v175\n"
"v_mfma_f32_16x16x32_bf16 v[156:159], a[160:163], a[16:19], v[156:159]\n"
"ds_read_b128 a[152:155], v240 offset:10240\n"
"ds_read_b128 a[156:159], v240 offset:10752\n"
"v_mfma_f32_16x16x32_bf16 v[156:159], a[164:167], a[20:23], v[156:159]\n"
"v_cvt_pk_bf16_f32 v204, v168, v169\n"
"v_cvt_pk_bf16_f32 v205, v170, v171\n"
"v_mfma_f32_16x16x32_bf16 v[160:163], a[164:167], a[44:47], v[160:163]\n"
"buffer_load_dwordx4 a[76:79], v220, s[44:47], s63 offen offset:3072 sc0 nt\n"
"v_mfma_f32_16x16x32_bf16 v[160:163], a[160:163], a[40:43], v[160:163]\n"
"v_exp_f32 v172, v172\n"
"v_mfma_f32_16x16x32_bf16 v[164:167], a[160:163], a[64:67], v[164:167]\n"
"v_exp_f32 v173, v173\n"
"v_mfma_f32_16x16x32_bf16 v[164:167], a[164:167], a[68:71], v[164:167]\n"
"s_waitcnt lgkmcnt(2)\n"
"v_exp_f32 v174, v174\n"
"v_exp_f32 v175, v175\n"
"v_mul_f32 v176, s55, v176\n"
"v_mul_f32 v177, s55, v177\n"
"v_mul_f32 v178, s55, v178\n"
"v_mul_f32 v179, s55, v179\n"
"v_mfma_f32_16x16x32_bf16 v[168:171], a[144:147], a[0:3], 0\n"
"ds_read_b128 a[160:163], v240 offset:11264\n"
"ds_read_b128 a[164:167], v240 offset:11776\n"
"v_mfma_f32_16x16x32_bf16 v[168:171], a[148:151], a[4:7], v[168:171]\n"
"v_cvt_pk_bf16_f32 v208, v172, v173\n"
"v_cvt_pk_bf16_f32 v209, v174, v175\n"
"v_exp_f32 v176, v176\n"
"v_exp_f32 v177, v177\n"
"v_exp_f32 v178, v178\n"
"v_exp_f32 v179, v179\n"
"v_mfma_f32_16x16x32_bf16 v[172:175], a[148:151], a[28:31], 0\n"
"buffer_load_dwordx4 a[96:99], v220, s[44:47], s64 offen offset:0 sc0 nt\n"
"v_mfma_f32_16x16x32_bf16 v[172:175], a[144:147], a[24:27], v[172:175]\n"
"v_cvt_pk_bf16_f32 v212, v176, v177\n"
"v_cvt_pk_bf16_f32 v213, v178, v179\n"
"v_mfma_f32_16x16x32_bf16 v[176:179], a[144:147], a[48:51], 0\n"
"v_mul_f32 v180, s55, v180\n"
"v_mul_f32 v181, s55, v181\n"
"v_mfma_f32_16x16x32_bf16 v[176:179], a[148:151], a[52:55], v[176:179]\n"
"s_waitcnt lgkmcnt(2)\n"
"v_mul_f32 v182, s55, v182\n"
"v_mul_f32 v183, s55, v183\n"
"v_mfma_f32_16x16x32_bf16 v[168:171], a[152:155], a[8:11], v[168:171]\n"
"ds_read_b128 a[144:147], v240 offset:9472\n"
"ds_read_b128 a[148:151], v240 offset:9984\n"
"v_mfma_f32_16x16x32_bf16 v[168:171], a[156:159], a[12:15], v[168:171]\n"
"v_exp_f32 v180, v180\n"
"v_mfma_f32_16x16x32_bf16 v[172:175], a[156:159], a[36:39], v[172:175]\n"
"buffer_load_dwordx4 a[100:103], v220, s[44:47], s64 offen offset:3072 sc0 nt\n"
"v_mfma_f32_16x16x32_bf16 v[172:175], a[152:155], a[32:35], v[172:175]\n"
"v_exp_f32 v181, v181\n"
"v_mfma_f32_16x16x32_bf16 v[176:179], a[152:155], a[56:59], v[176:179]\n"
"v_exp_f32 v182, v182\n"
"v_mfma_f32_16x16x32_bf16 v[176:179], a[156:159], a[60:63], v[176:179]\n"
"s_waitcnt lgkmcnt(2)\n"
"v_exp_f32 v183, v183\n"
"v_mfma_f32_16x16x32_bf16 v[168:171], a[160:163], a[16:19], v[168:171]\n"
"ds_read_b128 a[152:155], v240 offset:10496\n"
"ds_read_b128 a[156:159], v240 offset:11008\n"
"v_mfma_f32_16x16x32_bf16 v[168:171], a[164:167], a[20:23], v[168:171]\n"
"v_mul_f32 v184, s55, v184\n"
"v_mul_f32 v185, s55, v185\n"
"v_mfma_f32_16x16x32_bf16 v[172:175], a[164:167], a[44:47], v[172:175]\n"
"v_mul_f32 v186, s55, v186\n"
"v_mul_f32 v187, s55, v187\n"
"v_mfma_f32_16x16x32_bf16 v[172:175], a[160:163], a[40:43], v[172:175]\n"
"v_cvt_pk_bf16_f32 v206, v180, v181\n"
"v_cvt_pk_bf16_f32 v207, v182, v183\n"
"v_mfma_f32_16x16x32_bf16 v[176:179], a[160:163], a[64:67], v[176:179]\n"
"v_exp_f32 v184, v184\n"
"v_mfma_f32_16x16x32_bf16 v[176:179], a[164:167], a[68:71], v[176:179]\n"
"s_waitcnt lgkmcnt(2)\n"
"v_exp_f32 v185, v185\n"
"v_exp_f32 v186, v186\n"
"v_exp_f32 v187, v187\n"
"v_mul_f32 v188, s55, v188\n"
"v_mul_f32 v189, s55, v189\n"
"v_mul_f32 v190, s55, v190\n"
"v_mul_f32 v191, s55, v191\n"
"v_mfma_f32_16x16x32_bf16 v[180:183], a[144:147], a[0:3], 0\n"
"ds_read_b128 a[160:163], v240 offset:11520\n"
"ds_read_b128 a[164:167], v240 offset:12032\n"
"v_mfma_f32_16x16x32_bf16 v[180:183], a[148:151], a[4:7], v[180:183]\n"
"v_cvt_pk_bf16_f32 v210, v184, v185\n"
"v_cvt_pk_bf16_f32 v211, v186, v187\n"
"v_exp_f32 v188, v188\n"
"v_exp_f32 v189, v189\n"
"v_exp_f32 v190, v190\n"
"v_exp_f32 v191, v191\n"
"v_mfma_f32_16x16x32_bf16 v[184:187], a[148:151], a[28:31], 0\n"
"v_cvt_pk_bf16_f32 v214, v188, v189\n"
"v_cvt_pk_bf16_f32 v215, v190, v191\n"
"v_mfma_f32_16x16x32_bf16 v[184:187], a[144:147], a[24:27], v[184:187]\n"
"s_waitcnt vmcnt(16)\n"
"v_mul_f32 v144, s55, v144\n"
"v_mul_f32 v145, s55, v145\n"
"v_mfma_f32_16x16x32_bf16 v[188:191], a[144:147], a[48:51], 0\n"
"s_barrier\n"
"v_mul_f32 v146, s55, v146\n"
"v_mul_f32 v147, s55, v147\n"
"v_mfma_f32_16x16x32_bf16 v[188:191], a[148:151], a[52:55], v[188:191]\n"
"s_waitcnt lgkmcnt(2)\n"
"v_exp_f32 v144, v144\n"
"v_mfma_f32_16x16x32_bf16 v[180:183], a[152:155], a[8:11], v[180:183]\n"
"s_mov_b32 s53, 98304\n"
"ds_read_b64_tr_b16 a[144:145], v241 offset:21504\n"
"ds_read_b64_tr_b16 a[146:147], v241 offset:21760\n"
"v_mfma_f32_16x16x32_bf16 v[180:183], a[156:159], a[12:15], v[180:183]\n"
"s_add_u32 m0, s52, 3072\n"
"ds_read_b64_tr_b16 a[148:149], v241 offset:24576\n"
"ds_read_b64_tr_b16 a[150:151], v241 offset:24832\n"
"v_mfma_f32_16x16x32_bf16 v[184:187], a[156:159], a[36:39], v[184:187]\n"
"buffer_load_dwordx4 v242, s[36:39], s53 offen offset:0 sc0 nt lds\n"
"v_mfma_f32_16x16x32_bf16 v[184:187], a[152:155], a[32:35], v[184:187]\n"
"v_exp_f32 v145, v145\n"
"v_mfma_f32_16x16x32_bf16 v[188:191], a[152:155], a[56:59], v[188:191]\n"
"v_exp_f32 v146, v146\n"
"v_mfma_f32_16x16x32_bf16 v[188:191], a[156:159], a[60:63], v[188:191]\n"
"s_waitcnt lgkmcnt(4)\n"
"v_exp_f32 v147, v147\n"
"v_mfma_f32_16x16x32_bf16 v[180:183], a[160:163], a[16:19], v[180:183]\n"
"ds_read_b64_tr_b16 a[152:153], v241 offset:21536\n"
"ds_read_b64_tr_b16 a[154:155], v241 offset:21792\n"
"v_mfma_f32_16x16x32_bf16 v[180:183], a[164:167], a[20:23], v[180:183]\n"
"s_add_u32 m0, s52, 3968\n"
"ds_read_b64_tr_b16 a[156:157], v241 offset:24608\n"
"ds_read_b64_tr_b16 a[158:159], v241 offset:24864\n"
"v_mfma_f32_16x16x32_bf16 v[184:187], a[164:167], a[44:47], v[184:187]\n"
"buffer_load_dwordx4 v242, s[36:39], s53 offen offset:128 sc0 nt lds\n"
"v_mfma_f32_16x16x32_bf16 v[184:187], a[160:163], a[40:43], v[184:187]\n"
"v_mul_f32 v148, s55, v148\n"
"v_mul_f32 v149, s55, v149\n"
"v_mfma_f32_16x16x32_bf16 v[188:191], a[160:163], a[64:67], v[188:191]\n"
"v_mul_f32 v150, s55, v150\n"
"v_mul_f32 v151, s55, v151\n"
"v_mfma_f32_16x16x32_bf16 v[188:191], a[164:167], a[68:71], v[188:191]\n"
"v_exp_f32 v148, v148\n"
"v_mfma_f32_16x16x32_bf16 a[240:243], a[252:255], v[192:195], 0\n"
"v_exp_f32 v149, v149\n"
"v_mfma_f32_16x16x32_bf16 a[240:243], a[252:255], v[204:207], a[240:243]\n"
"s_add_u32 m0, s52, 4864\n"
"v_exp_f32 v150, v150\n"
"v_mfma_f32_16x16x32_bf16 a[244:247], a[252:255], v[196:199], 0\n"
"buffer_load_dwordx4 v242, s[36:39], s53 offen offset:256 sc0 nt lds\n"
"v_mfma_f32_16x16x32_bf16 a[244:247], a[252:255], v[208:211], a[244:247]\n"
"v_exp_f32 v151, v151\n"
"v_mfma_f32_16x16x32_bf16 a[248:251], a[252:255], v[200:203], 0\n"
"v_mul_f32 v152, s55, v152\n"
"v_mul_f32 v153, s55, v153\n"
"v_mfma_f32_16x16x32_bf16 a[248:251], a[252:255], v[212:215], a[248:251]\n"
"s_waitcnt lgkmcnt(4)\n"
"v_mul_f32 v154, s55, v154\n"
"v_mul_f32 v155, s55, v155\n"
"v_mfma_f32_16x16x32_bf16 v[0:3], a[144:147], v[192:195], 0\n"
"s_mov_b32 s54, 110592\n"
"ds_read_b64_tr_b16 a[160:161], v241 offset:22016\n"
"ds_read_b64_tr_b16 a[162:163], v241 offset:22272\n"
"v_mfma_f32_16x16x32_bf16 v[0:3], a[148:151], v[204:207], v[0:3]\n"
"s_add_u32 m0, s52, 6144\n"
"ds_read_b64_tr_b16 a[164:165], v241 offset:25088\n"
"ds_read_b64_tr_b16 a[166:167], v241 offset:25344\n"
"v_mfma_f32_16x16x32_bf16 v[48:51], a[148:151], v[208:211], 0\n"
"buffer_load_dwordx4 v242, s[36:39], s54 offen offset:0 sc0 nt lds\n"
"v_mfma_f32_16x16x32_bf16 v[48:51], a[144:147], v[196:199], v[48:51]\n"
"v_exp_f32 v152, v152\n"
"v_mfma_f32_16x16x32_bf16 v[96:99], a[144:147], v[200:203], 0\n"
"v_exp_f32 v153, v153\n"
"v_mfma_f32_16x16x32_bf16 v[96:99], a[148:151], v[212:215], v[96:99]\n"
"s_waitcnt lgkmcnt(4)\n"
"v_exp_f32 v154, v154\n"
"v_mfma_f32_16x16x32_bf16 v[4:7], a[152:155], v[192:195], 0\n"
"ds_read_b64_tr_b16 a[144:145], v241 offset:22048\n"
"ds_read_b64_tr_b16 a[146:147], v241 offset:22304\n"
"v_mfma_f32_16x16x32_bf16 v[4:7], a[156:159], v[204:207], v[4:7]\n"
"s_add_u32 m0, s52, 7040\n"
"ds_read_b64_tr_b16 a[148:149], v241 offset:25120\n"
"ds_read_b64_tr_b16 a[150:151], v241 offset:25376\n"
"v_mfma_f32_16x16x32_bf16 v[52:55], a[156:159], v[208:211], 0\n"
"buffer_load_dwordx4 v242, s[36:39], s54 offen offset:128 sc0 nt lds\n"
"v_mfma_f32_16x16x32_bf16 v[52:55], a[152:155], v[196:199], v[52:55]\n"
"v_exp_f32 v155, v155\n"
"v_mfma_f32_16x16x32_bf16 v[100:103], a[152:155], v[200:203], 0\n"
"v_mul_f32 v156, s55, v156\n"
"v_mul_f32 v157, s55, v157\n"
"v_mfma_f32_16x16x32_bf16 v[100:103], a[156:159], v[212:215], v[100:103]\n"
"s_waitcnt lgkmcnt(4)\n"
"v_mul_f32 v158, s55, v158\n"
"v_mul_f32 v159, s55, v159\n"
"v_mfma_f32_16x16x32_bf16 v[8:11], a[160:163], v[192:195], 0\n"
"ds_read_b64_tr_b16 a[152:153], v241 offset:22528\n"
"ds_read_b64_tr_b16 a[154:155], v241 offset:22784\n"
"v_mfma_f32_16x16x32_bf16 v[8:11], a[164:167], v[204:207], v[8:11]\n"
"s_add_u32 m0, s52, 7936\n"
"ds_read_b64_tr_b16 a[156:157], v241 offset:25600\n"
"ds_read_b64_tr_b16 a[158:159], v241 offset:25856\n"
"v_mfma_f32_16x16x32_bf16 v[56:59], a[164:167], v[208:211], 0\n"
"buffer_load_dwordx4 v242, s[36:39], s54 offen offset:256 sc0 nt lds\n"
"v_mfma_f32_16x16x32_bf16 v[56:59], a[160:163], v[196:199], v[56:59]\n"
"v_exp_f32 v156, v156\n"
"v_mfma_f32_16x16x32_bf16 v[104:107], a[160:163], v[200:203], 0\n"
"v_exp_f32 v157, v157\n"
"v_mfma_f32_16x16x32_bf16 v[104:107], a[164:167], v[212:215], v[104:107]\n"
"s_waitcnt lgkmcnt(4)\n"
"v_exp_f32 v158, v158\n"
"v_mfma_f32_16x16x32_bf16 v[12:15], a[144:147], v[192:195], 0\n"
"s_mov_b32 s59, 49152\n"
"ds_read_b64_tr_b16 a[160:161], v241 offset:22560\n"
"ds_read_b64_tr_b16 a[162:163], v241 offset:22816\n"
"v_mfma_f32_16x16x32_bf16 v[12:15], a[148:151], v[204:207], v[12:15]\n"
"s_add_u32 m0, s52, 33792\n"
"ds_read_b64_tr_b16 a[164:165], v241 offset:25632\n"
"ds_read_b64_tr_b16 a[166:167], v241 offset:25888\n"
"v_mfma_f32_16x16x32_bf16 v[60:63], a[148:151], v[208:211], 0\n"
"buffer_load_dwordx4 v242, s[40:43], s59 offen offset:0 sc0 nt lds\n"
"v_mfma_f32_16x16x32_bf16 v[60:63], a[144:147], v[196:199], v[60:63]\n"
"v_exp_f32 v159, v159\n"
"v_mfma_f32_16x16x32_bf16 v[108:111], a[144:147], v[200:203], 0\n"
"v_mul_f32 v160, s55, v160\n"
"v_mul_f32 v161, s55, v161\n"
"v_mfma_f32_16x16x32_bf16 v[108:111], a[148:151], v[212:215], v[108:111]\n"
"s_waitcnt lgkmcnt(4)\n"
"v_mul_f32 v162, s55, v162\n"
"v_mul_f32 v163, s55, v163\n"
"v_mfma_f32_16x16x32_bf16 v[16:19], a[152:155], v[192:195], 0\n"
"ds_read_b64_tr_b16 a[144:145], v241 offset:23040\n"
"ds_read_b64_tr_b16 a[146:147], v241 offset:23296\n"
"v_mfma_f32_16x16x32_bf16 v[16:19], a[156:159], v[204:207], v[16:19]\n"
"s_add_u32 m0, s52, 34688\n"
"ds_read_b64_tr_b16 a[148:149], v241 offset:26112\n"
"ds_read_b64_tr_b16 a[150:151], v241 offset:26368\n"
"v_mfma_f32_16x16x32_bf16 v[64:67], a[156:159], v[208:211], 0\n"
"buffer_load_dwordx4 v242, s[40:43], s59 offen offset:128 sc0 nt lds\n"
"v_mfma_f32_16x16x32_bf16 v[64:67], a[152:155], v[196:199], v[64:67]\n"
"v_exp_f32 v160, v160\n"
"v_mfma_f32_16x16x32_bf16 v[112:115], a[152:155], v[200:203], 0\n"
"v_exp_f32 v161, v161\n"
"v_mfma_f32_16x16x32_bf16 v[112:115], a[156:159], v[212:215], v[112:115]\n"
"s_waitcnt lgkmcnt(4)\n"
"v_exp_f32 v162, v162\n"
"v_mfma_f32_16x16x32_bf16 v[20:23], a[160:163], v[192:195], 0\n"
"ds_read_b64_tr_b16 a[152:153], v241 offset:23072\n"
"ds_read_b64_tr_b16 a[154:155], v241 offset:23328\n"
"v_mfma_f32_16x16x32_bf16 v[20:23], a[164:167], v[204:207], v[20:23]\n"
"s_add_u32 m0, s52, 35584\n"
"ds_read_b64_tr_b16 a[156:157], v241 offset:26144\n"
"ds_read_b64_tr_b16 a[158:159], v241 offset:26400\n"
"v_mfma_f32_16x16x32_bf16 v[68:71], a[164:167], v[208:211], 0\n"
"buffer_load_dwordx4 v242, s[40:43], s59 offen offset:256 sc0 nt lds\n"
"v_mfma_f32_16x16x32_bf16 v[68:71], a[160:163], v[196:199], v[68:71]\n"
"v_exp_f32 v163, v163\n"
"v_mfma_f32_16x16x32_bf16 v[116:119], a[160:163], v[200:203], 0\n"
"v_mul_f32 v164, s55, v164\n"
"v_mul_f32 v165, s55, v165\n"
"v_mfma_f32_16x16x32_bf16 v[116:119], a[164:167], v[212:215], v[116:119]\n"
"s_waitcnt lgkmcnt(4)\n"
"v_mul_f32 v166, s55, v166\n"
"v_mul_f32 v167, s55, v167\n"
"v_mfma_f32_16x16x32_bf16 v[24:27], a[144:147], v[192:195], 0\n"
"s_mov_b32 s70, 61440\n"
"ds_read_b64_tr_b16 a[160:161], v241 offset:23552\n"
"ds_read_b64_tr_b16 a[162:163], v241 offset:23808\n"
"v_mfma_f32_16x16x32_bf16 v[24:27], a[148:151], v[204:207], v[24:27]\n"
"s_add_u32 m0, s52, 36864\n"
"ds_read_b64_tr_b16 a[164:165], v241 offset:26624\n"
"ds_read_b64_tr_b16 a[166:167], v241 offset:26880\n"
"v_mfma_f32_16x16x32_bf16 v[72:75], a[148:151], v[208:211], 0\n"
"buffer_load_dwordx4 v242, s[40:43], s70 offen offset:0 sc0 nt lds\n"
"v_mfma_f32_16x16x32_bf16 v[72:75], a[144:147], v[196:199], v[72:75]\n"
"v_exp_f32 v164, v164\n"
"v_mfma_f32_16x16x32_bf16 v[120:123], a[144:147], v[200:203], 0\n"
"v_exp_f32 v165, v165\n"
"v_mfma_f32_16x16x32_bf16 v[120:123], a[148:151], v[212:215], v[120:123]\n"
"s_waitcnt lgkmcnt(4)\n"
"v_exp_f32 v166, v166\n"
"v_mfma_f32_16x16x32_bf16 v[28:31], a[152:155], v[192:195], 0\n"
"ds_read_b64_tr_b16 a[144:145], v241 offset:23584\n"
"ds_read_b64_tr_b16 a[146:147], v241 offset:23840\n"
"v_mfma_f32_16x16x32_bf16 v[28:31], a[156:159], v[204:207], v[28:31]\n"
"s_add_u32 m0, s52, 37760\n"
"ds_read_b64_tr_b16 a[148:149], v241 offset:26656\n"
"ds_read_b64_tr_b16 a[150:151], v241 offset:26912\n"
"v_mfma_f32_16x16x32_bf16 v[76:79], a[156:159], v[208:211], 0\n"
"buffer_load_dwordx4 v242, s[40:43], s70 offen offset:128 sc0 nt lds\n"
"v_mfma_f32_16x16x32_bf16 v[76:79], a[152:155], v[196:199], v[76:79]\n"
"v_exp_f32 v167, v167\n"
"v_mfma_f32_16x16x32_bf16 v[124:127], a[152:155], v[200:203], 0\n"
"v_mul_f32 v168, s55, v168\n"
"v_mul_f32 v169, s55, v169\n"
"v_mfma_f32_16x16x32_bf16 v[124:127], a[156:159], v[212:215], v[124:127]\n"
"s_waitcnt lgkmcnt(4)\n"
"v_mul_f32 v170, s55, v170\n"
"v_mul_f32 v171, s55, v171\n"
"v_mfma_f32_16x16x32_bf16 v[32:35], a[160:163], v[192:195], 0\n"
"ds_read_b64_tr_b16 a[152:153], v241 offset:24064\n"
"ds_read_b64_tr_b16 a[154:155], v241 offset:24320\n"
"v_mfma_f32_16x16x32_bf16 v[32:35], a[164:167], v[204:207], v[32:35]\n"
"s_add_u32 m0, s52, 38656\n"
"ds_read_b64_tr_b16 a[156:157], v241 offset:27136\n"
"ds_read_b64_tr_b16 a[158:159], v241 offset:27392\n"
"v_mfma_f32_16x16x32_bf16 v[80:83], a[164:167], v[208:211], 0\n"
"buffer_load_dwordx4 v242, s[40:43], s70 offen offset:256 sc0 nt lds\n"
"v_mfma_f32_16x16x32_bf16 v[80:83], a[160:163], v[196:199], v[80:83]\n"
"v_exp_f32 v168, v168\n"
"v_mfma_f32_16x16x32_bf16 v[128:131], a[160:163], v[200:203], 0\n"
"v_exp_f32 v169, v169\n"
"v_mfma_f32_16x16x32_bf16 v[128:131], a[164:167], v[212:215], v[128:131]\n"
"s_waitcnt lgkmcnt(4)\n"
"v_exp_f32 v170, v170\n"
"v_mfma_f32_16x16x32_bf16 v[36:39], a[144:147], v[192:195], 0\n"
"ds_read_b64_tr_b16 a[160:161], v241 offset:24096\n"
"ds_read_b64_tr_b16 a[162:163], v241 offset:24352\n"
"v_mfma_f32_16x16x32_bf16 v[36:39], a[148:151], v[204:207], v[36:39]\n"
"ds_read_b64_tr_b16 a[164:165], v241 offset:27168\n"
"ds_read_b64_tr_b16 a[166:167], v241 offset:27424\n"
"v_mfma_f32_16x16x32_bf16 v[84:87], a[148:151], v[208:211], 0\n"
"buffer_load_dwordx4 a[120:123], v220, s[44:47], s65 offen offset:0 sc0 nt\n"
"v_mfma_f32_16x16x32_bf16 v[84:87], a[144:147], v[196:199], v[84:87]\n"
"v_exp_f32 v171, v171\n"
"v_mfma_f32_16x16x32_bf16 v[132:135], a[144:147], v[200:203], 0\n"
"v_mul_f32 v172, s55, v172\n"
"v_mul_f32 v173, s55, v173\n"
"v_mfma_f32_16x16x32_bf16 v[132:135], a[148:151], v[212:215], v[132:135]\n"
"s_waitcnt lgkmcnt(4)\n"
"v_mul_f32 v174, s55, v174\n"
"v_mul_f32 v175, s55, v175\n"
"v_mfma_f32_16x16x32_bf16 v[40:43], a[152:155], v[192:195], 0\n"
"ds_read_b128 a[144:147], v240 offset:12288\n"
"ds_read_b128 a[148:151], v240 offset:12800\n"
"v_mfma_f32_16x16x32_bf16 v[40:43], a[156:159], v[204:207], v[40:43]\n"
"v_exp_f32 v172, v172\n"
"v_mfma_f32_16x16x32_bf16 v[88:91], a[156:159], v[208:211], 0\n"
"buffer_load_dwordx4 a[124:127], v220, s[44:47], s65 offen offset:3072 sc0 nt\n"
"v_mfma_f32_16x16x32_bf16 v[88:91], a[152:155], v[196:199], v[88:91]\n"
"v_exp_f32 v173, v173\n"
"v_mfma_f32_16x16x32_bf16 v[136:139], a[152:155], v[200:203], 0\n"
"v_exp_f32 v174, v174\n"
"v_mfma_f32_16x16x32_bf16 v[136:139], a[156:159], v[212:215], v[136:139]\n"
"s_waitcnt lgkmcnt(2)\n"
"v_exp_f32 v175, v175\n"
"v_mfma_f32_16x16x32_bf16 v[44:47], a[160:163], v[192:195], 0\n"
"ds_read_b128 a[152:155], v240 offset:13312\n"
"ds_read_b128 a[156:159], v240 offset:13824\n"
"v_mfma_f32_16x16x32_bf16 v[44:47], a[164:167], v[204:207], v[44:47]\n"
"v_cvt_pk_bf16_f32 v192, v144, v145\n"
"v_cvt_pk_bf16_f32 v193, v146, v147\n"
"v_mfma_f32_16x16x32_bf16 v[92:95], a[164:167], v[208:211], 0\n"
"buffer_load_dwordx4 a[80:83], v220, s[44:47], s63 offen offset:128 sc0 nt\n"
"v_mfma_f32_16x16x32_bf16 v[92:95], a[160:163], v[196:199], v[92:95]\n"
"v_cvt_pk_bf16_f32 v196, v148, v149\n"
"v_cvt_pk_bf16_f32 v197, v150, v151\n"
"v_mfma_f32_16x16x32_bf16 v[140:143], a[160:163], v[200:203], 0\n"
"v_cvt_pk_bf16_f32 v200, v152, v153\n"
"v_cvt_pk_bf16_f32 v201, v154, v155\n"
"v_mfma_f32_16x16x32_bf16 v[140:143], a[164:167], v[212:215], v[140:143]\n"
"s_waitcnt lgkmcnt(2)\n"
"v_cvt_pk_bf16_f32 v194, v156, v157\n"
"v_cvt_pk_bf16_f32 v195, v158, v159\n"
"v_mfma_f32_16x16x32_bf16 v[144:147], a[144:147], a[0:3], 0\n"
"ds_read_b128 a[160:163], v240 offset:14336\n"
"ds_read_b128 a[164:167], v240 offset:14848\n"
"v_mfma_f32_16x16x32_bf16 v[144:147], a[148:151], a[4:7], v[144:147]\n"
"v_cvt_pk_bf16_f32 v198, v160, v161\n"
"v_cvt_pk_bf16_f32 v199, v162, v163\n"
"v_mfma_f32_16x16x32_bf16 v[148:151], a[148:151], a[28:31], 0\n"
"buffer_load_dwordx4 a[84:87], v220, s[44:47], s63 offen offset:3200 sc0 nt\n"
"v_mfma_f32_16x16x32_bf16 v[148:151], a[144:147], a[24:27], v[148:151]\n"
"v_cvt_pk_bf16_f32 v202, v164, v165\n"
"v_cvt_pk_bf16_f32 v203, v166, v167\n"
"v_mfma_f32_16x16x32_bf16 v[152:155], a[144:147], a[48:51], 0\n"
"v_cvt_pk_bf16_f32 v204, v168, v169\n"
"v_cvt_pk_bf16_f32 v205, v170, v171\n"
"v_mfma_f32_16x16x32_bf16 v[152:155], a[148:151], a[52:55], v[152:155]\n"
"s_waitcnt lgkmcnt(2)\n"
"v_mul_f32 v176, s55, v176\n"
"v_mul_f32 v177, s55, v177\n"
"v_mfma_f32_16x16x32_bf16 v[144:147], a[152:155], a[8:11], v[144:147]\n"
"ds_read_b128 a[144:147], v240 offset:12544\n"
"ds_read_b128 a[148:151], v240 offset:13056\n"
"v_mfma_f32_16x16x32_bf16 v[144:147], a[156:159], a[12:15], v[144:147]\n"
"v_mul_f32 v178, s55, v178\n"
"v_mul_f32 v179, s55, v179\n"
"v_mfma_f32_16x16x32_bf16 v[148:151], a[156:159], a[36:39], v[148:151]\n"
"v_cvt_pk_bf16_f32 v208, v172, v173\n"
"v_cvt_pk_bf16_f32 v209, v174, v175\n"
"v_mfma_f32_16x16x32_bf16 v[148:151], a[152:155], a[32:35], v[148:151]\n"
"v_exp_f32 v176, v176\n"
"v_mfma_f32_16x16x32_bf16 v[152:155], a[152:155], a[56:59], v[152:155]\n"
"v_exp_f32 v177, v177\n"
"v_mfma_f32_16x16x32_bf16 v[152:155], a[156:159], a[60:63], v[152:155]\n"
"s_waitcnt lgkmcnt(2)\n"
"v_exp_f32 v178, v178\n"
"v_mfma_f32_16x16x32_bf16 v[144:147], a[160:163], a[16:19], v[144:147]\n"
"ds_read_b128 a[152:155], v240 offset:13568\n"
"ds_read_b128 a[156:159], v240 offset:14080\n"
"v_mfma_f32_16x16x32_bf16 v[144:147], a[164:167], a[20:23], v[144:147]\n"
"v_exp_f32 v179, v179\n"
"v_mfma_f32_16x16x32_bf16 v[148:151], a[164:167], a[44:47], v[148:151]\n"
"v_cvt_pk_bf16_f32 v212, v176, v177\n"
"v_cvt_pk_bf16_f32 v213, v178, v179\n"
"v_mfma_f32_16x16x32_bf16 v[148:151], a[160:163], a[40:43], v[148:151]\n"
"v_mul_f32 v180, s55, v180\n"
"v_mul_f32 v181, s55, v181\n"
"v_mfma_f32_16x16x32_bf16 v[152:155], a[160:163], a[64:67], v[152:155]\n"
"v_mul_f32 v182, s55, v182\n"
"v_mul_f32 v183, s55, v183\n"
"v_mfma_f32_16x16x32_bf16 v[152:155], a[164:167], a[68:71], v[152:155]\n"
"s_waitcnt lgkmcnt(2)\n"
"v_exp_f32 v180, v180\n"
"v_mfma_f32_16x16x32_bf16 v[156:159], a[144:147], a[0:3], 0\n"
"ds_read_b128 a[160:163], v240 offset:14592\n"
"ds_read_b128 a[164:167], v240 offset:15104\n"
"v_mfma_f32_16x16x32_bf16 v[156:159], a[148:151], a[4:7], v[156:159]\n"
"v_exp_f32 v181, v181\n"
"v_mfma_f32_16x16x32_bf16 v[160:163], a[148:151], a[28:31], 0\n"
"v_exp_f32 v182, v182\n"
"v_mfma_f32_16x16x32_bf16 v[160:163], a[144:147], a[24:27], v[160:163]\n"
"v_exp_f32 v183, v183\n"
"v_mfma_f32_16x16x32_bf16 v[164:167], a[144:147], a[48:51], 0\n"
"v_mul_f32 v184, s55, v184\n"
"v_mul_f32 v185, s55, v185\n"
"v_mfma_f32_16x16x32_bf16 v[164:167], a[148:151], a[52:55], v[164:167]\n"
"s_waitcnt lgkmcnt(2)\n"
"v_mul_f32 v186, s55, v186\n"
"v_mul_f32 v187, s55, v187\n"
"v_mfma_f32_16x16x32_bf16 v[156:159], a[152:155], a[8:11], v[156:159]\n"
"ds_read_b128 a[144:147], v240 offset:15360\n"
"ds_read_b128 a[148:151], v240 offset:15872\n"
"v_mfma_f32_16x16x32_bf16 v[156:159], a[156:159], a[12:15], v[156:159]\n"
"v_cvt_pk_bf16_f32 v206, v180, v181\n"
"v_cvt_pk_bf16_f32 v207, v182, v183\n"
"v_mfma_f32_16x16x32_bf16 v[160:163], a[156:159], a[36:39], v[160:163]\n"
"v_exp_f32 v184, v184\n"
"v_mfma_f32_16x16x32_bf16 v[160:163], a[152:155], a[32:35], v[160:163]\n"
"v_exp_f32 v185, v185\n"
"v_mfma_f32_16x16x32_bf16 v[164:167], a[152:155], a[56:59], v[164:167]\n"
"v_exp_f32 v186, v186\n"
"v_mfma_f32_16x16x32_bf16 v[164:167], a[156:159], a[60:63], v[164:167]\n"
"s_waitcnt lgkmcnt(2)\n"
"v_exp_f32 v187, v187\n"
"v_mfma_f32_16x16x32_bf16 v[156:159], a[160:163], a[16:19], v[156:159]\n"
"ds_read_b128 a[152:155], v240 offset:16384\n"
"ds_read_b128 a[156:159], v240 offset:16896\n"
"v_mfma_f32_16x16x32_bf16 v[156:159], a[164:167], a[20:23], v[156:159]\n"
"v_mul_f32 v188, s55, v188\n"
"v_mul_f32 v189, s55, v189\n"
"v_mfma_f32_16x16x32_bf16 v[160:163], a[164:167], a[44:47], v[160:163]\n"
"v_mul_f32 v190, s55, v190\n"
"v_mul_f32 v191, s55, v191\n"
"v_mfma_f32_16x16x32_bf16 v[160:163], a[160:163], a[40:43], v[160:163]\n"
"v_cvt_pk_bf16_f32 v210, v184, v185\n"
"v_cvt_pk_bf16_f32 v211, v186, v187\n"
"v_mfma_f32_16x16x32_bf16 v[164:167], a[160:163], a[64:67], v[164:167]\n"
"v_exp_f32 v188, v188\n"
"v_mfma_f32_16x16x32_bf16 v[164:167], a[164:167], a[68:71], v[164:167]\n"
"s_waitcnt lgkmcnt(2)\n"
"v_exp_f32 v189, v189\n"
"v_mfma_f32_16x16x32_bf16 v[168:171], a[144:147], a[0:3], 0\n"
"ds_read_b128 a[160:163], v240 offset:17408\n"
"ds_read_b128 a[164:167], v240 offset:17920\n"
"v_mfma_f32_16x16x32_bf16 v[168:171], a[148:151], a[4:7], v[168:171]\n"
"v_exp_f32 v190, v190\n"
"v_mfma_f32_16x16x32_bf16 v[172:175], a[148:151], a[28:31], 0\n"
"v_exp_f32 v191, v191\n"
"v_mfma_f32_16x16x32_bf16 v[172:175], a[144:147], a[24:27], v[172:175]\n"
"v_cvt_pk_bf16_f32 v214, v188, v189\n"
"v_cvt_pk_bf16_f32 v215, v190, v191\n"
"v_mfma_f32_16x16x32_bf16 v[176:179], a[144:147], a[48:51], 0\n"
"v_mul_f32 v144, s55, v144\n"
"v_mul_f32 v145, s55, v145\n"
"v_mfma_f32_16x16x32_bf16 v[176:179], a[148:151], a[52:55], v[176:179]\n"
"s_waitcnt lgkmcnt(2)\n"
"v_mul_f32 v146, s55, v146\n"
"v_mul_f32 v147, s55, v147\n"
"v_mfma_f32_16x16x32_bf16 v[168:171], a[152:155], a[8:11], v[168:171]\n"
"ds_read_b128 a[144:147], v240 offset:15616\n"
"ds_read_b128 a[148:151], v240 offset:16128\n"
"v_mfma_f32_16x16x32_bf16 v[168:171], a[156:159], a[12:15], v[168:171]\n"
"v_exp_f32 v144, v144\n"
"v_mfma_f32_16x16x32_bf16 v[172:175], a[156:159], a[36:39], v[172:175]\n"
"v_exp_f32 v145, v145\n"
"v_mfma_f32_16x16x32_bf16 v[172:175], a[152:155], a[32:35], v[172:175]\n"
"v_exp_f32 v146, v146\n"
"v_mfma_f32_16x16x32_bf16 v[176:179], a[152:155], a[56:59], v[176:179]\n"
"v_exp_f32 v147, v147\n"
"v_mfma_f32_16x16x32_bf16 v[176:179], a[156:159], a[60:63], v[176:179]\n"
"s_waitcnt lgkmcnt(2)\n"
"v_mul_f32 v148, s55, v148\n"
"v_mul_f32 v149, s55, v149\n"
"v_mfma_f32_16x16x32_bf16 v[168:171], a[160:163], a[16:19], v[168:171]\n"
"ds_read_b128 a[152:155], v240 offset:16640\n"
"ds_read_b128 a[156:159], v240 offset:17152\n"
"v_mfma_f32_16x16x32_bf16 v[168:171], a[164:167], a[20:23], v[168:171]\n"
"v_mul_f32 v150, s55, v150\n"
"v_mul_f32 v151, s55, v151\n"
"v_mfma_f32_16x16x32_bf16 v[172:175], a[164:167], a[44:47], v[172:175]\n"
"v_exp_f32 v148, v148\n"
"v_mfma_f32_16x16x32_bf16 v[172:175], a[160:163], a[40:43], v[172:175]\n"
"v_exp_f32 v149, v149\n"
"v_mfma_f32_16x16x32_bf16 v[176:179], a[160:163], a[64:67], v[176:179]\n"
"v_exp_f32 v150, v150\n"
"v_mfma_f32_16x16x32_bf16 v[176:179], a[164:167], a[68:71], v[176:179]\n"
"s_waitcnt lgkmcnt(2)\n"
"v_exp_f32 v151, v151\n"
"v_mfma_f32_16x16x32_bf16 v[180:183], a[144:147], a[0:3], 0\n"
"ds_read_b128 a[160:163], v240 offset:17664\n"
"ds_read_b128 a[164:167], v240 offset:18176\n"
"v_mfma_f32_16x16x32_bf16 v[180:183], a[148:151], a[4:7], v[180:183]\n"
"v_mul_f32 v152, s55, v152\n"
"v_mul_f32 v153, s55, v153\n"
"v_mfma_f32_16x16x32_bf16 v[184:187], a[148:151], a[28:31], 0\n"
"v_mul_f32 v154, s55, v154\n"
"v_mul_f32 v155, s55, v155\n"
"v_mfma_f32_16x16x32_bf16 v[184:187], a[144:147], a[24:27], v[184:187]\n"
"s_waitcnt vmcnt(20)\n"
"v_exp_f32 v152, v152\n"
"v_mfma_f32_16x16x32_bf16 v[188:191], a[144:147], a[48:51], 0\n"
"s_barrier\n"
"v_exp_f32 v153, v153\n"
"v_mfma_f32_16x16x32_bf16 v[188:191], a[148:151], a[52:55], v[188:191]\n"
"s_waitcnt lgkmcnt(2)\n"
"v_exp_f32 v154, v154\n"
"v_mfma_f32_16x16x32_bf16 v[180:183], a[152:155], a[8:11], v[180:183]\n"
"s_mov_b32 s53, 122880\n"
"ds_read_b64_tr_b16 a[144:145], v241 offset:27648\n"
"ds_read_b64_tr_b16 a[146:147], v241 offset:27904\n"
"v_mfma_f32_16x16x32_bf16 v[180:183], a[156:159], a[12:15], v[180:183]\n"
"s_add_u32 m0, s52, 9216\n"
"ds_read_b64_tr_b16 a[148:149], v241 offset:30720\n"
"ds_read_b64_tr_b16 a[150:151], v241 offset:30976\n"
"v_mfma_f32_16x16x32_bf16 v[184:187], a[156:159], a[36:39], v[184:187]\n"
"buffer_load_dwordx4 v242, s[36:39], s53 offen offset:0 sc0 nt lds\n"
"v_mfma_f32_16x16x32_bf16 v[184:187], a[152:155], a[32:35], v[184:187]\n"
"v_exp_f32 v155, v155\n"
"v_mfma_f32_16x16x32_bf16 v[188:191], a[152:155], a[56:59], v[188:191]\n"
"v_mul_f32 v156, s55, v156\n"
"v_mul_f32 v157, s55, v157\n"
"v_mfma_f32_16x16x32_bf16 v[188:191], a[156:159], a[60:63], v[188:191]\n"
"s_waitcnt lgkmcnt(4)\n"
"v_mul_f32 v158, s55, v158\n"
"v_mul_f32 v159, s55, v159\n"
"v_mfma_f32_16x16x32_bf16 v[180:183], a[160:163], a[16:19], v[180:183]\n"
"ds_read_b64_tr_b16 a[152:153], v241 offset:27680\n"
"ds_read_b64_tr_b16 a[154:155], v241 offset:27936\n"
"v_mfma_f32_16x16x32_bf16 v[180:183], a[164:167], a[20:23], v[180:183]\n"
"s_add_u32 m0, s52, 10112\n"
"ds_read_b64_tr_b16 a[156:157], v241 offset:30752\n"
"ds_read_b64_tr_b16 a[158:159], v241 offset:31008\n"
"v_mfma_f32_16x16x32_bf16 v[184:187], a[164:167], a[44:47], v[184:187]\n"
"buffer_load_dwordx4 v242, s[36:39], s53 offen offset:128 sc0 nt lds\n"
"v_mfma_f32_16x16x32_bf16 v[184:187], a[160:163], a[40:43], v[184:187]\n"
"v_exp_f32 v156, v156\n"
"v_mfma_f32_16x16x32_bf16 v[188:191], a[160:163], a[64:67], v[188:191]\n"
"v_exp_f32 v157, v157\n"
"v_mfma_f32_16x16x32_bf16 v[188:191], a[164:167], a[68:71], v[188:191]\n"
"v_exp_f32 v158, v158\n"
"v_mfma_f32_16x16x32_bf16 a[240:243], a[252:255], v[192:195], a[240:243]\n"
"v_exp_f32 v159, v159\n"
"v_mfma_f32_16x16x32_bf16 a[240:243], a[252:255], v[204:207], a[240:243]\n"
"s_add_u32 m0, s52, 11008\n"
"v_mul_f32 v160, s55, v160\n"
"v_mul_f32 v161, s55, v161\n"
"v_mfma_f32_16x16x32_bf16 a[244:247], a[252:255], v[196:199], a[244:247]\n"
"buffer_load_dwordx4 v242, s[36:39], s53 offen offset:256 sc0 nt lds\n"
"v_mfma_f32_16x16x32_bf16 a[244:247], a[252:255], v[208:211], a[244:247]\n"
"v_mul_f32 v162, s55, v162\n"
"v_mul_f32 v163, s55, v163\n"
"v_mfma_f32_16x16x32_bf16 a[248:251], a[252:255], v[200:203], a[248:251]\n"
"v_exp_f32 v160, v160\n"
"v_mfma_f32_16x16x32_bf16 a[248:251], a[252:255], v[212:215], a[248:251]\n"
"s_waitcnt lgkmcnt(4)\n"
"v_exp_f32 v161, v161\n"
"v_mfma_f32_16x16x32_bf16 v[0:3], a[144:147], v[192:195], v[0:3]\n"
"s_mov_b32 s54, 135168\n"
"ds_read_b64_tr_b16 a[160:161], v241 offset:28160\n"
"ds_read_b64_tr_b16 a[162:163], v241 offset:28416\n"
"v_mfma_f32_16x16x32_bf16 v[0:3], a[148:151], v[204:207], v[0:3]\n"
"s_add_u32 m0, s52, 12288\n"
"ds_read_b64_tr_b16 a[164:165], v241 offset:31232\n"
"ds_read_b64_tr_b16 a[166:167], v241 offset:31488\n"
"v_mfma_f32_16x16x32_bf16 v[48:51], a[148:151], v[208:211], v[48:51]\n"
"buffer_load_dwordx4 v242, s[36:39], s54 offen offset:0 sc0 nt lds\n"
"v_mfma_f32_16x16x32_bf16 v[48:51], a[144:147], v[196:199], v[48:51]\n"
"v_exp_f32 v162, v162\n"
"v_mfma_f32_16x16x32_bf16 v[96:99], a[144:147], v[200:203], v[96:99]\n"
"v_exp_f32 v163, v163\n"
"v_mfma_f32_16x16x32_bf16 v[96:99], a[148:151], v[212:215], v[96:99]\n"
"s_waitcnt lgkmcnt(4)\n"
"v_mul_f32 v164, s55, v164\n"
"v_mul_f32 v165, s55, v165\n"
"v_mfma_f32_16x16x32_bf16 v[4:7], a[152:155], v[192:195], v[4:7]\n"
"ds_read_b64_tr_b16 a[144:145], v241 offset:28192\n"
"ds_read_b64_tr_b16 a[146:147], v241 offset:28448\n"
"v_mfma_f32_16x16x32_bf16 v[4:7], a[156:159], v[204:207], v[4:7]\n"
"s_add_u32 m0, s52, 13184\n"
"ds_read_b64_tr_b16 a[148:149], v241 offset:31264\n"
"ds_read_b64_tr_b16 a[150:151], v241 offset:31520\n"
"v_mfma_f32_16x16x32_bf16 v[52:55], a[156:159], v[208:211], v[52:55]\n"
"buffer_load_dwordx4 v242, s[36:39], s54 offen offset:128 sc0 nt lds\n"
"v_mfma_f32_16x16x32_bf16 v[52:55], a[152:155], v[196:199], v[52:55]\n"
"v_mul_f32 v166, s55, v166\n"
"v_mul_f32 v167, s55, v167\n"
"v_mfma_f32_16x16x32_bf16 v[100:103], a[152:155], v[200:203], v[100:103]\n"
"v_exp_f32 v164, v164\n"
"v_mfma_f32_16x16x32_bf16 v[100:103], a[156:159], v[212:215], v[100:103]\n"
"s_waitcnt lgkmcnt(4)\n"
"v_exp_f32 v165, v165\n"
"v_mfma_f32_16x16x32_bf16 v[8:11], a[160:163], v[192:195], v[8:11]\n"
"ds_read_b64_tr_b16 a[152:153], v241 offset:28672\n"
"ds_read_b64_tr_b16 a[154:155], v241 offset:28928\n"
"v_mfma_f32_16x16x32_bf16 v[8:11], a[164:167], v[204:207], v[8:11]\n"
"s_add_u32 m0, s52, 14080\n"
"ds_read_b64_tr_b16 a[156:157], v241 offset:31744\n"
"ds_read_b64_tr_b16 a[158:159], v241 offset:32000\n"
"v_mfma_f32_16x16x32_bf16 v[56:59], a[164:167], v[208:211], v[56:59]\n"
"buffer_load_dwordx4 v242, s[36:39], s54 offen offset:256 sc0 nt lds\n"
"v_mfma_f32_16x16x32_bf16 v[56:59], a[160:163], v[196:199], v[56:59]\n"
"v_exp_f32 v166, v166\n"
"v_mfma_f32_16x16x32_bf16 v[104:107], a[160:163], v[200:203], v[104:107]\n"
"v_exp_f32 v167, v167\n"
"v_mfma_f32_16x16x32_bf16 v[104:107], a[164:167], v[212:215], v[104:107]\n"
"s_waitcnt lgkmcnt(4)\n"
"v_mul_f32 v168, s55, v168\n"
"v_mul_f32 v169, s55, v169\n"
"v_mfma_f32_16x16x32_bf16 v[12:15], a[144:147], v[192:195], v[12:15]\n"
"s_mov_b32 s59, 73728\n"
"ds_read_b64_tr_b16 a[160:161], v241 offset:28704\n"
"ds_read_b64_tr_b16 a[162:163], v241 offset:28960\n"
"v_mfma_f32_16x16x32_bf16 v[12:15], a[148:151], v[204:207], v[12:15]\n"
"s_add_u32 m0, s52, 21504\n"
"ds_read_b64_tr_b16 a[164:165], v241 offset:31776\n"
"ds_read_b64_tr_b16 a[166:167], v241 offset:32032\n"
"v_mfma_f32_16x16x32_bf16 v[60:63], a[148:151], v[208:211], v[60:63]\n"
"buffer_load_dwordx4 v242, s[40:43], s59 offen offset:0 sc0 nt lds\n"
"v_mfma_f32_16x16x32_bf16 v[60:63], a[144:147], v[196:199], v[60:63]\n"
"v_mul_f32 v170, s55, v170\n"
"v_mul_f32 v171, s55, v171\n"
"v_mfma_f32_16x16x32_bf16 v[108:111], a[144:147], v[200:203], v[108:111]\n"
"v_exp_f32 v168, v168\n"
"v_mfma_f32_16x16x32_bf16 v[108:111], a[148:151], v[212:215], v[108:111]\n"
"s_waitcnt lgkmcnt(4)\n"
"v_exp_f32 v169, v169\n"
"v_mfma_f32_16x16x32_bf16 v[16:19], a[152:155], v[192:195], v[16:19]\n"
"ds_read_b64_tr_b16 a[144:145], v241 offset:29184\n"
"ds_read_b64_tr_b16 a[146:147], v241 offset:29440\n"
"v_mfma_f32_16x16x32_bf16 v[16:19], a[156:159], v[204:207], v[16:19]\n"
"s_add_u32 m0, s52, 22400\n"
"ds_read_b64_tr_b16 a[148:149], v241 offset:32256\n"
"ds_read_b64_tr_b16 a[150:151], v241 offset:32512\n"
"v_mfma_f32_16x16x32_bf16 v[64:67], a[156:159], v[208:211], v[64:67]\n"
"buffer_load_dwordx4 v242, s[40:43], s59 offen offset:128 sc0 nt lds\n"
"v_mfma_f32_16x16x32_bf16 v[64:67], a[152:155], v[196:199], v[64:67]\n"
"v_exp_f32 v170, v170\n"
"v_mfma_f32_16x16x32_bf16 v[112:115], a[152:155], v[200:203], v[112:115]\n"
"v_exp_f32 v171, v171\n"
"v_mfma_f32_16x16x32_bf16 v[112:115], a[156:159], v[212:215], v[112:115]\n"
"s_waitcnt lgkmcnt(4)\n"
"v_mul_f32 v172, s55, v172\n"
"v_mul_f32 v173, s55, v173\n"
"v_mfma_f32_16x16x32_bf16 v[20:23], a[160:163], v[192:195], v[20:23]\n"
"ds_read_b64_tr_b16 a[152:153], v241 offset:29216\n"
"ds_read_b64_tr_b16 a[154:155], v241 offset:29472\n"
"v_mfma_f32_16x16x32_bf16 v[20:23], a[164:167], v[204:207], v[20:23]\n"
"s_add_u32 m0, s52, 23296\n"
"ds_read_b64_tr_b16 a[156:157], v241 offset:32288\n"
"ds_read_b64_tr_b16 a[158:159], v241 offset:32544\n"
"v_mfma_f32_16x16x32_bf16 v[68:71], a[164:167], v[208:211], v[68:71]\n"
"buffer_load_dwordx4 v242, s[40:43], s59 offen offset:256 sc0 nt lds\n"
"v_mfma_f32_16x16x32_bf16 v[68:71], a[160:163], v[196:199], v[68:71]\n"
"v_mul_f32 v174, s55, v174\n"
"v_mul_f32 v175, s55, v175\n"
"v_mfma_f32_16x16x32_bf16 v[116:119], a[160:163], v[200:203], v[116:119]\n"
"v_exp_f32 v172, v172\n"
"v_mfma_f32_16x16x32_bf16 v[116:119], a[164:167], v[212:215], v[116:119]\n"
"s_waitcnt lgkmcnt(4)\n"
"v_exp_f32 v173, v173\n"
"v_mfma_f32_16x16x32_bf16 v[24:27], a[144:147], v[192:195], v[24:27]\n"
"s_mov_b32 s70, 86016\n"
"ds_read_b64_tr_b16 a[160:161], v241 offset:29696\n"
"ds_read_b64_tr_b16 a[162:163], v241 offset:29952\n"
"v_mfma_f32_16x16x32_bf16 v[24:27], a[148:151], v[204:207], v[24:27]\n"
"s_add_u32 m0, s52, 24576\n"
"ds_read_b64_tr_b16 a[164:165], v241 offset:32768\n"
"ds_read_b64_tr_b16 a[166:167], v241 offset:33024\n"
"v_mfma_f32_16x16x32_bf16 v[72:75], a[148:151], v[208:211], v[72:75]\n"
"buffer_load_dwordx4 v242, s[40:43], s70 offen offset:0 sc0 nt lds\n"
"v_mfma_f32_16x16x32_bf16 v[72:75], a[144:147], v[196:199], v[72:75]\n"
"v_exp_f32 v174, v174\n"
"v_mfma_f32_16x16x32_bf16 v[120:123], a[144:147], v[200:203], v[120:123]\n"
"v_exp_f32 v175, v175\n"
"v_mfma_f32_16x16x32_bf16 v[120:123], a[148:151], v[212:215], v[120:123]\n"
"s_waitcnt lgkmcnt(4)\n"
"v_mul_f32 v176, s55, v176\n"
"v_mul_f32 v177, s55, v177\n"
"v_mfma_f32_16x16x32_bf16 v[28:31], a[152:155], v[192:195], v[28:31]\n"
"ds_read_b64_tr_b16 a[144:145], v241 offset:29728\n"
"ds_read_b64_tr_b16 a[146:147], v241 offset:29984\n"
"v_mfma_f32_16x16x32_bf16 v[28:31], a[156:159], v[204:207], v[28:31]\n"
"s_add_u32 m0, s52, 25472\n"
"ds_read_b64_tr_b16 a[148:149], v241 offset:32800\n"
"ds_read_b64_tr_b16 a[150:151], v241 offset:33056\n"
"v_mfma_f32_16x16x32_bf16 v[76:79], a[156:159], v[208:211], v[76:79]\n"
"buffer_load_dwordx4 v242, s[40:43], s70 offen offset:128 sc0 nt lds\n"
"v_mfma_f32_16x16x32_bf16 v[76:79], a[152:155], v[196:199], v[76:79]\n"
"v_mul_f32 v178, s55, v178\n"
"v_mul_f32 v179, s55, v179\n"
"v_mfma_f32_16x16x32_bf16 v[124:127], a[152:155], v[200:203], v[124:127]\n"
"v_exp_f32 v176, v176\n"
"v_mfma_f32_16x16x32_bf16 v[124:127], a[156:159], v[212:215], v[124:127]\n"
"s_waitcnt lgkmcnt(4)\n"
"v_exp_f32 v177, v177\n"
"v_mfma_f32_16x16x32_bf16 v[32:35], a[160:163], v[192:195], v[32:35]\n"
"ds_read_b64_tr_b16 a[152:153], v241 offset:30208\n"
"ds_read_b64_tr_b16 a[154:155], v241 offset:30464\n"
"v_mfma_f32_16x16x32_bf16 v[32:35], a[164:167], v[204:207], v[32:35]\n"
"s_add_u32 m0, s52, 26368\n"
"ds_read_b64_tr_b16 a[156:157], v241 offset:33280\n"
"ds_read_b64_tr_b16 a[158:159], v241 offset:33536\n"
"v_mfma_f32_16x16x32_bf16 v[80:83], a[164:167], v[208:211], v[80:83]\n"
"buffer_load_dwordx4 v242, s[40:43], s70 offen offset:256 sc0 nt lds\n"
"v_mfma_f32_16x16x32_bf16 v[80:83], a[160:163], v[196:199], v[80:83]\n"
"v_exp_f32 v178, v178\n"
"v_mfma_f32_16x16x32_bf16 v[128:131], a[160:163], v[200:203], v[128:131]\n"
"v_exp_f32 v179, v179\n"
"v_mfma_f32_16x16x32_bf16 v[128:131], a[164:167], v[212:215], v[128:131]\n"
"s_waitcnt lgkmcnt(4)\n"
"v_mul_f32 v180, s55, v180\n"
"v_mul_f32 v181, s55, v181\n"
"v_mfma_f32_16x16x32_bf16 v[36:39], a[144:147], v[192:195], v[36:39]\n"
"ds_read_b64_tr_b16 a[160:161], v241 offset:30240\n"
"ds_read_b64_tr_b16 a[162:163], v241 offset:30496\n"
"v_mfma_f32_16x16x32_bf16 v[36:39], a[148:151], v[204:207], v[36:39]\n"
"ds_read_b64_tr_b16 a[164:165], v241 offset:33312\n"
"ds_read_b64_tr_b16 a[166:167], v241 offset:33568\n"
"v_mfma_f32_16x16x32_bf16 v[84:87], a[148:151], v[208:211], v[84:87]\n"
"buffer_load_dwordx4 a[104:107], v220, s[44:47], s64 offen offset:128 sc0 nt\n"
"v_mfma_f32_16x16x32_bf16 v[84:87], a[144:147], v[196:199], v[84:87]\n"
"v_mul_f32 v182, s55, v182\n"
"v_mul_f32 v183, s55, v183\n"
"v_mfma_f32_16x16x32_bf16 v[132:135], a[144:147], v[200:203], v[132:135]\n"
"v_exp_f32 v180, v180\n"
"v_mfma_f32_16x16x32_bf16 v[132:135], a[148:151], v[212:215], v[132:135]\n"
"s_waitcnt lgkmcnt(4)\n"
"v_exp_f32 v181, v181\n"
"v_mfma_f32_16x16x32_bf16 v[40:43], a[152:155], v[192:195], v[40:43]\n"
"ds_read_b128 a[144:147], v240 offset:18432\n"
"ds_read_b128 a[148:151], v240 offset:18944\n"
"v_mfma_f32_16x16x32_bf16 v[40:43], a[156:159], v[204:207], v[40:43]\n"
"v_exp_f32 v182, v182\n"
"v_mfma_f32_16x16x32_bf16 v[88:91], a[156:159], v[208:211], v[88:91]\n"
"buffer_load_dwordx4 a[108:111], v220, s[44:47], s64 offen offset:3200 sc0 nt\n"
"v_mfma_f32_16x16x32_bf16 v[88:91], a[152:155], v[196:199], v[88:91]\n"
"v_exp_f32 v183, v183\n"
"v_mfma_f32_16x16x32_bf16 v[136:139], a[152:155], v[200:203], v[136:139]\n"
"v_mul_f32 v184, s55, v184\n"
"v_mul_f32 v185, s55, v185\n"
"v_mfma_f32_16x16x32_bf16 v[136:139], a[156:159], v[212:215], v[136:139]\n"
"s_waitcnt lgkmcnt(2)\n"
"v_mul_f32 v186, s55, v186\n"
"v_mul_f32 v187, s55, v187\n"
"v_mfma_f32_16x16x32_bf16 v[44:47], a[160:163], v[192:195], v[44:47]\n"
"ds_read_b128 a[152:155], v240 offset:19456\n"
"ds_read_b128 a[156:159], v240 offset:19968\n"
"v_mfma_f32_16x16x32_bf16 v[44:47], a[164:167], v[204:207], v[44:47]\n"
"v_cvt_pk_bf16_f32 v192, v144, v145\n"
"v_cvt_pk_bf16_f32 v193, v146, v147\n"
"v_mfma_f32_16x16x32_bf16 v[92:95], a[164:167], v[208:211], v[92:95]\n"
"buffer_load_dwordx4 a[128:131], v220, s[44:47], s65 offen offset:128 sc0 nt\n"
"v_mfma_f32_16x16x32_bf16 v[92:95], a[160:163], v[196:199], v[92:95]\n"
"v_cvt_pk_bf16_f32 v196, v148, v149\n"
"v_cvt_pk_bf16_f32 v197, v150, v151\n"
"v_mfma_f32_16x16x32_bf16 v[140:143], a[160:163], v[200:203], v[140:143]\n"
"v_cvt_pk_bf16_f32 v200, v152, v153\n"
"v_cvt_pk_bf16_f32 v201, v154, v155\n"
"v_mfma_f32_16x16x32_bf16 v[140:143], a[164:167], v[212:215], v[140:143]\n"
"s_waitcnt lgkmcnt(2)\n"
"v_cvt_pk_bf16_f32 v194, v156, v157\n"
"v_cvt_pk_bf16_f32 v195, v158, v159\n"
"v_mfma_f32_16x16x32_bf16 v[144:147], a[144:147], a[0:3], 0\n"
"ds_read_b128 a[160:163], v240 offset:20480\n"
"ds_read_b128 a[164:167], v240 offset:20992\n"
"v_mfma_f32_16x16x32_bf16 v[144:147], a[148:151], a[4:7], v[144:147]\n"
"v_cvt_pk_bf16_f32 v198, v160, v161\n"
"v_cvt_pk_bf16_f32 v199, v162, v163\n"
"v_mfma_f32_16x16x32_bf16 v[148:151], a[148:151], a[28:31], 0\n"
"buffer_load_dwordx4 a[132:135], v220, s[44:47], s65 offen offset:3200 sc0 nt\n"
"v_mfma_f32_16x16x32_bf16 v[148:151], a[144:147], a[24:27], v[148:151]\n"
"v_cvt_pk_bf16_f32 v202, v164, v165\n"
"v_cvt_pk_bf16_f32 v203, v166, v167\n"
"v_mfma_f32_16x16x32_bf16 v[152:155], a[144:147], a[48:51], 0\n"
"v_cvt_pk_bf16_f32 v204, v168, v169\n"
"v_cvt_pk_bf16_f32 v205, v170, v171\n"
"v_mfma_f32_16x16x32_bf16 v[152:155], a[148:151], a[52:55], v[152:155]\n"
"s_waitcnt lgkmcnt(2)\n"
"v_cvt_pk_bf16_f32 v208, v172, v173\n"
"v_cvt_pk_bf16_f32 v209, v174, v175\n"
"v_mfma_f32_16x16x32_bf16 v[144:147], a[152:155], a[8:11], v[144:147]\n"
"ds_read_b128 a[144:147], v240 offset:18688\n"
"ds_read_b128 a[148:151], v240 offset:19200\n"
"v_mfma_f32_16x16x32_bf16 v[144:147], a[156:159], a[12:15], v[144:147]\n"
"v_cvt_pk_bf16_f32 v212, v176, v177\n"
"v_cvt_pk_bf16_f32 v213, v178, v179\n"
"v_mfma_f32_16x16x32_bf16 v[148:151], a[156:159], a[36:39], v[148:151]\n"
"v_cvt_pk_bf16_f32 v206, v180, v181\n"
"v_cvt_pk_bf16_f32 v207, v182, v183\n"
"v_mfma_f32_16x16x32_bf16 v[148:151], a[152:155], a[32:35], v[148:151]\n"
"v_exp_f32 v184, v184\n"
"v_mfma_f32_16x16x32_bf16 v[152:155], a[152:155], a[56:59], v[152:155]\n"
"v_exp_f32 v185, v185\n"
"v_mfma_f32_16x16x32_bf16 v[152:155], a[156:159], a[60:63], v[152:155]\n"
"s_waitcnt lgkmcnt(2)\n"
"v_exp_f32 v186, v186\n"
"v_mfma_f32_16x16x32_bf16 v[144:147], a[160:163], a[16:19], v[144:147]\n"
"ds_read_b128 a[152:155], v240 offset:19712\n"
"ds_read_b128 a[156:159], v240 offset:20224\n"
"v_mfma_f32_16x16x32_bf16 v[144:147], a[164:167], a[20:23], v[144:147]\n"
"v_exp_f32 v187, v187\n"
"v_mfma_f32_16x16x32_bf16 v[148:151], a[164:167], a[44:47], v[148:151]\n"
"v_mul_f32 v188, s55, v188\n"
"v_mul_f32 v189, s55, v189\n"
"v_mfma_f32_16x16x32_bf16 v[148:151], a[160:163], a[40:43], v[148:151]\n"
"v_mul_f32 v190, s55, v190\n"
"v_mul_f32 v191, s55, v191\n"
"v_mfma_f32_16x16x32_bf16 v[152:155], a[160:163], a[64:67], v[152:155]\n"
"v_cvt_pk_bf16_f32 v210, v184, v185\n"
"v_cvt_pk_bf16_f32 v211, v186, v187\n"
"v_mfma_f32_16x16x32_bf16 v[152:155], a[164:167], a[68:71], v[152:155]\n"
"s_waitcnt lgkmcnt(2)\n"
"v_exp_f32 v188, v188\n"
"v_mfma_f32_16x16x32_bf16 v[156:159], a[144:147], a[0:3], 0\n"
"ds_read_b128 a[160:163], v240 offset:20736\n"
"ds_read_b128 a[164:167], v240 offset:21248\n"
"v_mfma_f32_16x16x32_bf16 v[156:159], a[148:151], a[4:7], v[156:159]\n"
"v_exp_f32 v189, v189\n"
"v_mfma_f32_16x16x32_bf16 v[160:163], a[148:151], a[28:31], 0\n"
"v_exp_f32 v190, v190\n"
"v_mfma_f32_16x16x32_bf16 v[160:163], a[144:147], a[24:27], v[160:163]\n"
"v_exp_f32 v191, v191\n"
"v_mfma_f32_16x16x32_bf16 v[164:167], a[144:147], a[48:51], 0\n"
"v_cvt_pk_bf16_f32 v214, v188, v189\n"
"v_cvt_pk_bf16_f32 v215, v190, v191\n"
"v_mfma_f32_16x16x32_bf16 v[164:167], a[148:151], a[52:55], v[164:167]\n"
"s_waitcnt lgkmcnt(2)\n"
"v_mul_f32 v144, s55, v144\n"
"v_mul_f32 v145, s55, v145\n"
"v_mfma_f32_16x16x32_bf16 v[156:159], a[152:155], a[8:11], v[156:159]\n"
"ds_read_b128 a[144:147], v240 offset:0\n"
"ds_read_b128 a[148:151], v240 offset:512\n"
"v_mfma_f32_16x16x32_bf16 v[156:159], a[156:159], a[12:15], v[156:159]\n"
"v_mul_f32 v146, s55, v146\n"
"v_mul_f32 v147, s55, v147\n"
"v_mfma_f32_16x16x32_bf16 v[160:163], a[156:159], a[36:39], v[160:163]\n"
"v_exp_f32 v144, v144\n"
"v_mfma_f32_16x16x32_bf16 v[160:163], a[152:155], a[32:35], v[160:163]\n"
"v_exp_f32 v145, v145\n"
"v_mfma_f32_16x16x32_bf16 v[164:167], a[152:155], a[56:59], v[164:167]\n"
"v_exp_f32 v146, v146\n"
"v_mfma_f32_16x16x32_bf16 v[164:167], a[156:159], a[60:63], v[164:167]\n"
"s_waitcnt lgkmcnt(2)\n"
"v_exp_f32 v147, v147\n"
"v_mfma_f32_16x16x32_bf16 v[156:159], a[160:163], a[16:19], v[156:159]\n"
"ds_read_b128 a[152:155], v240 offset:1024\n"
"ds_read_b128 a[156:159], v240 offset:1536\n"
"v_mfma_f32_16x16x32_bf16 v[156:159], a[164:167], a[20:23], v[156:159]\n"
"v_mul_f32 v148, s55, v148\n"
"v_mul_f32 v149, s55, v149\n"
"v_mfma_f32_16x16x32_bf16 v[160:163], a[164:167], a[44:47], v[160:163]\n"
"v_mul_f32 v150, s55, v150\n"
"v_mul_f32 v151, s55, v151\n"
"v_mfma_f32_16x16x32_bf16 v[160:163], a[160:163], a[40:43], v[160:163]\n"
"v_exp_f32 v148, v148\n"
"v_mfma_f32_16x16x32_bf16 v[164:167], a[160:163], a[64:67], v[164:167]\n"
"v_exp_f32 v149, v149\n"
"v_mfma_f32_16x16x32_bf16 v[164:167], a[164:167], a[68:71], v[164:167]\n"
"s_waitcnt lgkmcnt(2)\n"
"v_exp_f32 v150, v150\n"
"v_mfma_f32_16x16x32_bf16 v[168:171], a[144:147], a[0:3], 0\n"
"ds_read_b128 a[160:163], v240 offset:2048\n"
"ds_read_b128 a[164:167], v240 offset:2560\n"
"v_mfma_f32_16x16x32_bf16 v[168:171], a[148:151], a[4:7], v[168:171]\n"
"v_exp_f32 v151, v151\n"
"v_mfma_f32_16x16x32_bf16 v[172:175], a[148:151], a[28:31], 0\n"
"v_mul_f32 v152, s55, v152\n"
"v_mul_f32 v153, s55, v153\n"
"v_mfma_f32_16x16x32_bf16 v[172:175], a[144:147], a[24:27], v[172:175]\n"
"v_mul_f32 v154, s55, v154\n"
"v_mul_f32 v155, s55, v155\n"
"v_mfma_f32_16x16x32_bf16 v[176:179], a[144:147], a[48:51], 0\n"
"v_exp_f32 v152, v152\n"
"v_mfma_f32_16x16x32_bf16 v[176:179], a[148:151], a[52:55], v[176:179]\n"
"s_waitcnt lgkmcnt(2)\n"
"v_exp_f32 v153, v153\n"
"v_mfma_f32_16x16x32_bf16 v[168:171], a[152:155], a[8:11], v[168:171]\n"
"ds_read_b128 a[144:147], v240 offset:256\n"
"ds_read_b128 a[148:151], v240 offset:768\n"
"v_mfma_f32_16x16x32_bf16 v[168:171], a[156:159], a[12:15], v[168:171]\n"
"v_exp_f32 v154, v154\n"
"v_mfma_f32_16x16x32_bf16 v[172:175], a[156:159], a[36:39], v[172:175]\n"
"v_exp_f32 v155, v155\n"
"v_mfma_f32_16x16x32_bf16 v[172:175], a[152:155], a[32:35], v[172:175]\n"
"v_mul_f32 v156, s55, v156\n"
"v_mul_f32 v157, s55, v157\n"
"v_mfma_f32_16x16x32_bf16 v[176:179], a[152:155], a[56:59], v[176:179]\n"
"v_mul_f32 v158, s55, v158\n"
"v_mul_f32 v159, s55, v159\n"
"v_mfma_f32_16x16x32_bf16 v[176:179], a[156:159], a[60:63], v[176:179]\n"
"s_waitcnt lgkmcnt(2)\n"
"v_exp_f32 v156, v156\n"
"v_mfma_f32_16x16x32_bf16 v[168:171], a[160:163], a[16:19], v[168:171]\n"
"ds_read_b128 a[152:155], v240 offset:1280\n"
"ds_read_b128 a[156:159], v240 offset:1792\n"
"v_mfma_f32_16x16x32_bf16 v[168:171], a[164:167], a[20:23], v[168:171]\n"
"v_exp_f32 v157, v157\n"
"v_mfma_f32_16x16x32_bf16 v[172:175], a[164:167], a[44:47], v[172:175]\n"
"v_exp_f32 v158, v158\n"
"v_mfma_f32_16x16x32_bf16 v[172:175], a[160:163], a[40:43], v[172:175]\n"
"v_exp_f32 v159, v159\n"
"v_mfma_f32_16x16x32_bf16 v[176:179], a[160:163], a[64:67], v[176:179]\n"
"v_mul_f32 v160, s55, v160\n"
"v_mul_f32 v161, s55, v161\n"
"v_mfma_f32_16x16x32_bf16 v[176:179], a[164:167], a[68:71], v[176:179]\n"
"s_waitcnt lgkmcnt(2)\n"
"v_mul_f32 v162, s55, v162\n"
"v_mul_f32 v163, s55, v163\n"
"v_mfma_f32_16x16x32_bf16 v[180:183], a[144:147], a[0:3], 0\n"
"ds_read_b128 a[160:163], v240 offset:2304\n"
"ds_read_b128 a[164:167], v240 offset:2816\n"
"v_mfma_f32_16x16x32_bf16 v[180:183], a[148:151], a[4:7], v[180:183]\n"
"v_exp_f32 v160, v160\n"
"v_mfma_f32_16x16x32_bf16 v[184:187], a[148:151], a[28:31], 0\n"
"v_exp_f32 v161, v161\n"
"v_mfma_f32_16x16x32_bf16 v[184:187], a[144:147], a[24:27], v[184:187]\n"
"s_waitcnt vmcnt(20)\n"
"v_exp_f32 v162, v162\n"
"v_mfma_f32_16x16x32_bf16 v[188:191], a[144:147], a[48:51], 0\n"
"s_barrier\n"
"v_exp_f32 v163, v163\n"
"v_mfma_f32_16x16x32_bf16 v[188:191], a[148:151], a[52:55], v[188:191]\n"
"s_waitcnt lgkmcnt(2)\n"
"v_mul_f32 v164, s55, v164\n"
"v_mul_f32 v165, s55, v165\n"
"v_mfma_f32_16x16x32_bf16 v[180:183], a[152:155], a[8:11], v[180:183]\n"
"s_mov_b32 s53, 147456\n"
"ds_read_b64_tr_b16 a[144:145], v241 offset:33792\n"
"ds_read_b64_tr_b16 a[146:147], v241 offset:34048\n"
"v_mfma_f32_16x16x32_bf16 v[180:183], a[156:159], a[12:15], v[180:183]\n"
"s_add_u32 m0, s52, 15360\n"
"ds_read_b64_tr_b16 a[148:149], v241 offset:36864\n"
"ds_read_b64_tr_b16 a[150:151], v241 offset:37120\n"
"v_mfma_f32_16x16x32_bf16 v[184:187], a[156:159], a[36:39], v[184:187]\n"
"buffer_load_dwordx4 v242, s[36:39], s53 offen offset:0 sc0 nt lds\n"
"v_mfma_f32_16x16x32_bf16 v[184:187], a[152:155], a[32:35], v[184:187]\n"
"v_mul_f32 v166, s55, v166\n"
"v_mul_f32 v167, s55, v167\n"
"v_mfma_f32_16x16x32_bf16 v[188:191], a[152:155], a[56:59], v[188:191]\n"
"v_exp_f32 v164, v164\n"
"v_mfma_f32_16x16x32_bf16 v[188:191], a[156:159], a[60:63], v[188:191]\n"
"s_waitcnt lgkmcnt(4)\n"
"v_exp_f32 v165, v165\n"
"v_mfma_f32_16x16x32_bf16 v[180:183], a[160:163], a[16:19], v[180:183]\n"
"ds_read_b64_tr_b16 a[152:153], v241 offset:33824\n"
"ds_read_b64_tr_b16 a[154:155], v241 offset:34080\n"
"v_mfma_f32_16x16x32_bf16 v[180:183], a[164:167], a[20:23], v[180:183]\n"
"s_add_u32 m0, s52, 16256\n"
"ds_read_b64_tr_b16 a[156:157], v241 offset:36896\n"
"ds_read_b64_tr_b16 a[158:159], v241 offset:37152\n"
"v_mfma_f32_16x16x32_bf16 v[184:187], a[164:167], a[44:47], v[184:187]\n"
"buffer_load_dwordx4 v242, s[36:39], s53 offen offset:128 sc0 nt lds\n"
"v_mfma_f32_16x16x32_bf16 v[184:187], a[160:163], a[40:43], v[184:187]\n"
"v_exp_f32 v166, v166\n"
"v_mfma_f32_16x16x32_bf16 v[188:191], a[160:163], a[64:67], v[188:191]\n"
"v_exp_f32 v167, v167\n"
"v_mfma_f32_16x16x32_bf16 v[188:191], a[164:167], a[68:71], v[188:191]\n"
"v_mul_f32 v168, s55, v168\n"
"v_mul_f32 v169, s55, v169\n"
"v_mfma_f32_16x16x32_bf16 a[240:243], a[252:255], v[192:195], a[240:243]\n"
"v_mul_f32 v170, s55, v170\n"
"v_mul_f32 v171, s55, v171\n"
"v_mfma_f32_16x16x32_bf16 a[240:243], a[252:255], v[204:207], a[240:243]\n"
"s_add_u32 m0, s52, 17152\n"
"v_exp_f32 v168, v168\n"
"v_mfma_f32_16x16x32_bf16 a[244:247], a[252:255], v[196:199], a[244:247]\n"
"buffer_load_dwordx4 v242, s[36:39], s53 offen offset:256 sc0 nt lds\n"
"v_mfma_f32_16x16x32_bf16 a[244:247], a[252:255], v[208:211], a[244:247]\n"
"v_exp_f32 v169, v169\n"
"v_mfma_f32_16x16x32_bf16 a[248:251], a[252:255], v[200:203], a[248:251]\n"
"v_exp_f32 v170, v170\n"
"v_mfma_f32_16x16x32_bf16 a[248:251], a[252:255], v[212:215], a[248:251]\n"
"s_waitcnt lgkmcnt(4)\n"
"v_exp_f32 v171, v171\n"
"v_mfma_f32_16x16x32_bf16 v[0:3], a[144:147], v[192:195], v[0:3]\n"
"s_mov_b32 s54, 159744\n"
"ds_read_b64_tr_b16 a[160:161], v241 offset:34304\n"
"ds_read_b64_tr_b16 a[162:163], v241 offset:34560\n"
"v_mfma_f32_16x16x32_bf16 v[0:3], a[148:151], v[204:207], v[0:3]\n"
"s_add_u32 m0, s52, 18432\n"
"ds_read_b64_tr_b16 a[164:165], v241 offset:37376\n"
"ds_read_b64_tr_b16 a[166:167], v241 offset:37632\n"
"v_mfma_f32_16x16x32_bf16 v[48:51], a[148:151], v[208:211], v[48:51]\n"
"buffer_load_dwordx4 v242, s[36:39], s54 offen offset:0 sc0 nt lds\n"
"v_mfma_f32_16x16x32_bf16 v[48:51], a[144:147], v[196:199], v[48:51]\n"
"v_mul_f32 v172, s55, v172\n"
"v_mul_f32 v173, s55, v173\n"
"v_mfma_f32_16x16x32_bf16 v[96:99], a[144:147], v[200:203], v[96:99]\n"
"v_mul_f32 v174, s55, v174\n"
"v_mul_f32 v175, s55, v175\n"
"v_mfma_f32_16x16x32_bf16 v[96:99], a[148:151], v[212:215], v[96:99]\n"
"s_waitcnt lgkmcnt(4)\n"
"v_exp_f32 v172, v172\n"
"v_mfma_f32_16x16x32_bf16 v[4:7], a[152:155], v[192:195], v[4:7]\n"
"ds_read_b64_tr_b16 a[144:145], v241 offset:34336\n"
"ds_read_b64_tr_b16 a[146:147], v241 offset:34592\n"
"v_mfma_f32_16x16x32_bf16 v[4:7], a[156:159], v[204:207], v[4:7]\n"
"s_add_u32 m0, s52, 19328\n"
"ds_read_b64_tr_b16 a[148:149], v241 offset:37408\n"
"ds_read_b64_tr_b16 a[150:151], v241 offset:37664\n"
"v_mfma_f32_16x16x32_bf16 v[52:55], a[156:159], v[208:211], v[52:55]\n"
"buffer_load_dwordx4 v242, s[36:39], s54 offen offset:128 sc0 nt lds\n"
"v_mfma_f32_16x16x32_bf16 v[52:55], a[152:155], v[196:199], v[52:55]\n"
"v_exp_f32 v173, v173\n"
"v_mfma_f32_16x16x32_bf16 v[100:103], a[152:155], v[200:203], v[100:103]\n"
"v_exp_f32 v174, v174\n"
"v_mfma_f32_16x16x32_bf16 v[100:103], a[156:159], v[212:215], v[100:103]\n"
"s_waitcnt lgkmcnt(4)\n"
"v_exp_f32 v175, v175\n"
"v_mfma_f32_16x16x32_bf16 v[8:11], a[160:163], v[192:195], v[8:11]\n"
"ds_read_b64_tr_b16 a[152:153], v241 offset:34816\n"
"ds_read_b64_tr_b16 a[154:155], v241 offset:35072\n"
"v_mfma_f32_16x16x32_bf16 v[8:11], a[164:167], v[204:207], v[8:11]\n"
"s_add_u32 m0, s52, 20224\n"
"ds_read_b64_tr_b16 a[156:157], v241 offset:37888\n"
"ds_read_b64_tr_b16 a[158:159], v241 offset:38144\n"
"v_mfma_f32_16x16x32_bf16 v[56:59], a[164:167], v[208:211], v[56:59]\n"
"buffer_load_dwordx4 v242, s[36:39], s54 offen offset:256 sc0 nt lds\n"
"v_mfma_f32_16x16x32_bf16 v[56:59], a[160:163], v[196:199], v[56:59]\n"
"v_mul_f32 v176, s55, v176\n"
"v_mul_f32 v177, s55, v177\n"
"v_mfma_f32_16x16x32_bf16 v[104:107], a[160:163], v[200:203], v[104:107]\n"
"v_mul_f32 v178, s55, v178\n"
"v_mul_f32 v179, s55, v179\n"
"v_mfma_f32_16x16x32_bf16 v[104:107], a[164:167], v[212:215], v[104:107]\n"
"s_waitcnt lgkmcnt(4)\n"
"v_exp_f32 v176, v176\n"
"v_mfma_f32_16x16x32_bf16 v[12:15], a[144:147], v[192:195], v[12:15]\n"
"s_mov_b32 s59, 98304\n"
"ds_read_b64_tr_b16 a[160:161], v241 offset:34848\n"
"ds_read_b64_tr_b16 a[162:163], v241 offset:35104\n"
"v_mfma_f32_16x16x32_bf16 v[12:15], a[148:151], v[204:207], v[12:15]\n"
"s_add_u32 m0, s52, 27648\n"
"ds_read_b64_tr_b16 a[164:165], v241 offset:37920\n"
"ds_read_b64_tr_b16 a[166:167], v241 offset:38176\n"
"v_mfma_f32_16x16x32_bf16 v[60:63], a[148:151], v[208:211], v[60:63]\n"
"buffer_load_dwordx4 v242, s[40:43], s59 offen offset:0 sc0 nt lds\n"
"v_mfma_f32_16x16x32_bf16 v[60:63], a[144:147], v[196:199], v[60:63]\n"
"v_exp_f32 v177, v177\n"
"v_mfma_f32_16x16x32_bf16 v[108:111], a[144:147], v[200:203], v[108:111]\n"
"v_exp_f32 v178, v178\n"
"v_mfma_f32_16x16x32_bf16 v[108:111], a[148:151], v[212:215], v[108:111]\n"
"s_waitcnt lgkmcnt(4)\n"
"v_exp_f32 v179, v179\n"
"v_mfma_f32_16x16x32_bf16 v[16:19], a[152:155], v[192:195], v[16:19]\n"
"ds_read_b64_tr_b16 a[144:145], v241 offset:35328\n"
"ds_read_b64_tr_b16 a[146:147], v241 offset:35584\n"
"v_mfma_f32_16x16x32_bf16 v[16:19], a[156:159], v[204:207], v[16:19]\n"
"s_add_u32 m0, s52, 28544\n"
"ds_read_b64_tr_b16 a[148:149], v241 offset:38400\n"
"ds_read_b64_tr_b16 a[150:151], v241 offset:38656\n"
"v_mfma_f32_16x16x32_bf16 v[64:67], a[156:159], v[208:211], v[64:67]\n"
"buffer_load_dwordx4 v242, s[40:43], s59 offen offset:128 sc0 nt lds\n"
"v_mfma_f32_16x16x32_bf16 v[64:67], a[152:155], v[196:199], v[64:67]\n"
"v_mul_f32 v180, s55, v180\n"
"v_mul_f32 v181, s55, v181\n"
"v_mfma_f32_16x16x32_bf16 v[112:115], a[152:155], v[200:203], v[112:115]\n"
"v_mul_f32 v182, s55, v182\n"
"v_mul_f32 v183, s55, v183\n"
"v_mfma_f32_16x16x32_bf16 v[112:115], a[156:159], v[212:215], v[112:115]\n"
"s_waitcnt lgkmcnt(4)\n"
"v_exp_f32 v180, v180\n"
"v_mfma_f32_16x16x32_bf16 v[20:23], a[160:163], v[192:195], v[20:23]\n"
"ds_read_b64_tr_b16 a[152:153], v241 offset:35360\n"
"ds_read_b64_tr_b16 a[154:155], v241 offset:35616\n"
"v_mfma_f32_16x16x32_bf16 v[20:23], a[164:167], v[204:207], v[20:23]\n"
"s_add_u32 m0, s52, 29440\n"
"ds_read_b64_tr_b16 a[156:157], v241 offset:38432\n"
"ds_read_b64_tr_b16 a[158:159], v241 offset:38688\n"
"v_mfma_f32_16x16x32_bf16 v[68:71], a[164:167], v[208:211], v[68:71]\n"
"buffer_load_dwordx4 v242, s[40:43], s59 offen offset:256 sc0 nt lds\n"
"v_mfma_f32_16x16x32_bf16 v[68:71], a[160:163], v[196:199], v[68:71]\n"
"v_exp_f32 v181, v181\n"
"v_mfma_f32_16x16x32_bf16 v[116:119], a[160:163], v[200:203], v[116:119]\n"
"v_exp_f32 v182, v182\n"
"v_mfma_f32_16x16x32_bf16 v[116:119], a[164:167], v[212:215], v[116:119]\n"
"s_waitcnt lgkmcnt(4)\n"
"v_exp_f32 v183, v183\n"
"v_mfma_f32_16x16x32_bf16 v[24:27], a[144:147], v[192:195], v[24:27]\n"
"s_mov_b32 s70, 110592\n"
"ds_read_b64_tr_b16 a[160:161], v241 offset:35840\n"
"ds_read_b64_tr_b16 a[162:163], v241 offset:36096\n"
"v_mfma_f32_16x16x32_bf16 v[24:27], a[148:151], v[204:207], v[24:27]\n"
"s_add_u32 m0, s52, 30720\n"
"ds_read_b64_tr_b16 a[164:165], v241 offset:38912\n"
"ds_read_b64_tr_b16 a[166:167], v241 offset:39168\n"
"v_mfma_f32_16x16x32_bf16 v[72:75], a[148:151], v[208:211], v[72:75]\n"
"buffer_load_dwordx4 v242, s[40:43], s70 offen offset:0 sc0 nt lds\n"
"v_mfma_f32_16x16x32_bf16 v[72:75], a[144:147], v[196:199], v[72:75]\n"
"v_mul_f32 v184, s55, v184\n"
"v_mul_f32 v185, s55, v185\n"
"v_mfma_f32_16x16x32_bf16 v[120:123], a[144:147], v[200:203], v[120:123]\n"
"v_mul_f32 v186, s55, v186\n"
"v_mul_f32 v187, s55, v187\n"
"v_mfma_f32_16x16x32_bf16 v[120:123], a[148:151], v[212:215], v[120:123]\n"
"s_waitcnt lgkmcnt(4)\n"
"v_exp_f32 v184, v184\n"
"v_mfma_f32_16x16x32_bf16 v[28:31], a[152:155], v[192:195], v[28:31]\n"
"ds_read_b64_tr_b16 a[144:145], v241 offset:35872\n"
"ds_read_b64_tr_b16 a[146:147], v241 offset:36128\n"
"v_mfma_f32_16x16x32_bf16 v[28:31], a[156:159], v[204:207], v[28:31]\n"
"s_add_u32 m0, s52, 31616\n"
"ds_read_b64_tr_b16 a[148:149], v241 offset:38944\n"
"ds_read_b64_tr_b16 a[150:151], v241 offset:39200\n"
"v_mfma_f32_16x16x32_bf16 v[76:79], a[156:159], v[208:211], v[76:79]\n"
"buffer_load_dwordx4 v242, s[40:43], s70 offen offset:128 sc0 nt lds\n"
"v_mfma_f32_16x16x32_bf16 v[76:79], a[152:155], v[196:199], v[76:79]\n"
"v_exp_f32 v185, v185\n"
"v_mfma_f32_16x16x32_bf16 v[124:127], a[152:155], v[200:203], v[124:127]\n"
"v_exp_f32 v186, v186\n"
"v_mfma_f32_16x16x32_bf16 v[124:127], a[156:159], v[212:215], v[124:127]\n"
"s_waitcnt lgkmcnt(4)\n"
"v_exp_f32 v187, v187\n"
"v_mfma_f32_16x16x32_bf16 v[32:35], a[160:163], v[192:195], v[32:35]\n"
"ds_read_b64_tr_b16 a[152:153], v241 offset:36352\n"
"ds_read_b64_tr_b16 a[154:155], v241 offset:36608\n"
"v_mfma_f32_16x16x32_bf16 v[32:35], a[164:167], v[204:207], v[32:35]\n"
"s_add_u32 m0, s52, 32512\n"
"ds_read_b64_tr_b16 a[156:157], v241 offset:39424\n"
"ds_read_b64_tr_b16 a[158:159], v241 offset:39680\n"
"v_mfma_f32_16x16x32_bf16 v[80:83], a[164:167], v[208:211], v[80:83]\n"
"buffer_load_dwordx4 v242, s[40:43], s70 offen offset:256 sc0 nt lds\n"
"v_mfma_f32_16x16x32_bf16 v[80:83], a[160:163], v[196:199], v[80:83]\n"
"v_mul_f32 v188, s55, v188\n"
"v_mul_f32 v189, s55, v189\n"
"v_mfma_f32_16x16x32_bf16 v[128:131], a[160:163], v[200:203], v[128:131]\n"
"v_mul_f32 v190, s55, v190\n"
"v_mul_f32 v191, s55, v191\n"
"v_mfma_f32_16x16x32_bf16 v[128:131], a[164:167], v[212:215], v[128:131]\n"
"s_waitcnt lgkmcnt(4)\n"
"v_exp_f32 v188, v188\n"
"v_mfma_f32_16x16x32_bf16 v[36:39], a[144:147], v[192:195], v[36:39]\n"
"ds_read_b64_tr_b16 a[160:161], v241 offset:36384\n"
"ds_read_b64_tr_b16 a[162:163], v241 offset:36640\n"
"v_mfma_f32_16x16x32_bf16 v[36:39], a[148:151], v[204:207], v[36:39]\n"
"ds_read_b64_tr_b16 a[164:165], v241 offset:39456\n"
"ds_read_b64_tr_b16 a[166:167], v241 offset:39712\n"
"v_mfma_f32_16x16x32_bf16 v[84:87], a[148:151], v[208:211], v[84:87]\n"
"buffer_load_dwordx4 a[88:91], v220, s[44:47], s63 offen offset:256 sc0 nt\n"
"v_mfma_f32_16x16x32_bf16 v[84:87], a[144:147], v[196:199], v[84:87]\n"
"v_exp_f32 v189, v189\n"
"v_mfma_f32_16x16x32_bf16 v[132:135], a[144:147], v[200:203], v[132:135]\n"
"v_exp_f32 v190, v190\n"
"v_mfma_f32_16x16x32_bf16 v[132:135], a[148:151], v[212:215], v[132:135]\n"
"s_waitcnt lgkmcnt(4)\n"
"v_exp_f32 v191, v191\n"
"v_mfma_f32_16x16x32_bf16 v[40:43], a[152:155], v[192:195], v[40:43]\n"
"ds_read_b128 a[144:147], v240 offset:3072\n"
"ds_read_b128 a[148:151], v240 offset:3584\n"
"v_mfma_f32_16x16x32_bf16 v[40:43], a[156:159], v[204:207], v[40:43]\n"
"v_mfma_f32_16x16x32_bf16 v[88:91], a[156:159], v[208:211], v[88:91]\n"
"buffer_load_dwordx4 a[92:95], v220, s[44:47], s63 offen offset:3328 sc0 nt\n"
"v_mfma_f32_16x16x32_bf16 v[88:91], a[152:155], v[196:199], v[88:91]\n"
"v_mfma_f32_16x16x32_bf16 v[136:139], a[152:155], v[200:203], v[136:139]\n"
"v_mfma_f32_16x16x32_bf16 v[136:139], a[156:159], v[212:215], v[136:139]\n"
"s_waitcnt lgkmcnt(2)\n"
"v_mfma_f32_16x16x32_bf16 v[44:47], a[160:163], v[192:195], v[44:47]\n"
"ds_read_b128 a[152:155], v240 offset:4096\n"
"ds_read_b128 a[156:159], v240 offset:4608\n"
"v_mfma_f32_16x16x32_bf16 v[44:47], a[164:167], v[204:207], v[44:47]\n"
"v_cvt_pk_bf16_f32 v192, v144, v145\n"
"v_cvt_pk_bf16_f32 v193, v146, v147\n"
"v_mfma_f32_16x16x32_bf16 v[92:95], a[164:167], v[208:211], v[92:95]\n"
"v_cvt_pk_bf16_f32 v194, v156, v157\n"
"v_cvt_pk_bf16_f32 v195, v158, v159\n"
"v_mfma_f32_16x16x32_bf16 v[92:95], a[160:163], v[196:199], v[92:95]\n"
"v_cvt_pk_bf16_f32 v196, v148, v149\n"
"v_cvt_pk_bf16_f32 v197, v150, v151\n"
"v_mfma_f32_16x16x32_bf16 v[140:143], a[160:163], v[200:203], v[140:143]\n"
"v_cvt_pk_bf16_f32 v200, v152, v153\n"
"v_cvt_pk_bf16_f32 v201, v154, v155\n"
"v_mfma_f32_16x16x32_bf16 v[140:143], a[164:167], v[212:215], v[140:143]\n"
"s_waitcnt lgkmcnt(2)\n"
"v_cvt_pk_bf16_f32 v198, v160, v161\n"
"v_cvt_pk_bf16_f32 v199, v162, v163\n"
"v_mfma_f32_16x16x32_bf16 v[144:147], a[144:147], a[0:3], 0\n"
"ds_read_b128 a[160:163], v240 offset:5120\n"
"ds_read_b128 a[164:167], v240 offset:5632\n"
"v_mfma_f32_16x16x32_bf16 v[144:147], a[148:151], a[4:7], v[144:147]\n"
"v_cvt_pk_bf16_f32 v202, v164, v165\n"
"v_cvt_pk_bf16_f32 v203, v166, v167\n"
"v_mfma_f32_16x16x32_bf16 v[148:151], a[148:151], a[28:31], 0\n"
"v_cvt_pk_bf16_f32 v204, v168, v169\n"
"v_cvt_pk_bf16_f32 v205, v170, v171\n"
"v_mfma_f32_16x16x32_bf16 v[148:151], a[144:147], a[24:27], v[148:151]\n"
"v_cvt_pk_bf16_f32 v208, v172, v173\n"
"v_cvt_pk_bf16_f32 v209, v174, v175\n"
"v_mfma_f32_16x16x32_bf16 v[152:155], a[144:147], a[48:51], 0\n"
"v_cvt_pk_bf16_f32 v212, v176, v177\n"
"v_cvt_pk_bf16_f32 v213, v178, v179\n"
"v_mfma_f32_16x16x32_bf16 v[152:155], a[148:151], a[52:55], v[152:155]\n"
"s_waitcnt lgkmcnt(2)\n"
"v_cvt_pk_bf16_f32 v206, v180, v181\n"
"v_cvt_pk_bf16_f32 v207, v182, v183\n"
"v_mfma_f32_16x16x32_bf16 v[144:147], a[152:155], a[8:11], v[144:147]\n"
"ds_read_b128 a[144:147], v240 offset:3328\n"
"ds_read_b128 a[148:151], v240 offset:3840\n"
"v_mfma_f32_16x16x32_bf16 v[144:147], a[156:159], a[12:15], v[144:147]\n"
"v_cvt_pk_bf16_f32 v210, v184, v185\n"
"v_cvt_pk_bf16_f32 v211, v186, v187\n"
"v_mfma_f32_16x16x32_bf16 v[148:151], a[156:159], a[36:39], v[148:151]\n"
"v_cvt_pk_bf16_f32 v214, v188, v189\n"
"v_cvt_pk_bf16_f32 v215, v190, v191\n"
"v_mfma_f32_16x16x32_bf16 v[148:151], a[152:155], a[32:35], v[148:151]\n"
"v_mfma_f32_16x16x32_bf16 v[152:155], a[152:155], a[56:59], v[152:155]\n"
"v_mfma_f32_16x16x32_bf16 v[152:155], a[156:159], a[60:63], v[152:155]\n"
"s_waitcnt lgkmcnt(2)\n"
"v_mfma_f32_16x16x32_bf16 v[144:147], a[160:163], a[16:19], v[144:147]\n"
"ds_read_b128 a[152:155], v240 offset:4352\n"
"ds_read_b128 a[156:159], v240 offset:4864\n"
"v_mfma_f32_16x16x32_bf16 v[144:147], a[164:167], a[20:23], v[144:147]\n"
"v_mfma_f32_16x16x32_bf16 v[148:151], a[164:167], a[44:47], v[148:151]\n"
"v_mfma_f32_16x16x32_bf16 v[148:151], a[160:163], a[40:43], v[148:151]\n"
"v_mfma_f32_16x16x32_bf16 v[152:155], a[160:163], a[64:67], v[152:155]\n"
"v_mfma_f32_16x16x32_bf16 v[152:155], a[164:167], a[68:71], v[152:155]\n"
"s_waitcnt lgkmcnt(2)\n"
"v_mfma_f32_16x16x32_bf16 v[156:159], a[144:147], a[0:3], 0\n"
"ds_read_b128 a[160:163], v240 offset:5376\n"
"ds_read_b128 a[164:167], v240 offset:5888\n"
"v_mfma_f32_16x16x32_bf16 v[156:159], a[148:151], a[4:7], v[156:159]\n"
"v_mfma_f32_16x16x32_bf16 v[160:163], a[148:151], a[28:31], 0\n"
"v_mfma_f32_16x16x32_bf16 v[160:163], a[144:147], a[24:27], v[160:163]\n"
"v_mul_f32 v144, s55, v144\n"
"v_mul_f32 v145, s55, v145\n"
"v_mfma_f32_16x16x32_bf16 v[164:167], a[144:147], a[48:51], 0\n"
"v_mul_f32 v146, s55, v146\n"
"v_mul_f32 v147, s55, v147\n"
"v_mfma_f32_16x16x32_bf16 v[164:167], a[148:151], a[52:55], v[164:167]\n"
"s_waitcnt lgkmcnt(2)\n"
"v_exp_f32 v144, v144\n"
"v_mfma_f32_16x16x32_bf16 v[156:159], a[152:155], a[8:11], v[156:159]\n"
"ds_read_b128 a[144:147], v240 offset:6144\n"
"ds_read_b128 a[148:151], v240 offset:6656\n"
"v_mfma_f32_16x16x32_bf16 v[156:159], a[156:159], a[12:15], v[156:159]\n"
"v_exp_f32 v145, v145\n"
"v_mfma_f32_16x16x32_bf16 v[160:163], a[156:159], a[36:39], v[160:163]\n"
"v_exp_f32 v146, v146\n"
"v_mfma_f32_16x16x32_bf16 v[160:163], a[152:155], a[32:35], v[160:163]\n"
"v_exp_f32 v147, v147\n"
"v_mfma_f32_16x16x32_bf16 v[164:167], a[152:155], a[56:59], v[164:167]\n"
"v_mul_f32 v148, s55, v148\n"
"v_mul_f32 v149, s55, v149\n"
"v_mfma_f32_16x16x32_bf16 v[164:167], a[156:159], a[60:63], v[164:167]\n"
"s_waitcnt lgkmcnt(2)\n"
"v_mul_f32 v150, s55, v150\n"
"v_mul_f32 v151, s55, v151\n"
"v_mfma_f32_16x16x32_bf16 v[156:159], a[160:163], a[16:19], v[156:159]\n"
"ds_read_b128 a[152:155], v240 offset:7168\n"
"ds_read_b128 a[156:159], v240 offset:7680\n"
"v_mfma_f32_16x16x32_bf16 v[156:159], a[164:167], a[20:23], v[156:159]\n"
"v_exp_f32 v148, v148\n"
"v_mfma_f32_16x16x32_bf16 v[160:163], a[164:167], a[44:47], v[160:163]\n"
"v_exp_f32 v149, v149\n"
"v_mfma_f32_16x16x32_bf16 v[160:163], a[160:163], a[40:43], v[160:163]\n"
"v_exp_f32 v150, v150\n"
"v_mfma_f32_16x16x32_bf16 v[164:167], a[160:163], a[64:67], v[164:167]\n"
"v_exp_f32 v151, v151\n"
"v_mfma_f32_16x16x32_bf16 v[164:167], a[164:167], a[68:71], v[164:167]\n"
"s_waitcnt lgkmcnt(2)\n"
"v_mul_f32 v152, s55, v152\n"
"v_mul_f32 v153, s55, v153\n"
"v_mfma_f32_16x16x32_bf16 v[168:171], a[144:147], a[0:3], 0\n"
"ds_read_b128 a[160:163], v240 offset:8192\n"
"ds_read_b128 a[164:167], v240 offset:8704\n"
"v_mfma_f32_16x16x32_bf16 v[168:171], a[148:151], a[4:7], v[168:171]\n"
"v_mul_f32 v154, s55, v154\n"
"v_mul_f32 v155, s55, v155\n"
"v_mfma_f32_16x16x32_bf16 v[172:175], a[148:151], a[28:31], 0\n"
"v_exp_f32 v152, v152\n"
"v_mfma_f32_16x16x32_bf16 v[172:175], a[144:147], a[24:27], v[172:175]\n"
"v_exp_f32 v153, v153\n"
"v_mfma_f32_16x16x32_bf16 v[176:179], a[144:147], a[48:51], 0\n"
"v_exp_f32 v154, v154\n"
"v_mfma_f32_16x16x32_bf16 v[176:179], a[148:151], a[52:55], v[176:179]\n"
"s_waitcnt lgkmcnt(2)\n"
"v_exp_f32 v155, v155\n"
"v_mfma_f32_16x16x32_bf16 v[168:171], a[152:155], a[8:11], v[168:171]\n"
"ds_read_b128 a[144:147], v240 offset:6400\n"
"ds_read_b128 a[148:151], v240 offset:6912\n"
"v_mfma_f32_16x16x32_bf16 v[168:171], a[156:159], a[12:15], v[168:171]\n"
"v_mul_f32 v156, s55, v156\n"
"v_mul_f32 v157, s55, v157\n"
"v_mfma_f32_16x16x32_bf16 v[172:175], a[156:159], a[36:39], v[172:175]\n"
"v_mul_f32 v158, s55, v158\n"
"v_mul_f32 v159, s55, v159\n"
"v_mfma_f32_16x16x32_bf16 v[172:175], a[152:155], a[32:35], v[172:175]\n"
"v_exp_f32 v156, v156\n"
"v_mfma_f32_16x16x32_bf16 v[176:179], a[152:155], a[56:59], v[176:179]\n"
"v_exp_f32 v157, v157\n"
"v_mfma_f32_16x16x32_bf16 v[176:179], a[156:159], a[60:63], v[176:179]\n"
"s_waitcnt lgkmcnt(2)\n"
"v_exp_f32 v158, v158\n"
"v_mfma_f32_16x16x32_bf16 v[168:171], a[160:163], a[16:19], v[168:171]\n"
"ds_read_b128 a[152:155], v240 offset:7424\n"
"ds_read_b128 a[156:159], v240 offset:7936\n"
"v_mfma_f32_16x16x32_bf16 v[168:171], a[164:167], a[20:23], v[168:171]\n"
"v_exp_f32 v159, v159\n"
"v_mfma_f32_16x16x32_bf16 v[172:175], a[164:167], a[44:47], v[172:175]\n"
"v_mul_f32 v160, s55, v160\n"
"v_mul_f32 v161, s55, v161\n"
"v_mfma_f32_16x16x32_bf16 v[172:175], a[160:163], a[40:43], v[172:175]\n"
"v_mul_f32 v162, s55, v162\n"
"v_mul_f32 v163, s55, v163\n"
"v_mfma_f32_16x16x32_bf16 v[176:179], a[160:163], a[64:67], v[176:179]\n"
"v_exp_f32 v160, v160\n"
"v_mfma_f32_16x16x32_bf16 v[176:179], a[164:167], a[68:71], v[176:179]\n"
"s_waitcnt lgkmcnt(2)\n"
"v_exp_f32 v161, v161\n"
"v_mfma_f32_16x16x32_bf16 v[180:183], a[144:147], a[0:3], 0\n"
"ds_read_b128 a[160:163], v240 offset:8448\n"
"ds_read_b128 a[164:167], v240 offset:8960\n"
"v_mfma_f32_16x16x32_bf16 v[180:183], a[148:151], a[4:7], v[180:183]\n"
"v_exp_f32 v162, v162\n"
"v_mfma_f32_16x16x32_bf16 v[184:187], a[148:151], a[28:31], 0\n"
"v_exp_f32 v163, v163\n"
"v_mfma_f32_16x16x32_bf16 v[184:187], a[144:147], a[24:27], v[184:187]\n"
"s_waitcnt vmcnt(18)\n"
"v_mul_f32 v164, s55, v164\n"
"v_mul_f32 v165, s55, v165\n"
"v_mfma_f32_16x16x32_bf16 v[188:191], a[144:147], a[48:51], 0\n"
"s_barrier\n"
"v_mul_f32 v166, s55, v166\n"
"v_mul_f32 v167, s55, v167\n"
"v_mfma_f32_16x16x32_bf16 v[188:191], a[148:151], a[52:55], v[188:191]\n"
"s_waitcnt lgkmcnt(2)\n"
"v_exp_f32 v164, v164\n"
"v_mfma_f32_16x16x32_bf16 v[180:183], a[152:155], a[8:11], v[180:183]\n"
"s_mov_b32 s53, 172032\n"
"ds_read_b64_tr_b16 a[144:145], v241 offset:21504\n"
"ds_read_b64_tr_b16 a[146:147], v241 offset:21760\n"
"v_mfma_f32_16x16x32_bf16 v[180:183], a[156:159], a[12:15], v[180:183]\n"
"s_add_u32 m0, s52, 0\n"
"ds_read_b64_tr_b16 a[148:149], v241 offset:24576\n"
"ds_read_b64_tr_b16 a[150:151], v241 offset:24832\n"
"v_mfma_f32_16x16x32_bf16 v[184:187], a[156:159], a[36:39], v[184:187]\n"
"buffer_load_dwordx4 v242, s[36:39], s53 offen offset:0 sc0 nt lds\n"
"v_mfma_f32_16x16x32_bf16 v[184:187], a[152:155], a[32:35], v[184:187]\n"
"v_exp_f32 v165, v165\n"
"v_mfma_f32_16x16x32_bf16 v[188:191], a[152:155], a[56:59], v[188:191]\n"
"v_exp_f32 v166, v166\n"
"v_mfma_f32_16x16x32_bf16 v[188:191], a[156:159], a[60:63], v[188:191]\n"
"s_waitcnt lgkmcnt(4)\n"
"v_exp_f32 v167, v167\n"
"v_mfma_f32_16x16x32_bf16 v[180:183], a[160:163], a[16:19], v[180:183]\n"
"ds_read_b64_tr_b16 a[152:153], v241 offset:21536\n"
"ds_read_b64_tr_b16 a[154:155], v241 offset:21792\n"
"v_mfma_f32_16x16x32_bf16 v[180:183], a[164:167], a[20:23], v[180:183]\n"
"s_add_u32 m0, s52, 896\n"
"ds_read_b64_tr_b16 a[156:157], v241 offset:24608\n"
"ds_read_b64_tr_b16 a[158:159], v241 offset:24864\n"
"v_mfma_f32_16x16x32_bf16 v[184:187], a[164:167], a[44:47], v[184:187]\n"
"buffer_load_dwordx4 v242, s[36:39], s53 offen offset:128 sc0 nt lds\n"
"v_mfma_f32_16x16x32_bf16 v[184:187], a[160:163], a[40:43], v[184:187]\n"
"v_mul_f32 v168, s55, v168\n"
"v_mul_f32 v169, s55, v169\n"
"v_mfma_f32_16x16x32_bf16 v[188:191], a[160:163], a[64:67], v[188:191]\n"
"v_mul_f32 v170, s55, v170\n"
"v_mul_f32 v171, s55, v171\n"
"v_mfma_f32_16x16x32_bf16 v[188:191], a[164:167], a[68:71], v[188:191]\n"
"v_exp_f32 v168, v168\n"
"v_mfma_f32_16x16x32_bf16 a[240:243], a[252:255], v[192:195], a[240:243]\n"
"v_exp_f32 v169, v169\n"
"v_mfma_f32_16x16x32_bf16 a[240:243], a[252:255], v[204:207], a[240:243]\n"
"s_add_u32 m0, s52, 1792\n"
"v_exp_f32 v170, v170\n"
"v_mfma_f32_16x16x32_bf16 a[244:247], a[252:255], v[196:199], a[244:247]\n"
"buffer_load_dwordx4 v242, s[36:39], s53 offen offset:256 sc0 nt lds\n"
"v_mfma_f32_16x16x32_bf16 a[244:247], a[252:255], v[208:211], a[244:247]\n"
"v_exp_f32 v171, v171\n"
"v_mfma_f32_16x16x32_bf16 a[248:251], a[252:255], v[200:203], a[248:251]\n"
"v_mul_f32 v172, s55, v172\n"
"v_mul_f32 v173, s55, v173\n"
"v_mfma_f32_16x16x32_bf16 a[248:251], a[252:255], v[212:215], a[248:251]\n"
"s_waitcnt lgkmcnt(4)\n"
"v_mul_f32 v174, s55, v174\n"
"v_mul_f32 v175, s55, v175\n"
"v_mfma_f32_16x16x32_bf16 v[0:3], a[144:147], v[192:195], v[0:3]\n"
"s_mov_b32 s54, 184320\n"
"ds_read_b64_tr_b16 a[160:161], v241 offset:22016\n"
"ds_read_b64_tr_b16 a[162:163], v241 offset:22272\n"
"v_mfma_f32_16x16x32_bf16 v[0:3], a[148:151], v[204:207], v[0:3]\n"
"s_add_u32 m0, s52, 3072\n"
"ds_read_b64_tr_b16 a[164:165], v241 offset:25088\n"
"ds_read_b64_tr_b16 a[166:167], v241 offset:25344\n"
"v_mfma_f32_16x16x32_bf16 v[48:51], a[148:151], v[208:211], v[48:51]\n"
"buffer_load_dwordx4 v242, s[36:39], s54 offen offset:0 sc0 nt lds\n"
"v_mfma_f32_16x16x32_bf16 v[48:51], a[144:147], v[196:199], v[48:51]\n"
"v_exp_f32 v172, v172\n"
"v_mfma_f32_16x16x32_bf16 v[96:99], a[144:147], v[200:203], v[96:99]\n"
"v_exp_f32 v173, v173\n"
"v_mfma_f32_16x16x32_bf16 v[96:99], a[148:151], v[212:215], v[96:99]\n"
"s_waitcnt lgkmcnt(4)\n"
"v_exp_f32 v174, v174\n"
"v_mfma_f32_16x16x32_bf16 v[4:7], a[152:155], v[192:195], v[4:7]\n"
"ds_read_b64_tr_b16 a[144:145], v241 offset:22048\n"
"ds_read_b64_tr_b16 a[146:147], v241 offset:22304\n"
"v_mfma_f32_16x16x32_bf16 v[4:7], a[156:159], v[204:207], v[4:7]\n"
"s_add_u32 m0, s52, 3968\n"
"ds_read_b64_tr_b16 a[148:149], v241 offset:25120\n"
"ds_read_b64_tr_b16 a[150:151], v241 offset:25376\n"
"v_mfma_f32_16x16x32_bf16 v[52:55], a[156:159], v[208:211], v[52:55]\n"
"buffer_load_dwordx4 v242, s[36:39], s54 offen offset:128 sc0 nt lds\n"
"v_mfma_f32_16x16x32_bf16 v[52:55], a[152:155], v[196:199], v[52:55]\n"
"v_exp_f32 v175, v175\n"
"v_mfma_f32_16x16x32_bf16 v[100:103], a[152:155], v[200:203], v[100:103]\n"
"v_mul_f32 v176, s55, v176\n"
"v_mul_f32 v177, s55, v177\n"
"v_mfma_f32_16x16x32_bf16 v[100:103], a[156:159], v[212:215], v[100:103]\n"
"s_waitcnt lgkmcnt(4)\n"
"v_mul_f32 v178, s55, v178\n"
"v_mul_f32 v179, s55, v179\n"
"v_mfma_f32_16x16x32_bf16 v[8:11], a[160:163], v[192:195], v[8:11]\n"
"ds_read_b64_tr_b16 a[152:153], v241 offset:22528\n"
"ds_read_b64_tr_b16 a[154:155], v241 offset:22784\n"
"v_mfma_f32_16x16x32_bf16 v[8:11], a[164:167], v[204:207], v[8:11]\n"
"s_add_u32 m0, s52, 4864\n"
"ds_read_b64_tr_b16 a[156:157], v241 offset:25600\n"
"ds_read_b64_tr_b16 a[158:159], v241 offset:25856\n"
"v_mfma_f32_16x16x32_bf16 v[56:59], a[164:167], v[208:211], v[56:59]\n"
"buffer_load_dwordx4 v242, s[36:39], s54 offen offset:256 sc0 nt lds\n"
"v_mfma_f32_16x16x32_bf16 v[56:59], a[160:163], v[196:199], v[56:59]\n"
"v_exp_f32 v176, v176\n"
"v_mfma_f32_16x16x32_bf16 v[104:107], a[160:163], v[200:203], v[104:107]\n"
"v_exp_f32 v177, v177\n"
"v_mfma_f32_16x16x32_bf16 v[104:107], a[164:167], v[212:215], v[104:107]\n"
"s_waitcnt lgkmcnt(4)\n"
"v_exp_f32 v178, v178\n"
"v_mfma_f32_16x16x32_bf16 v[12:15], a[144:147], v[192:195], v[12:15]\n"
"s_mov_b32 s59, 122880\n"
"ds_read_b64_tr_b16 a[160:161], v241 offset:22560\n"
"ds_read_b64_tr_b16 a[162:163], v241 offset:22816\n"
"v_mfma_f32_16x16x32_bf16 v[12:15], a[148:151], v[204:207], v[12:15]\n"
"s_add_u32 m0, s52, 33792\n"
"ds_read_b64_tr_b16 a[164:165], v241 offset:25632\n"
"ds_read_b64_tr_b16 a[166:167], v241 offset:25888\n"
"v_mfma_f32_16x16x32_bf16 v[60:63], a[148:151], v[208:211], v[60:63]\n"
"buffer_load_dwordx4 v242, s[40:43], s59 offen offset:0 sc0 nt lds\n"
"v_mfma_f32_16x16x32_bf16 v[60:63], a[144:147], v[196:199], v[60:63]\n"
"v_exp_f32 v179, v179\n"
"v_mfma_f32_16x16x32_bf16 v[108:111], a[144:147], v[200:203], v[108:111]\n"
"v_mul_f32 v180, s55, v180\n"
"v_mul_f32 v181, s55, v181\n"
"v_mfma_f32_16x16x32_bf16 v[108:111], a[148:151], v[212:215], v[108:111]\n"
"s_waitcnt lgkmcnt(4)\n"
"v_mul_f32 v182, s55, v182\n"
"v_mul_f32 v183, s55, v183\n"
"v_mfma_f32_16x16x32_bf16 v[16:19], a[152:155], v[192:195], v[16:19]\n"
"ds_read_b64_tr_b16 a[144:145], v241 offset:23040\n"
"ds_read_b64_tr_b16 a[146:147], v241 offset:23296\n"
"v_mfma_f32_16x16x32_bf16 v[16:19], a[156:159], v[204:207], v[16:19]\n"
"s_add_u32 m0, s52, 34688\n"
"ds_read_b64_tr_b16 a[148:149], v241 offset:26112\n"
"ds_read_b64_tr_b16 a[150:151], v241 offset:26368\n"
"v_mfma_f32_16x16x32_bf16 v[64:67], a[156:159], v[208:211], v[64:67]\n"
"buffer_load_dwordx4 v242, s[40:43], s59 offen offset:128 sc0 nt lds\n"
"v_mfma_f32_16x16x32_bf16 v[64:67], a[152:155], v[196:199], v[64:67]\n"
"v_exp_f32 v180, v180\n"
"v_mfma_f32_16x16x32_bf16 v[112:115], a[152:155], v[200:203], v[112:115]\n"
"v_exp_f32 v181, v181\n"
"v_mfma_f32_16x16x32_bf16 v[112:115], a[156:159], v[212:215], v[112:115]\n"
"s_waitcnt lgkmcnt(4)\n"
"v_exp_f32 v182, v182\n"
"v_mfma_f32_16x16x32_bf16 v[20:23], a[160:163], v[192:195], v[20:23]\n"
"ds_read_b64_tr_b16 a[152:153], v241 offset:23072\n"
"ds_read_b64_tr_b16 a[154:155], v241 offset:23328\n"
"v_mfma_f32_16x16x32_bf16 v[20:23], a[164:167], v[204:207], v[20:23]\n"
"s_add_u32 m0, s52, 35584\n"
"ds_read_b64_tr_b16 a[156:157], v241 offset:26144\n"
"ds_read_b64_tr_b16 a[158:159], v241 offset:26400\n"
"v_mfma_f32_16x16x32_bf16 v[68:71], a[164:167], v[208:211], v[68:71]\n"
"buffer_load_dwordx4 v242, s[40:43], s59 offen offset:256 sc0 nt lds\n"
"v_mfma_f32_16x16x32_bf16 v[68:71], a[160:163], v[196:199], v[68:71]\n"
"v_exp_f32 v183, v183\n"
"v_mfma_f32_16x16x32_bf16 v[116:119], a[160:163], v[200:203], v[116:119]\n"
"v_mul_f32 v184, s55, v184\n"
"v_mul_f32 v185, s55, v185\n"
"v_mfma_f32_16x16x32_bf16 v[116:119], a[164:167], v[212:215], v[116:119]\n"
"s_waitcnt lgkmcnt(4)\n"
"v_mul_f32 v186, s55, v186\n"
"v_mul_f32 v187, s55, v187\n"
"v_mfma_f32_16x16x32_bf16 v[24:27], a[144:147], v[192:195], v[24:27]\n"
"s_mov_b32 s70, 135168\n"
"ds_read_b64_tr_b16 a[160:161], v241 offset:23552\n"
"ds_read_b64_tr_b16 a[162:163], v241 offset:23808\n"
"v_mfma_f32_16x16x32_bf16 v[24:27], a[148:151], v[204:207], v[24:27]\n"
"s_add_u32 m0, s52, 36864\n"
"ds_read_b64_tr_b16 a[164:165], v241 offset:26624\n"
"ds_read_b64_tr_b16 a[166:167], v241 offset:26880\n"
"v_mfma_f32_16x16x32_bf16 v[72:75], a[148:151], v[208:211], v[72:75]\n"
"buffer_load_dwordx4 v242, s[40:43], s70 offen offset:0 sc0 nt lds\n"
"v_mfma_f32_16x16x32_bf16 v[72:75], a[144:147], v[196:199], v[72:75]\n"
"v_exp_f32 v184, v184\n"
"v_mfma_f32_16x16x32_bf16 v[120:123], a[144:147], v[200:203], v[120:123]\n"
"v_exp_f32 v185, v185\n"
"v_mfma_f32_16x16x32_bf16 v[120:123], a[148:151], v[212:215], v[120:123]\n"
"s_waitcnt lgkmcnt(4)\n"
"v_exp_f32 v186, v186\n"
"v_mfma_f32_16x16x32_bf16 v[28:31], a[152:155], v[192:195], v[28:31]\n"
"ds_read_b64_tr_b16 a[144:145], v241 offset:23584\n"
"ds_read_b64_tr_b16 a[146:147], v241 offset:23840\n"
"v_mfma_f32_16x16x32_bf16 v[28:31], a[156:159], v[204:207], v[28:31]\n"
"s_add_u32 m0, s52, 37760\n"
"ds_read_b64_tr_b16 a[148:149], v241 offset:26656\n"
"ds_read_b64_tr_b16 a[150:151], v241 offset:26912\n"
"v_mfma_f32_16x16x32_bf16 v[76:79], a[156:159], v[208:211], v[76:79]\n"
"buffer_load_dwordx4 v242, s[40:43], s70 offen offset:128 sc0 nt lds\n"
"v_mfma_f32_16x16x32_bf16 v[76:79], a[152:155], v[196:199], v[76:79]\n"
"v_exp_f32 v187, v187\n"
"v_mfma_f32_16x16x32_bf16 v[124:127], a[152:155], v[200:203], v[124:127]\n"
"v_mul_f32 v188, s55, v188\n"
"v_mul_f32 v189, s55, v189\n"
"v_mfma_f32_16x16x32_bf16 v[124:127], a[156:159], v[212:215], v[124:127]\n"
"s_waitcnt lgkmcnt(4)\n"
"v_mul_f32 v190, s55, v190\n"
"v_mul_f32 v191, s55, v191\n"
"v_mfma_f32_16x16x32_bf16 v[32:35], a[160:163], v[192:195], v[32:35]\n"
"ds_read_b64_tr_b16 a[152:153], v241 offset:24064\n"
"ds_read_b64_tr_b16 a[154:155], v241 offset:24320\n"
"v_mfma_f32_16x16x32_bf16 v[32:35], a[164:167], v[204:207], v[32:35]\n"
"s_add_u32 m0, s52, 38656\n"
"ds_read_b64_tr_b16 a[156:157], v241 offset:27136\n"
"ds_read_b64_tr_b16 a[158:159], v241 offset:27392\n"
"v_mfma_f32_16x16x32_bf16 v[80:83], a[164:167], v[208:211], v[80:83]\n"
"buffer_load_dwordx4 v242, s[40:43], s70 offen offset:256 sc0 nt lds\n"
"v_mfma_f32_16x16x32_bf16 v[80:83], a[160:163], v[196:199], v[80:83]\n"
"v_exp_f32 v188, v188\n"
"v_mfma_f32_16x16x32_bf16 v[128:131], a[160:163], v[200:203], v[128:131]\n"
"v_exp_f32 v189, v189\n"
"v_mfma_f32_16x16x32_bf16 v[128:131], a[164:167], v[212:215], v[128:131]\n"
"s_waitcnt lgkmcnt(4)\n"
"v_exp_f32 v190, v190\n"
"v_mfma_f32_16x16x32_bf16 v[36:39], a[144:147], v[192:195], v[36:39]\n"
"ds_read_b64_tr_b16 a[160:161], v241 offset:24096\n"
"ds_read_b64_tr_b16 a[162:163], v241 offset:24352\n"
"v_mfma_f32_16x16x32_bf16 v[36:39], a[148:151], v[204:207], v[36:39]\n"
"ds_read_b64_tr_b16 a[164:165], v241 offset:27168\n"
"ds_read_b64_tr_b16 a[166:167], v241 offset:27424\n"
"v_mfma_f32_16x16x32_bf16 v[84:87], a[148:151], v[208:211], v[84:87]\n"
"buffer_load_dwordx4 a[112:115], v220, s[44:47], s64 offen offset:256 sc0 nt\n"
"v_mfma_f32_16x16x32_bf16 v[84:87], a[144:147], v[196:199], v[84:87]\n"
"v_exp_f32 v191, v191\n"
"v_mfma_f32_16x16x32_bf16 v[132:135], a[144:147], v[200:203], v[132:135]\n"
"v_mfma_f32_16x16x32_bf16 v[132:135], a[148:151], v[212:215], v[132:135]\n"
"s_waitcnt lgkmcnt(4)\n"
"v_mfma_f32_16x16x32_bf16 v[40:43], a[152:155], v[192:195], v[40:43]\n"
"ds_read_b128 a[144:147], v240 offset:9216\n"
"ds_read_b128 a[148:151], v240 offset:9728\n"
"v_mfma_f32_16x16x32_bf16 v[40:43], a[156:159], v[204:207], v[40:43]\n"
"v_mfma_f32_16x16x32_bf16 v[88:91], a[156:159], v[208:211], v[88:91]\n"
"buffer_load_dwordx4 a[116:119], v220, s[44:47], s64 offen offset:3328 sc0 nt\n"
"v_mfma_f32_16x16x32_bf16 v[88:91], a[152:155], v[196:199], v[88:91]\n"
"v_mfma_f32_16x16x32_bf16 v[136:139], a[152:155], v[200:203], v[136:139]\n"
"v_mfma_f32_16x16x32_bf16 v[136:139], a[156:159], v[212:215], v[136:139]\n"
"s_waitcnt lgkmcnt(2)\n"
"v_mfma_f32_16x16x32_bf16 v[44:47], a[160:163], v[192:195], v[44:47]\n"
"ds_read_b128 a[152:155], v240 offset:10240\n"
"ds_read_b128 a[156:159], v240 offset:10752\n"
"v_mfma_f32_16x16x32_bf16 v[44:47], a[164:167], v[204:207], v[44:47]\n"
"v_cvt_pk_bf16_f32 v192, v144, v145\n"
"v_cvt_pk_bf16_f32 v193, v146, v147\n"
"v_mfma_f32_16x16x32_bf16 v[92:95], a[164:167], v[208:211], v[92:95]\n"
"v_cvt_pk_bf16_f32 v194, v156, v157\n"
"v_cvt_pk_bf16_f32 v195, v158, v159\n"
"v_mfma_f32_16x16x32_bf16 v[92:95], a[160:163], v[196:199], v[92:95]\n"
"v_cvt_pk_bf16_f32 v196, v148, v149\n"
"v_cvt_pk_bf16_f32 v197, v150, v151\n"
"v_mfma_f32_16x16x32_bf16 v[140:143], a[160:163], v[200:203], v[140:143]\n"
"v_cvt_pk_bf16_f32 v200, v152, v153\n"
"v_cvt_pk_bf16_f32 v201, v154, v155\n"
"v_mfma_f32_16x16x32_bf16 v[140:143], a[164:167], v[212:215], v[140:143]\n"
"s_waitcnt lgkmcnt(2)\n"
"v_cvt_pk_bf16_f32 v198, v160, v161\n"
"v_cvt_pk_bf16_f32 v199, v162, v163\n"
"v_mfma_f32_16x16x32_bf16 v[144:147], a[144:147], a[0:3], 0\n"
"ds_read_b128 a[160:163], v240 offset:11264\n"
"ds_read_b128 a[164:167], v240 offset:11776\n"
"v_mfma_f32_16x16x32_bf16 v[144:147], a[148:151], a[4:7], v[144:147]\n"
"v_cvt_pk_bf16_f32 v202, v164, v165\n"
"v_cvt_pk_bf16_f32 v203, v166, v167\n"
"v_mfma_f32_16x16x32_bf16 v[148:151], a[148:151], a[28:31], 0\n"
"v_cvt_pk_bf16_f32 v204, v168, v169\n"
"v_cvt_pk_bf16_f32 v205, v170, v171\n"
"v_mfma_f32_16x16x32_bf16 v[148:151], a[144:147], a[24:27], v[148:151]\n"
"v_cvt_pk_bf16_f32 v208, v172, v173\n"
"v_cvt_pk_bf16_f32 v209, v174, v175\n"
"v_mfma_f32_16x16x32_bf16 v[152:155], a[144:147], a[48:51], 0\n"
"v_cvt_pk_bf16_f32 v212, v176, v177\n"
"v_cvt_pk_bf16_f32 v213, v178, v179\n"
"v_mfma_f32_16x16x32_bf16 v[152:155], a[148:151], a[52:55], v[152:155]\n"
"s_waitcnt lgkmcnt(2)\n"
"v_cvt_pk_bf16_f32 v206, v180, v181\n"
"v_cvt_pk_bf16_f32 v207, v182, v183\n"
"v_mfma_f32_16x16x32_bf16 v[144:147], a[152:155], a[8:11], v[144:147]\n"
"ds_read_b128 a[144:147], v240 offset:9472\n"
"ds_read_b128 a[148:151], v240 offset:9984\n"
"v_mfma_f32_16x16x32_bf16 v[144:147], a[156:159], a[12:15], v[144:147]\n"
"v_cvt_pk_bf16_f32 v210, v184, v185\n"
"v_cvt_pk_bf16_f32 v211, v186, v187\n"
"v_mfma_f32_16x16x32_bf16 v[148:151], a[156:159], a[36:39], v[148:151]\n"
"v_cvt_pk_bf16_f32 v214, v188, v189\n"
"v_cvt_pk_bf16_f32 v215, v190, v191\n"
"v_mfma_f32_16x16x32_bf16 v[148:151], a[152:155], a[32:35], v[148:151]\n"
"v_mfma_f32_16x16x32_bf16 v[152:155], a[152:155], a[56:59], v[152:155]\n"
"v_mfma_f32_16x16x32_bf16 v[152:155], a[156:159], a[60:63], v[152:155]\n"
"s_waitcnt lgkmcnt(2)\n"
"v_mfma_f32_16x16x32_bf16 v[144:147], a[160:163], a[16:19], v[144:147]\n"
"ds_read_b128 a[152:155], v240 offset:10496\n"
"ds_read_b128 a[156:159], v240 offset:11008\n"
"v_mfma_f32_16x16x32_bf16 v[144:147], a[164:167], a[20:23], v[144:147]\n"
"v_mfma_f32_16x16x32_bf16 v[148:151], a[164:167], a[44:47], v[148:151]\n"
"v_mfma_f32_16x16x32_bf16 v[148:151], a[160:163], a[40:43], v[148:151]\n"
"v_mfma_f32_16x16x32_bf16 v[152:155], a[160:163], a[64:67], v[152:155]\n"
"v_mfma_f32_16x16x32_bf16 v[152:155], a[164:167], a[68:71], v[152:155]\n"
"s_waitcnt lgkmcnt(2)\n"
"v_mfma_f32_16x16x32_bf16 v[156:159], a[144:147], a[0:3], 0\n"
"ds_read_b128 a[160:163], v240 offset:11520\n"
"ds_read_b128 a[164:167], v240 offset:12032\n"
"v_mfma_f32_16x16x32_bf16 v[156:159], a[148:151], a[4:7], v[156:159]\n"
"v_mfma_f32_16x16x32_bf16 v[160:163], a[148:151], a[28:31], 0\n"
"v_mfma_f32_16x16x32_bf16 v[160:163], a[144:147], a[24:27], v[160:163]\n"
"v_mul_f32 v144, s55, v144\n"
"v_mul_f32 v145, s55, v145\n"
"v_mfma_f32_16x16x32_bf16 v[164:167], a[144:147], a[48:51], 0\n"
"v_mul_f32 v146, s55, v146\n"
"v_mul_f32 v147, s55, v147\n"
"v_mfma_f32_16x16x32_bf16 v[164:167], a[148:151], a[52:55], v[164:167]\n"
"s_waitcnt lgkmcnt(2)\n"
"v_exp_f32 v144, v144\n"
"v_mfma_f32_16x16x32_bf16 v[156:159], a[152:155], a[8:11], v[156:159]\n"
"ds_read_b128 a[144:147], v240 offset:12288\n"
"ds_read_b128 a[148:151], v240 offset:12800\n"
"v_mfma_f32_16x16x32_bf16 v[156:159], a[156:159], a[12:15], v[156:159]\n"
"v_exp_f32 v145, v145\n"
"v_mfma_f32_16x16x32_bf16 v[160:163], a[156:159], a[36:39], v[160:163]\n"
"v_exp_f32 v146, v146\n"
"v_mfma_f32_16x16x32_bf16 v[160:163], a[152:155], a[32:35], v[160:163]\n"
"v_exp_f32 v147, v147\n"
"v_mfma_f32_16x16x32_bf16 v[164:167], a[152:155], a[56:59], v[164:167]\n"
"v_mul_f32 v148, s55, v148\n"
"v_mul_f32 v149, s55, v149\n"
"v_mfma_f32_16x16x32_bf16 v[164:167], a[156:159], a[60:63], v[164:167]\n"
"s_waitcnt lgkmcnt(2)\n"
"v_mul_f32 v150, s55, v150\n"
"v_mul_f32 v151, s55, v151\n"
"v_mfma_f32_16x16x32_bf16 v[156:159], a[160:163], a[16:19], v[156:159]\n"
"ds_read_b128 a[152:155], v240 offset:13312\n"
"ds_read_b128 a[156:159], v240 offset:13824\n"
"v_mfma_f32_16x16x32_bf16 v[156:159], a[164:167], a[20:23], v[156:159]\n"
"v_exp_f32 v148, v148\n"
"v_mfma_f32_16x16x32_bf16 v[160:163], a[164:167], a[44:47], v[160:163]\n"
"v_exp_f32 v149, v149\n"
"v_mfma_f32_16x16x32_bf16 v[160:163], a[160:163], a[40:43], v[160:163]\n"
"v_exp_f32 v150, v150\n"
"v_mfma_f32_16x16x32_bf16 v[164:167], a[160:163], a[64:67], v[164:167]\n"
"v_exp_f32 v151, v151\n"
"v_mfma_f32_16x16x32_bf16 v[164:167], a[164:167], a[68:71], v[164:167]\n"
"s_waitcnt lgkmcnt(2)\n"
"v_mul_f32 v152, s55, v152\n"
"v_mul_f32 v153, s55, v153\n"
"v_mfma_f32_16x16x32_bf16 v[168:171], a[144:147], a[0:3], 0\n"
"ds_read_b128 a[160:163], v240 offset:14336\n"
"ds_read_b128 a[164:167], v240 offset:14848\n"
"v_mfma_f32_16x16x32_bf16 v[168:171], a[148:151], a[4:7], v[168:171]\n"
"v_mul_f32 v154, s55, v154\n"
"v_mul_f32 v155, s55, v155\n"
"v_mfma_f32_16x16x32_bf16 v[172:175], a[148:151], a[28:31], 0\n"
"v_exp_f32 v152, v152\n"
"v_mfma_f32_16x16x32_bf16 v[172:175], a[144:147], a[24:27], v[172:175]\n"
"v_exp_f32 v153, v153\n"
"v_mfma_f32_16x16x32_bf16 v[176:179], a[144:147], a[48:51], 0\n"
"v_exp_f32 v154, v154\n"
"v_mfma_f32_16x16x32_bf16 v[176:179], a[148:151], a[52:55], v[176:179]\n"
"s_waitcnt lgkmcnt(2)\n"
"v_exp_f32 v155, v155\n"
"v_mfma_f32_16x16x32_bf16 v[168:171], a[152:155], a[8:11], v[168:171]\n"
"ds_read_b128 a[144:147], v240 offset:12544\n"
"ds_read_b128 a[148:151], v240 offset:13056\n"
"v_mfma_f32_16x16x32_bf16 v[168:171], a[156:159], a[12:15], v[168:171]\n"
"v_mul_f32 v156, s55, v156\n"
"v_mul_f32 v157, s55, v157\n"
"v_mfma_f32_16x16x32_bf16 v[172:175], a[156:159], a[36:39], v[172:175]\n"
"v_mul_f32 v158, s55, v158\n"
"v_mul_f32 v159, s55, v159\n"
"v_mfma_f32_16x16x32_bf16 v[172:175], a[152:155], a[32:35], v[172:175]\n"
"v_exp_f32 v156, v156\n"
"v_mfma_f32_16x16x32_bf16 v[176:179], a[152:155], a[56:59], v[176:179]\n"
"v_exp_f32 v157, v157\n"
"v_mfma_f32_16x16x32_bf16 v[176:179], a[156:159], a[60:63], v[176:179]\n"
"s_waitcnt lgkmcnt(2)\n"
"v_exp_f32 v158, v158\n"
"v_mfma_f32_16x16x32_bf16 v[168:171], a[160:163], a[16:19], v[168:171]\n"
"ds_read_b128 a[152:155], v240 offset:13568\n"
"ds_read_b128 a[156:159], v240 offset:14080\n"
"v_mfma_f32_16x16x32_bf16 v[168:171], a[164:167], a[20:23], v[168:171]\n"
"v_exp_f32 v159, v159\n"
"v_mfma_f32_16x16x32_bf16 v[172:175], a[164:167], a[44:47], v[172:175]\n"
"v_mul_f32 v160, s55, v160\n"
"v_mul_f32 v161, s55, v161\n"
"v_mfma_f32_16x16x32_bf16 v[172:175], a[160:163], a[40:43], v[172:175]\n"
"v_mul_f32 v162, s55, v162\n"
"v_mul_f32 v163, s55, v163\n"
"v_mfma_f32_16x16x32_bf16 v[176:179], a[160:163], a[64:67], v[176:179]\n"
"v_exp_f32 v160, v160\n"
"v_mfma_f32_16x16x32_bf16 v[176:179], a[164:167], a[68:71], v[176:179]\n"
"s_waitcnt lgkmcnt(2)\n"
"v_exp_f32 v161, v161\n"
"v_mfma_f32_16x16x32_bf16 v[180:183], a[144:147], a[0:3], 0\n"
"ds_read_b128 a[160:163], v240 offset:14592\n"
"ds_read_b128 a[164:167], v240 offset:15104\n"
"v_mfma_f32_16x16x32_bf16 v[180:183], a[148:151], a[4:7], v[180:183]\n"
"v_exp_f32 v162, v162\n"
"v_mfma_f32_16x16x32_bf16 v[184:187], a[148:151], a[28:31], 0\n"
"v_exp_f32 v163, v163\n"
"v_mfma_f32_16x16x32_bf16 v[184:187], a[144:147], a[24:27], v[184:187]\n"
"s_waitcnt vmcnt(16)\n"
"v_mul_f32 v164, s55, v164\n"
"v_mul_f32 v165, s55, v165\n"
"v_mfma_f32_16x16x32_bf16 v[188:191], a[144:147], a[48:51], 0\n"
"s_barrier\n"
"v_mul_f32 v166, s55, v166\n"
"v_mul_f32 v167, s55, v167\n"
"v_mfma_f32_16x16x32_bf16 v[188:191], a[148:151], a[52:55], v[188:191]\n"
"s_waitcnt lgkmcnt(2)\n"
"v_exp_f32 v164, v164\n"
"v_mfma_f32_16x16x32_bf16 v[180:183], a[152:155], a[8:11], v[180:183]\n"
"s_mov_b32 s53, 196608\n"
"ds_read_b64_tr_b16 a[144:145], v241 offset:27648\n"
"ds_read_b64_tr_b16 a[146:147], v241 offset:27904\n"
"v_mfma_f32_16x16x32_bf16 v[180:183], a[156:159], a[12:15], v[180:183]\n"
"s_add_u32 m0, s52, 6144\n"
"ds_read_b64_tr_b16 a[148:149], v241 offset:30720\n"
"ds_read_b64_tr_b16 a[150:151], v241 offset:30976\n"
"v_mfma_f32_16x16x32_bf16 v[184:187], a[156:159], a[36:39], v[184:187]\n"
"buffer_load_dwordx4 v242, s[36:39], s53 offen offset:0 sc0 nt lds\n"
"v_mfma_f32_16x16x32_bf16 v[184:187], a[152:155], a[32:35], v[184:187]\n"
"v_exp_f32 v165, v165\n"
"v_mfma_f32_16x16x32_bf16 v[188:191], a[152:155], a[56:59], v[188:191]\n"
"v_exp_f32 v166, v166\n"
"v_mfma_f32_16x16x32_bf16 v[188:191], a[156:159], a[60:63], v[188:191]\n"
"s_waitcnt lgkmcnt(4)\n"
"v_exp_f32 v167, v167\n"
"v_mfma_f32_16x16x32_bf16 v[180:183], a[160:163], a[16:19], v[180:183]\n"
"ds_read_b64_tr_b16 a[152:153], v241 offset:27680\n"
"ds_read_b64_tr_b16 a[154:155], v241 offset:27936\n"
"v_mfma_f32_16x16x32_bf16 v[180:183], a[164:167], a[20:23], v[180:183]\n"
"s_add_u32 m0, s52, 7040\n"
"ds_read_b64_tr_b16 a[156:157], v241 offset:30752\n"
"ds_read_b64_tr_b16 a[158:159], v241 offset:31008\n"
"v_mfma_f32_16x16x32_bf16 v[184:187], a[164:167], a[44:47], v[184:187]\n"
"buffer_load_dwordx4 v242, s[36:39], s53 offen offset:128 sc0 nt lds\n"
"v_mfma_f32_16x16x32_bf16 v[184:187], a[160:163], a[40:43], v[184:187]\n"
"v_mul_f32 v168, s55, v168\n"
"v_mul_f32 v169, s55, v169\n"
"v_mfma_f32_16x16x32_bf16 v[188:191], a[160:163], a[64:67], v[188:191]\n"
"v_mul_f32 v170, s55, v170\n"
"v_mul_f32 v171, s55, v171\n"
"v_mfma_f32_16x16x32_bf16 v[188:191], a[164:167], a[68:71], v[188:191]\n"
"v_exp_f32 v168, v168\n"
"v_mfma_f32_16x16x32_bf16 a[240:243], a[252:255], v[192:195], a[240:243]\n"
"v_exp_f32 v169, v169\n"
"v_mfma_f32_16x16x32_bf16 a[240:243], a[252:255], v[204:207], a[240:243]\n"
"s_add_u32 m0, s52, 7936\n"
"v_exp_f32 v170, v170\n"
"v_mfma_f32_16x16x32_bf16 a[244:247], a[252:255], v[196:199], a[244:247]\n"
"buffer_load_dwordx4 v242, s[36:39], s53 offen offset:256 sc0 nt lds\n"
"v_mfma_f32_16x16x32_bf16 a[244:247], a[252:255], v[208:211], a[244:247]\n"
"v_exp_f32 v171, v171\n"
"v_mfma_f32_16x16x32_bf16 a[248:251], a[252:255], v[200:203], a[248:251]\n"
"v_mul_f32 v172, s55, v172\n"
"v_mul_f32 v173, s55, v173\n"
"v_mfma_f32_16x16x32_bf16 a[248:251], a[252:255], v[212:215], a[248:251]\n"
"s_waitcnt lgkmcnt(4)\n"
"v_mul_f32 v174, s55, v174\n"
"v_mul_f32 v175, s55, v175\n"
"v_mfma_f32_16x16x32_bf16 v[0:3], a[144:147], v[192:195], v[0:3]\n"
"s_mov_b32 s54, 208896\n"
"ds_read_b64_tr_b16 a[160:161], v241 offset:28160\n"
"ds_read_b64_tr_b16 a[162:163], v241 offset:28416\n"
"v_mfma_f32_16x16x32_bf16 v[0:3], a[148:151], v[204:207], v[0:3]\n"
"s_add_u32 m0, s52, 9216\n"
"ds_read_b64_tr_b16 a[164:165], v241 offset:31232\n"
"ds_read_b64_tr_b16 a[166:167], v241 offset:31488\n"
"v_mfma_f32_16x16x32_bf16 v[48:51], a[148:151], v[208:211], v[48:51]\n"
"buffer_load_dwordx4 v242, s[36:39], s54 offen offset:0 sc0 nt lds\n"
"v_mfma_f32_16x16x32_bf16 v[48:51], a[144:147], v[196:199], v[48:51]\n"
"v_exp_f32 v172, v172\n"
"v_mfma_f32_16x16x32_bf16 v[96:99], a[144:147], v[200:203], v[96:99]\n"
"v_exp_f32 v173, v173\n"
"v_mfma_f32_16x16x32_bf16 v[96:99], a[148:151], v[212:215], v[96:99]\n"
"s_waitcnt lgkmcnt(4)\n"
"v_exp_f32 v174, v174\n"
"v_mfma_f32_16x16x32_bf16 v[4:7], a[152:155], v[192:195], v[4:7]\n"
"ds_read_b64_tr_b16 a[144:145], v241 offset:28192\n"
"ds_read_b64_tr_b16 a[146:147], v241 offset:28448\n"
"v_mfma_f32_16x16x32_bf16 v[4:7], a[156:159], v[204:207], v[4:7]\n"
"s_add_u32 m0, s52, 10112\n"
"ds_read_b64_tr_b16 a[148:149], v241 offset:31264\n"
"ds_read_b64_tr_b16 a[150:151], v241 offset:31520\n"
"v_mfma_f32_16x16x32_bf16 v[52:55], a[156:159], v[208:211], v[52:55]\n"
"buffer_load_dwordx4 v242, s[36:39], s54 offen offset:128 sc0 nt lds\n"
"v_mfma_f32_16x16x32_bf16 v[52:55], a[152:155], v[196:199], v[52:55]\n"
"v_exp_f32 v175, v175\n"
"v_mfma_f32_16x16x32_bf16 v[100:103], a[152:155], v[200:203], v[100:103]\n"
"v_mul_f32 v176, s55, v176\n"
"v_mul_f32 v177, s55, v177\n"
"v_mfma_f32_16x16x32_bf16 v[100:103], a[156:159], v[212:215], v[100:103]\n"
"s_waitcnt lgkmcnt(4)\n"
"v_mul_f32 v178, s55, v178\n"
"v_mul_f32 v179, s55, v179\n"
"v_mfma_f32_16x16x32_bf16 v[8:11], a[160:163], v[192:195], v[8:11]\n"
"ds_read_b64_tr_b16 a[152:153], v241 offset:28672\n"
"ds_read_b64_tr_b16 a[154:155], v241 offset:28928\n"
"v_mfma_f32_16x16x32_bf16 v[8:11], a[164:167], v[204:207], v[8:11]\n"
"s_add_u32 m0, s52, 11008\n"
"ds_read_b64_tr_b16 a[156:157], v241 offset:31744\n"
"ds_read_b64_tr_b16 a[158:159], v241 offset:32000\n"
"v_mfma_f32_16x16x32_bf16 v[56:59], a[164:167], v[208:211], v[56:59]\n"
"buffer_load_dwordx4 v242, s[36:39], s54 offen offset:256 sc0 nt lds\n"
"v_mfma_f32_16x16x32_bf16 v[56:59], a[160:163], v[196:199], v[56:59]\n"
"v_exp_f32 v176, v176\n"
"v_mfma_f32_16x16x32_bf16 v[104:107], a[160:163], v[200:203], v[104:107]\n"
"v_exp_f32 v177, v177\n"
"v_mfma_f32_16x16x32_bf16 v[104:107], a[164:167], v[212:215], v[104:107]\n"
"s_waitcnt lgkmcnt(4)\n"
"v_exp_f32 v178, v178\n"
"v_mfma_f32_16x16x32_bf16 v[12:15], a[144:147], v[192:195], v[12:15]\n"
"s_mov_b32 s59, 147456\n"
"ds_read_b64_tr_b16 a[160:161], v241 offset:28704\n"
"ds_read_b64_tr_b16 a[162:163], v241 offset:28960\n"
"v_mfma_f32_16x16x32_bf16 v[12:15], a[148:151], v[204:207], v[12:15]\n"
"s_add_u32 m0, s52, 21504\n"
"ds_read_b64_tr_b16 a[164:165], v241 offset:31776\n"
"ds_read_b64_tr_b16 a[166:167], v241 offset:32032\n"
"v_mfma_f32_16x16x32_bf16 v[60:63], a[148:151], v[208:211], v[60:63]\n"
"buffer_load_dwordx4 v242, s[40:43], s59 offen offset:0 sc0 nt lds\n"
"v_mfma_f32_16x16x32_bf16 v[60:63], a[144:147], v[196:199], v[60:63]\n"
"v_exp_f32 v179, v179\n"
"v_mfma_f32_16x16x32_bf16 v[108:111], a[144:147], v[200:203], v[108:111]\n"
"v_mul_f32 v180, s55, v180\n"
"v_mul_f32 v181, s55, v181\n"
"v_mfma_f32_16x16x32_bf16 v[108:111], a[148:151], v[212:215], v[108:111]\n"
"s_waitcnt lgkmcnt(4)\n"
"v_mul_f32 v182, s55, v182\n"
"v_mul_f32 v183, s55, v183\n"
"v_mfma_f32_16x16x32_bf16 v[16:19], a[152:155], v[192:195], v[16:19]\n"
"ds_read_b64_tr_b16 a[144:145], v241 offset:29184\n"
"ds_read_b64_tr_b16 a[146:147], v241 offset:29440\n"
"v_mfma_f32_16x16x32_bf16 v[16:19], a[156:159], v[204:207], v[16:19]\n"
"s_add_u32 m0, s52, 22400\n"
"ds_read_b64_tr_b16 a[148:149], v241 offset:32256\n"
"ds_read_b64_tr_b16 a[150:151], v241 offset:32512\n"
"v_mfma_f32_16x16x32_bf16 v[64:67], a[156:159], v[208:211], v[64:67]\n"
"buffer_load_dwordx4 v242, s[40:43], s59 offen offset:128 sc0 nt lds\n"
"v_mfma_f32_16x16x32_bf16 v[64:67], a[152:155], v[196:199], v[64:67]\n"
"v_exp_f32 v180, v180\n"
"v_mfma_f32_16x16x32_bf16 v[112:115], a[152:155], v[200:203], v[112:115]\n"
"v_exp_f32 v181, v181\n"
"v_mfma_f32_16x16x32_bf16 v[112:115], a[156:159], v[212:215], v[112:115]\n"
"s_waitcnt lgkmcnt(4)\n"
"v_exp_f32 v182, v182\n"
"v_mfma_f32_16x16x32_bf16 v[20:23], a[160:163], v[192:195], v[20:23]\n"
"ds_read_b64_tr_b16 a[152:153], v241 offset:29216\n"
"ds_read_b64_tr_b16 a[154:155], v241 offset:29472\n"
"v_mfma_f32_16x16x32_bf16 v[20:23], a[164:167], v[204:207], v[20:23]\n"
"s_add_u32 m0, s52, 23296\n"
"ds_read_b64_tr_b16 a[156:157], v241 offset:32288\n"
"ds_read_b64_tr_b16 a[158:159], v241 offset:32544\n"
"v_mfma_f32_16x16x32_bf16 v[68:71], a[164:167], v[208:211], v[68:71]\n"
"buffer_load_dwordx4 v242, s[40:43], s59 offen offset:256 sc0 nt lds\n"
"v_mfma_f32_16x16x32_bf16 v[68:71], a[160:163], v[196:199], v[68:71]\n"
"v_exp_f32 v183, v183\n"
"v_mfma_f32_16x16x32_bf16 v[116:119], a[160:163], v[200:203], v[116:119]\n"
"v_mul_f32 v184, s55, v184\n"
"v_mul_f32 v185, s55, v185\n"
"v_mfma_f32_16x16x32_bf16 v[116:119], a[164:167], v[212:215], v[116:119]\n"
"s_waitcnt lgkmcnt(4)\n"
"v_mul_f32 v186, s55, v186\n"
"v_mul_f32 v187, s55, v187\n"
"v_mfma_f32_16x16x32_bf16 v[24:27], a[144:147], v[192:195], v[24:27]\n"
"s_mov_b32 s70, 159744\n"
"ds_read_b64_tr_b16 a[160:161], v241 offset:29696\n"
"ds_read_b64_tr_b16 a[162:163], v241 offset:29952\n"
"v_mfma_f32_16x16x32_bf16 v[24:27], a[148:151], v[204:207], v[24:27]\n"
"s_add_u32 m0, s52, 24576\n"
"ds_read_b64_tr_b16 a[164:165], v241 offset:32768\n"
"ds_read_b64_tr_b16 a[166:167], v241 offset:33024\n"
"v_mfma_f32_16x16x32_bf16 v[72:75], a[148:151], v[208:211], v[72:75]\n"
"buffer_load_dwordx4 v242, s[40:43], s70 offen offset:0 sc0 nt lds\n"
"v_mfma_f32_16x16x32_bf16 v[72:75], a[144:147], v[196:199], v[72:75]\n"
"v_exp_f32 v184, v184\n"
"v_mfma_f32_16x16x32_bf16 v[120:123], a[144:147], v[200:203], v[120:123]\n"
"v_exp_f32 v185, v185\n"
"v_mfma_f32_16x16x32_bf16 v[120:123], a[148:151], v[212:215], v[120:123]\n"
"s_waitcnt lgkmcnt(4)\n"
"v_exp_f32 v186, v186\n"
"v_mfma_f32_16x16x32_bf16 v[28:31], a[152:155], v[192:195], v[28:31]\n"
"ds_read_b64_tr_b16 a[144:145], v241 offset:29728\n"
"ds_read_b64_tr_b16 a[146:147], v241 offset:29984\n"
"v_mfma_f32_16x16x32_bf16 v[28:31], a[156:159], v[204:207], v[28:31]\n"
"s_add_u32 m0, s52, 25472\n"
"ds_read_b64_tr_b16 a[148:149], v241 offset:32800\n"
"ds_read_b64_tr_b16 a[150:151], v241 offset:33056\n"
"v_mfma_f32_16x16x32_bf16 v[76:79], a[156:159], v[208:211], v[76:79]\n"
"buffer_load_dwordx4 v242, s[40:43], s70 offen offset:128 sc0 nt lds\n"
"v_mfma_f32_16x16x32_bf16 v[76:79], a[152:155], v[196:199], v[76:79]\n"
"v_exp_f32 v187, v187\n"
"v_mfma_f32_16x16x32_bf16 v[124:127], a[152:155], v[200:203], v[124:127]\n"
"v_mul_f32 v188, s55, v188\n"
"v_mul_f32 v189, s55, v189\n"
"v_mfma_f32_16x16x32_bf16 v[124:127], a[156:159], v[212:215], v[124:127]\n"
"s_waitcnt lgkmcnt(4)\n"
"v_mul_f32 v190, s55, v190\n"
"v_mul_f32 v191, s55, v191\n"
"v_mfma_f32_16x16x32_bf16 v[32:35], a[160:163], v[192:195], v[32:35]\n"
"ds_read_b64_tr_b16 a[152:153], v241 offset:30208\n"
"ds_read_b64_tr_b16 a[154:155], v241 offset:30464\n"
"v_mfma_f32_16x16x32_bf16 v[32:35], a[164:167], v[204:207], v[32:35]\n"
"s_add_u32 m0, s52, 26368\n"
"ds_read_b64_tr_b16 a[156:157], v241 offset:33280\n"
"ds_read_b64_tr_b16 a[158:159], v241 offset:33536\n"
"v_mfma_f32_16x16x32_bf16 v[80:83], a[164:167], v[208:211], v[80:83]\n"
"buffer_load_dwordx4 v242, s[40:43], s70 offen offset:256 sc0 nt lds\n"
"v_mfma_f32_16x16x32_bf16 v[80:83], a[160:163], v[196:199], v[80:83]\n"
"v_exp_f32 v188, v188\n"
"v_mfma_f32_16x16x32_bf16 v[128:131], a[160:163], v[200:203], v[128:131]\n"
"v_exp_f32 v189, v189\n"
"v_mfma_f32_16x16x32_bf16 v[128:131], a[164:167], v[212:215], v[128:131]\n"
"s_waitcnt lgkmcnt(4)\n"
"v_exp_f32 v190, v190\n"
"v_mfma_f32_16x16x32_bf16 v[36:39], a[144:147], v[192:195], v[36:39]\n"
"ds_read_b64_tr_b16 a[160:161], v241 offset:30240\n"
"ds_read_b64_tr_b16 a[162:163], v241 offset:30496\n"
"v_mfma_f32_16x16x32_bf16 v[36:39], a[148:151], v[204:207], v[36:39]\n"
"ds_read_b64_tr_b16 a[164:165], v241 offset:33312\n"
"ds_read_b64_tr_b16 a[166:167], v241 offset:33568\n"
"v_mfma_f32_16x16x32_bf16 v[84:87], a[148:151], v[208:211], v[84:87]\n"
"buffer_load_dwordx4 a[136:139], v220, s[44:47], s65 offen offset:256 sc0 nt\n"
"v_mfma_f32_16x16x32_bf16 v[84:87], a[144:147], v[196:199], v[84:87]\n"
"v_exp_f32 v191, v191\n"
"v_mfma_f32_16x16x32_bf16 v[132:135], a[144:147], v[200:203], v[132:135]\n"
"v_mfma_f32_16x16x32_bf16 v[132:135], a[148:151], v[212:215], v[132:135]\n"
"s_waitcnt lgkmcnt(4)\n"
"v_mfma_f32_16x16x32_bf16 v[40:43], a[152:155], v[192:195], v[40:43]\n"
"ds_read_b128 a[144:147], v240 offset:15360\n"
"ds_read_b128 a[148:151], v240 offset:15872\n"
"v_mfma_f32_16x16x32_bf16 v[40:43], a[156:159], v[204:207], v[40:43]\n"
"v_mfma_f32_16x16x32_bf16 v[88:91], a[156:159], v[208:211], v[88:91]\n"
"buffer_load_dwordx4 a[140:143], v220, s[44:47], s65 offen offset:3328 sc0 nt\n"
"v_mfma_f32_16x16x32_bf16 v[88:91], a[152:155], v[196:199], v[88:91]\n"
"v_mfma_f32_16x16x32_bf16 v[136:139], a[152:155], v[200:203], v[136:139]\n"
"v_mfma_f32_16x16x32_bf16 v[136:139], a[156:159], v[212:215], v[136:139]\n"
"s_waitcnt lgkmcnt(2)\n"
"v_mfma_f32_16x16x32_bf16 v[44:47], a[160:163], v[192:195], v[44:47]\n"
"ds_read_b128 a[152:155], v240 offset:16384\n"
"ds_read_b128 a[156:159], v240 offset:16896\n"
"v_mfma_f32_16x16x32_bf16 v[44:47], a[164:167], v[204:207], v[44:47]\n"
"v_cvt_pk_bf16_f32 v192, v144, v145\n"
"v_cvt_pk_bf16_f32 v193, v146, v147\n"
"v_mfma_f32_16x16x32_bf16 v[92:95], a[164:167], v[208:211], v[92:95]\n"
"v_cvt_pk_bf16_f32 v194, v156, v157\n"
"v_cvt_pk_bf16_f32 v195, v158, v159\n"
"v_mfma_f32_16x16x32_bf16 v[92:95], a[160:163], v[196:199], v[92:95]\n"
"v_cvt_pk_bf16_f32 v196, v148, v149\n"
"v_cvt_pk_bf16_f32 v197, v150, v151\n"
"v_mfma_f32_16x16x32_bf16 v[140:143], a[160:163], v[200:203], v[140:143]\n"
"v_cvt_pk_bf16_f32 v200, v152, v153\n"
"v_cvt_pk_bf16_f32 v201, v154, v155\n"
"v_mfma_f32_16x16x32_bf16 v[140:143], a[164:167], v[212:215], v[140:143]\n"
"s_waitcnt lgkmcnt(2)\n"
"v_cvt_pk_bf16_f32 v198, v160, v161\n"
"v_cvt_pk_bf16_f32 v199, v162, v163\n"
"v_mfma_f32_16x16x32_bf16 v[144:147], a[144:147], a[0:3], 0\n"
"ds_read_b128 a[160:163], v240 offset:17408\n"
"ds_read_b128 a[164:167], v240 offset:17920\n"
"v_mfma_f32_16x16x32_bf16 v[144:147], a[148:151], a[4:7], v[144:147]\n"
"v_cvt_pk_bf16_f32 v202, v164, v165\n"
"v_cvt_pk_bf16_f32 v203, v166, v167\n"
"v_mfma_f32_16x16x32_bf16 v[148:151], a[148:151], a[28:31], 0\n"
"v_cvt_pk_bf16_f32 v204, v168, v169\n"
"v_cvt_pk_bf16_f32 v205, v170, v171\n"
"v_mfma_f32_16x16x32_bf16 v[148:151], a[144:147], a[24:27], v[148:151]\n"
"v_cvt_pk_bf16_f32 v208, v172, v173\n"
"v_cvt_pk_bf16_f32 v209, v174, v175\n"
"v_mfma_f32_16x16x32_bf16 v[152:155], a[144:147], a[48:51], 0\n"
"v_cvt_pk_bf16_f32 v212, v176, v177\n"
"v_cvt_pk_bf16_f32 v213, v178, v179\n"
"v_mfma_f32_16x16x32_bf16 v[152:155], a[148:151], a[52:55], v[152:155]\n"
"s_waitcnt lgkmcnt(2)\n"
"v_cvt_pk_bf16_f32 v206, v180, v181\n"
"v_cvt_pk_bf16_f32 v207, v182, v183\n"
"v_mfma_f32_16x16x32_bf16 v[144:147], a[152:155], a[8:11], v[144:147]\n"
"ds_read_b128 a[144:147], v240 offset:15616\n"
"ds_read_b128 a[148:151], v240 offset:16128\n"
"v_mfma_f32_16x16x32_bf16 v[144:147], a[156:159], a[12:15], v[144:147]\n"
"v_cvt_pk_bf16_f32 v210, v184, v185\n"
"v_cvt_pk_bf16_f32 v211, v186, v187\n"
"v_mfma_f32_16x16x32_bf16 v[148:151], a[156:159], a[36:39], v[148:151]\n"
"v_cvt_pk_bf16_f32 v214, v188, v189\n"
"v_cvt_pk_bf16_f32 v215, v190, v191\n"
"v_mfma_f32_16x16x32_bf16 v[148:151], a[152:155], a[32:35], v[148:151]\n"
"v_mfma_f32_16x16x32_bf16 v[152:155], a[152:155], a[56:59], v[152:155]\n"
"v_mfma_f32_16x16x32_bf16 v[152:155], a[156:159], a[60:63], v[152:155]\n"
"s_waitcnt lgkmcnt(2)\n"
"v_mfma_f32_16x16x32_bf16 v[144:147], a[160:163], a[16:19], v[144:147]\n"
"ds_read_b128 a[152:155], v240 offset:16640\n"
"ds_read_b128 a[156:159], v240 offset:17152\n"
"v_mfma_f32_16x16x32_bf16 v[144:147], a[164:167], a[20:23], v[144:147]\n"
"v_mfma_f32_16x16x32_bf16 v[148:151], a[164:167], a[44:47], v[148:151]\n"
"v_mfma_f32_16x16x32_bf16 v[148:151], a[160:163], a[40:43], v[148:151]\n"
"v_mfma_f32_16x16x32_bf16 v[152:155], a[160:163], a[64:67], v[152:155]\n"
"v_mfma_f32_16x16x32_bf16 v[152:155], a[164:167], a[68:71], v[152:155]\n"
"s_waitcnt lgkmcnt(2)\n"
"v_mfma_f32_16x16x32_bf16 v[156:159], a[144:147], a[0:3], 0\n"
"ds_read_b128 a[160:163], v240 offset:17664\n"
"ds_read_b128 a[164:167], v240 offset:18176\n"
"v_mfma_f32_16x16x32_bf16 v[156:159], a[148:151], a[4:7], v[156:159]\n"
"v_mfma_f32_16x16x32_bf16 v[160:163], a[148:151], a[28:31], 0\n"
"v_mfma_f32_16x16x32_bf16 v[160:163], a[144:147], a[24:27], v[160:163]\n"
"v_mul_f32 v144, s55, v144\n"
"v_mul_f32 v145, s55, v145\n"
"v_mfma_f32_16x16x32_bf16 v[164:167], a[144:147], a[48:51], 0\n"
"v_mul_f32 v146, s55, v146\n"
"v_mul_f32 v147, s55, v147\n"
"v_mfma_f32_16x16x32_bf16 v[164:167], a[148:151], a[52:55], v[164:167]\n"
"s_waitcnt lgkmcnt(2)\n"
"v_exp_f32 v144, v144\n"
"v_mfma_f32_16x16x32_bf16 v[156:159], a[152:155], a[8:11], v[156:159]\n"
"ds_read_b128 a[144:147], v240 offset:18432\n"
"ds_read_b128 a[148:151], v240 offset:18944\n"
"v_mfma_f32_16x16x32_bf16 v[156:159], a[156:159], a[12:15], v[156:159]\n"
"v_exp_f32 v145, v145\n"
"v_mfma_f32_16x16x32_bf16 v[160:163], a[156:159], a[36:39], v[160:163]\n"
"v_exp_f32 v146, v146\n"
"v_mfma_f32_16x16x32_bf16 v[160:163], a[152:155], a[32:35], v[160:163]\n"
"v_exp_f32 v147, v147\n"
"v_mfma_f32_16x16x32_bf16 v[164:167], a[152:155], a[56:59], v[164:167]\n"
"v_mul_f32 v148, s55, v148\n"
"v_mul_f32 v149, s55, v149\n"
"v_mfma_f32_16x16x32_bf16 v[164:167], a[156:159], a[60:63], v[164:167]\n"
"s_waitcnt lgkmcnt(2)\n"
"v_mul_f32 v150, s55, v150\n"
"v_mul_f32 v151, s55, v151\n"
"v_mfma_f32_16x16x32_bf16 v[156:159], a[160:163], a[16:19], v[156:159]\n"
"ds_read_b128 a[152:155], v240 offset:19456\n"
"ds_read_b128 a[156:159], v240 offset:19968\n"
"v_mfma_f32_16x16x32_bf16 v[156:159], a[164:167], a[20:23], v[156:159]\n"
"v_exp_f32 v148, v148\n"
"v_mfma_f32_16x16x32_bf16 v[160:163], a[164:167], a[44:47], v[160:163]\n"
"v_exp_f32 v149, v149\n"
"v_mfma_f32_16x16x32_bf16 v[160:163], a[160:163], a[40:43], v[160:163]\n"
"v_exp_f32 v150, v150\n"
"v_mfma_f32_16x16x32_bf16 v[164:167], a[160:163], a[64:67], v[164:167]\n"
"v_exp_f32 v151, v151\n"
"v_mfma_f32_16x16x32_bf16 v[164:167], a[164:167], a[68:71], v[164:167]\n"
"s_waitcnt lgkmcnt(2)\n"
"v_mul_f32 v152, s55, v152\n"
"v_mul_f32 v153, s55, v153\n"
"v_mfma_f32_16x16x32_bf16 v[168:171], a[144:147], a[0:3], 0\n"
"ds_read_b128 a[160:163], v240 offset:20480\n"
"ds_read_b128 a[164:167], v240 offset:20992\n"
"v_mfma_f32_16x16x32_bf16 v[168:171], a[148:151], a[4:7], v[168:171]\n"
"v_mul_f32 v154, s55, v154\n"
"v_mul_f32 v155, s55, v155\n"
"v_mfma_f32_16x16x32_bf16 v[172:175], a[148:151], a[28:31], 0\n"
"v_exp_f32 v152, v152\n"
"v_mfma_f32_16x16x32_bf16 v[172:175], a[144:147], a[24:27], v[172:175]\n"
"v_exp_f32 v153, v153\n"
"v_mfma_f32_16x16x32_bf16 v[176:179], a[144:147], a[48:51], 0\n"
"v_exp_f32 v154, v154\n"
"v_mfma_f32_16x16x32_bf16 v[176:179], a[148:151], a[52:55], v[176:179]\n"
"s_waitcnt lgkmcnt(2)\n"
"v_exp_f32 v155, v155\n"
"v_mfma_f32_16x16x32_bf16 v[168:171], a[152:155], a[8:11], v[168:171]\n"
"ds_read_b128 a[144:147], v240 offset:18688\n"
"ds_read_b128 a[148:151], v240 offset:19200\n"
"v_mfma_f32_16x16x32_bf16 v[168:171], a[156:159], a[12:15], v[168:171]\n"
"v_mul_f32 v156, s55, v156\n"
"v_mul_f32 v157, s55, v157\n"
"v_mfma_f32_16x16x32_bf16 v[172:175], a[156:159], a[36:39], v[172:175]\n"
"v_mul_f32 v158, s55, v158\n"
"v_mul_f32 v159, s55, v159\n"
"v_mfma_f32_16x16x32_bf16 v[172:175], a[152:155], a[32:35], v[172:175]\n"
"v_exp_f32 v156, v156\n"
"v_mfma_f32_16x16x32_bf16 v[176:179], a[152:155], a[56:59], v[176:179]\n"
"v_exp_f32 v157, v157\n"
"v_mfma_f32_16x16x32_bf16 v[176:179], a[156:159], a[60:63], v[176:179]\n"
"s_waitcnt lgkmcnt(2)\n"
"v_exp_f32 v158, v158\n"
"v_mfma_f32_16x16x32_bf16 v[168:171], a[160:163], a[16:19], v[168:171]\n"
"ds_read_b128 a[152:155], v240 offset:19712\n"
"ds_read_b128 a[156:159], v240 offset:20224\n"
"v_mfma_f32_16x16x32_bf16 v[168:171], a[164:167], a[20:23], v[168:171]\n"
"v_exp_f32 v159, v159\n"
"v_mfma_f32_16x16x32_bf16 v[172:175], a[164:167], a[44:47], v[172:175]\n"
"v_mul_f32 v160, s55, v160\n"
"v_mul_f32 v161, s55, v161\n"
"v_mfma_f32_16x16x32_bf16 v[172:175], a[160:163], a[40:43], v[172:175]\n"
"v_mul_f32 v162, s55, v162\n"
"v_mul_f32 v163, s55, v163\n"
"v_mfma_f32_16x16x32_bf16 v[176:179], a[160:163], a[64:67], v[176:179]\n"
"v_exp_f32 v160, v160\n"
"v_mfma_f32_16x16x32_bf16 v[176:179], a[164:167], a[68:71], v[176:179]\n"
"s_waitcnt lgkmcnt(2)\n"
"v_exp_f32 v161, v161\n"
"v_mfma_f32_16x16x32_bf16 v[180:183], a[144:147], a[0:3], 0\n"
"ds_read_b128 a[160:163], v240 offset:20736\n"
"ds_read_b128 a[164:167], v240 offset:21248\n"
"v_mfma_f32_16x16x32_bf16 v[180:183], a[148:151], a[4:7], v[180:183]\n"
"v_exp_f32 v162, v162\n"
"v_mfma_f32_16x16x32_bf16 v[184:187], a[148:151], a[28:31], 0\n"
"v_exp_f32 v163, v163\n"
"v_mfma_f32_16x16x32_bf16 v[184:187], a[144:147], a[24:27], v[184:187]\n"
"s_waitcnt vmcnt(16)\n"
"v_mul_f32 v164, s55, v164\n"
"v_mul_f32 v165, s55, v165\n"
"v_mfma_f32_16x16x32_bf16 v[188:191], a[144:147], a[48:51], 0\n"
"s_barrier\n"
"v_mul_f32 v166, s55, v166\n"
"v_mul_f32 v167, s55, v167\n"
"v_mfma_f32_16x16x32_bf16 v[188:191], a[148:151], a[52:55], v[188:191]\n"
"s_waitcnt lgkmcnt(2)\n"
"v_exp_f32 v164, v164\n"
"v_mfma_f32_16x16x32_bf16 v[180:183], a[152:155], a[8:11], v[180:183]\n"
"s_mov_b32 s53, 221184\n"
"ds_read_b64_tr_b16 a[144:145], v241 offset:33792\n"
"ds_read_b64_tr_b16 a[146:147], v241 offset:34048\n"
"v_mfma_f32_16x16x32_bf16 v[180:183], a[156:159], a[12:15], v[180:183]\n"
"s_add_u32 m0, s52, 12288\n"
"ds_read_b64_tr_b16 a[148:149], v241 offset:36864\n"
"ds_read_b64_tr_b16 a[150:151], v241 offset:37120\n"
"v_mfma_f32_16x16x32_bf16 v[184:187], a[156:159], a[36:39], v[184:187]\n"
"buffer_load_dwordx4 v242, s[36:39], s53 offen offset:0 sc0 nt lds\n"
"v_mfma_f32_16x16x32_bf16 v[184:187], a[152:155], a[32:35], v[184:187]\n"
"v_exp_f32 v165, v165\n"
"v_mfma_f32_16x16x32_bf16 v[188:191], a[152:155], a[56:59], v[188:191]\n"
"v_exp_f32 v166, v166\n"
"v_mfma_f32_16x16x32_bf16 v[188:191], a[156:159], a[60:63], v[188:191]\n"
"s_waitcnt lgkmcnt(4)\n"
"v_exp_f32 v167, v167\n"
"v_mfma_f32_16x16x32_bf16 v[180:183], a[160:163], a[16:19], v[180:183]\n"
"ds_read_b64_tr_b16 a[152:153], v241 offset:33824\n"
"ds_read_b64_tr_b16 a[154:155], v241 offset:34080\n"
"v_mfma_f32_16x16x32_bf16 v[180:183], a[164:167], a[20:23], v[180:183]\n"
"s_add_u32 m0, s52, 13184\n"
"ds_read_b64_tr_b16 a[156:157], v241 offset:36896\n"
"ds_read_b64_tr_b16 a[158:159], v241 offset:37152\n"
"v_mfma_f32_16x16x32_bf16 v[184:187], a[164:167], a[44:47], v[184:187]\n"
"buffer_load_dwordx4 v242, s[36:39], s53 offen offset:128 sc0 nt lds\n"
"v_mfma_f32_16x16x32_bf16 v[184:187], a[160:163], a[40:43], v[184:187]\n"
"v_mul_f32 v168, s55, v168\n"
"v_mul_f32 v169, s55, v169\n"
"v_mfma_f32_16x16x32_bf16 v[188:191], a[160:163], a[64:67], v[188:191]\n"
"v_mul_f32 v170, s55, v170\n"
"v_mul_f32 v171, s55, v171\n"
"v_mfma_f32_16x16x32_bf16 v[188:191], a[164:167], a[68:71], v[188:191]\n"
"v_exp_f32 v168, v168\n"
"v_mfma_f32_16x16x32_bf16 a[240:243], a[252:255], v[192:195], a[240:243]\n"
"v_exp_f32 v169, v169\n"
"v_mfma_f32_16x16x32_bf16 a[240:243], a[252:255], v[204:207], a[240:243]\n"
"s_add_u32 m0, s52, 14080\n"
"v_exp_f32 v170, v170\n"
"v_mfma_f32_16x16x32_bf16 a[244:247], a[252:255], v[196:199], a[244:247]\n"
"buffer_load_dwordx4 v242, s[36:39], s53 offen offset:256 sc0 nt lds\n"
"v_mfma_f32_16x16x32_bf16 a[244:247], a[252:255], v[208:211], a[244:247]\n"
"v_exp_f32 v171, v171\n"
"v_mfma_f32_16x16x32_bf16 a[248:251], a[252:255], v[200:203], a[248:251]\n"
"v_mul_f32 v172, s55, v172\n"
"v_mul_f32 v173, s55, v173\n"
"v_mfma_f32_16x16x32_bf16 a[248:251], a[252:255], v[212:215], a[248:251]\n"
"s_waitcnt lgkmcnt(4)\n"
"v_mul_f32 v174, s55, v174\n"
"v_mul_f32 v175, s55, v175\n"
"v_mfma_f32_16x16x32_bf16 v[0:3], a[144:147], v[192:195], v[0:3]\n"
"s_mov_b32 s54, 233472\n"
"ds_read_b64_tr_b16 a[160:161], v241 offset:34304\n"
"ds_read_b64_tr_b16 a[162:163], v241 offset:34560\n"
"v_mfma_f32_16x16x32_bf16 v[0:3], a[148:151], v[204:207], v[0:3]\n"
"s_add_u32 m0, s52, 15360\n"
"ds_read_b64_tr_b16 a[164:165], v241 offset:37376\n"
"ds_read_b64_tr_b16 a[166:167], v241 offset:37632\n"
"v_mfma_f32_16x16x32_bf16 v[48:51], a[148:151], v[208:211], v[48:51]\n"
"buffer_load_dwordx4 v242, s[36:39], s54 offen offset:0 sc0 nt lds\n"
"v_mfma_f32_16x16x32_bf16 v[48:51], a[144:147], v[196:199], v[48:51]\n"
"v_exp_f32 v172, v172\n"
"v_mfma_f32_16x16x32_bf16 v[96:99], a[144:147], v[200:203], v[96:99]\n"
"v_exp_f32 v173, v173\n"
"v_mfma_f32_16x16x32_bf16 v[96:99], a[148:151], v[212:215], v[96:99]\n"
"s_waitcnt lgkmcnt(4)\n"
"v_exp_f32 v174, v174\n"
"v_mfma_f32_16x16x32_bf16 v[4:7], a[152:155], v[192:195], v[4:7]\n"
"ds_read_b64_tr_b16 a[144:145], v241 offset:34336\n"
"ds_read_b64_tr_b16 a[146:147], v241 offset:34592\n"
"v_mfma_f32_16x16x32_bf16 v[4:7], a[156:159], v[204:207], v[4:7]\n"
"s_add_u32 m0, s52, 16256\n"
"ds_read_b64_tr_b16 a[148:149], v241 offset:37408\n"
"ds_read_b64_tr_b16 a[150:151], v241 offset:37664\n"
"v_mfma_f32_16x16x32_bf16 v[52:55], a[156:159], v[208:211], v[52:55]\n"
"buffer_load_dwordx4 v242, s[36:39], s54 offen offset:128 sc0 nt lds\n"
"v_mfma_f32_16x16x32_bf16 v[52:55], a[152:155], v[196:199], v[52:55]\n"
"v_exp_f32 v175, v175\n"
"v_mfma_f32_16x16x32_bf16 v[100:103], a[152:155], v[200:203], v[100:103]\n"
"v_mul_f32 v176, s55, v176\n"
"v_mul_f32 v177, s55, v177\n"
"v_mfma_f32_16x16x32_bf16 v[100:103], a[156:159], v[212:215], v[100:103]\n"
"s_waitcnt lgkmcnt(4)\n"
"v_mul_f32 v178, s55, v178\n"
"v_mul_f32 v179, s55, v179\n"
"v_mfma_f32_16x16x32_bf16 v[8:11], a[160:163], v[192:195], v[8:11]\n"
"ds_read_b64_tr_b16 a[152:153], v241 offset:34816\n"
"ds_read_b64_tr_b16 a[154:155], v241 offset:35072\n"
"v_mfma_f32_16x16x32_bf16 v[8:11], a[164:167], v[204:207], v[8:11]\n"
"s_add_u32 m0, s52, 17152\n"
"ds_read_b64_tr_b16 a[156:157], v241 offset:37888\n"
"ds_read_b64_tr_b16 a[158:159], v241 offset:38144\n"
"v_mfma_f32_16x16x32_bf16 v[56:59], a[164:167], v[208:211], v[56:59]\n"
"buffer_load_dwordx4 v242, s[36:39], s54 offen offset:256 sc0 nt lds\n"
"v_mfma_f32_16x16x32_bf16 v[56:59], a[160:163], v[196:199], v[56:59]\n"
"v_exp_f32 v176, v176\n"
"v_mfma_f32_16x16x32_bf16 v[104:107], a[160:163], v[200:203], v[104:107]\n"
"v_exp_f32 v177, v177\n"
"v_mfma_f32_16x16x32_bf16 v[104:107], a[164:167], v[212:215], v[104:107]\n"
"s_waitcnt lgkmcnt(4)\n"
"v_exp_f32 v178, v178\n"
"v_mfma_f32_16x16x32_bf16 v[12:15], a[144:147], v[192:195], v[12:15]\n"
"s_mov_b32 s59, 172032\n"
"ds_read_b64_tr_b16 a[160:161], v241 offset:34848\n"
"ds_read_b64_tr_b16 a[162:163], v241 offset:35104\n"
"v_mfma_f32_16x16x32_bf16 v[12:15], a[148:151], v[204:207], v[12:15]\n"
"s_add_u32 m0, s52, 27648\n"
"ds_read_b64_tr_b16 a[164:165], v241 offset:37920\n"
"ds_read_b64_tr_b16 a[166:167], v241 offset:38176\n"
"v_mfma_f32_16x16x32_bf16 v[60:63], a[148:151], v[208:211], v[60:63]\n"
"buffer_load_dwordx4 v242, s[40:43], s59 offen offset:0 sc0 nt lds\n"
"v_mfma_f32_16x16x32_bf16 v[60:63], a[144:147], v[196:199], v[60:63]\n"
"v_exp_f32 v179, v179\n"
"v_mfma_f32_16x16x32_bf16 v[108:111], a[144:147], v[200:203], v[108:111]\n"
"v_mul_f32 v180, s55, v180\n"
"v_mul_f32 v181, s55, v181\n"
"v_mfma_f32_16x16x32_bf16 v[108:111], a[148:151], v[212:215], v[108:111]\n"
"s_waitcnt lgkmcnt(4)\n"
"v_mul_f32 v182, s55, v182\n"
"v_mul_f32 v183, s55, v183\n"
"v_mfma_f32_16x16x32_bf16 v[16:19], a[152:155], v[192:195], v[16:19]\n"
"ds_read_b64_tr_b16 a[144:145], v241 offset:35328\n"
"ds_read_b64_tr_b16 a[146:147], v241 offset:35584\n"
"v_mfma_f32_16x16x32_bf16 v[16:19], a[156:159], v[204:207], v[16:19]\n"
"s_add_u32 m0, s52, 28544\n"
"ds_read_b64_tr_b16 a[148:149], v241 offset:38400\n"
"ds_read_b64_tr_b16 a[150:151], v241 offset:38656\n"
"v_mfma_f32_16x16x32_bf16 v[64:67], a[156:159], v[208:211], v[64:67]\n"
"buffer_load_dwordx4 v242, s[40:43], s59 offen offset:128 sc0 nt lds\n"
"v_mfma_f32_16x16x32_bf16 v[64:67], a[152:155], v[196:199], v[64:67]\n"
"v_exp_f32 v180, v180\n"
"v_mfma_f32_16x16x32_bf16 v[112:115], a[152:155], v[200:203], v[112:115]\n"
"v_exp_f32 v181, v181\n"
"v_mfma_f32_16x16x32_bf16 v[112:115], a[156:159], v[212:215], v[112:115]\n"
"s_waitcnt lgkmcnt(4)\n"
"v_exp_f32 v182, v182\n"
"v_mfma_f32_16x16x32_bf16 v[20:23], a[160:163], v[192:195], v[20:23]\n"
"ds_read_b64_tr_b16 a[152:153], v241 offset:35360\n"
"ds_read_b64_tr_b16 a[154:155], v241 offset:35616\n"
"v_mfma_f32_16x16x32_bf16 v[20:23], a[164:167], v[204:207], v[20:23]\n"
"s_add_u32 m0, s52, 29440\n"
"ds_read_b64_tr_b16 a[156:157], v241 offset:38432\n"
"ds_read_b64_tr_b16 a[158:159], v241 offset:38688\n"
"v_mfma_f32_16x16x32_bf16 v[68:71], a[164:167], v[208:211], v[68:71]\n"
"buffer_load_dwordx4 v242, s[40:43], s59 offen offset:256 sc0 nt lds\n"
"v_mfma_f32_16x16x32_bf16 v[68:71], a[160:163], v[196:199], v[68:71]\n"
"v_exp_f32 v183, v183\n"
"v_mfma_f32_16x16x32_bf16 v[116:119], a[160:163], v[200:203], v[116:119]\n"
"v_mul_f32 v184, s55, v184\n"
"v_mul_f32 v185, s55, v185\n"
"v_mfma_f32_16x16x32_bf16 v[116:119], a[164:167], v[212:215], v[116:119]\n"
"s_waitcnt lgkmcnt(4)\n"
"v_mul_f32 v186, s55, v186\n"
"v_mul_f32 v187, s55, v187\n"
"v_mfma_f32_16x16x32_bf16 v[24:27], a[144:147], v[192:195], v[24:27]\n"
"s_mov_b32 s70, 184320\n"
"ds_read_b64_tr_b16 a[160:161], v241 offset:35840\n"
"ds_read_b64_tr_b16 a[162:163], v241 offset:36096\n"
"v_mfma_f32_16x16x32_bf16 v[24:27], a[148:151], v[204:207], v[24:27]\n"
"s_add_u32 m0, s52, 30720\n"
"ds_read_b64_tr_b16 a[164:165], v241 offset:38912\n"
"ds_read_b64_tr_b16 a[166:167], v241 offset:39168\n"
"v_mfma_f32_16x16x32_bf16 v[72:75], a[148:151], v[208:211], v[72:75]\n"
"buffer_load_dwordx4 v242, s[40:43], s70 offen offset:0 sc0 nt lds\n"
"v_mfma_f32_16x16x32_bf16 v[72:75], a[144:147], v[196:199], v[72:75]\n"
"v_exp_f32 v184, v184\n"
"v_mfma_f32_16x16x32_bf16 v[120:123], a[144:147], v[200:203], v[120:123]\n"
"v_exp_f32 v185, v185\n"
"v_mfma_f32_16x16x32_bf16 v[120:123], a[148:151], v[212:215], v[120:123]\n"
"s_waitcnt lgkmcnt(4)\n"
"v_exp_f32 v186, v186\n"
"v_mfma_f32_16x16x32_bf16 v[28:31], a[152:155], v[192:195], v[28:31]\n"
"ds_read_b64_tr_b16 a[144:145], v241 offset:35872\n"
"ds_read_b64_tr_b16 a[146:147], v241 offset:36128\n"
"v_mfma_f32_16x16x32_bf16 v[28:31], a[156:159], v[204:207], v[28:31]\n"
"s_add_u32 m0, s52, 31616\n"
"ds_read_b64_tr_b16 a[148:149], v241 offset:38944\n"
"ds_read_b64_tr_b16 a[150:151], v241 offset:39200\n"
"v_mfma_f32_16x16x32_bf16 v[76:79], a[156:159], v[208:211], v[76:79]\n"
"buffer_load_dwordx4 v242, s[40:43], s70 offen offset:128 sc0 nt lds\n"
"v_mfma_f32_16x16x32_bf16 v[76:79], a[152:155], v[196:199], v[76:79]\n"
"v_exp_f32 v187, v187\n"
"v_mfma_f32_16x16x32_bf16 v[124:127], a[152:155], v[200:203], v[124:127]\n"
"v_mul_f32 v188, s55, v188\n"
"v_mul_f32 v189, s55, v189\n"
"v_mfma_f32_16x16x32_bf16 v[124:127], a[156:159], v[212:215], v[124:127]\n"
"s_waitcnt lgkmcnt(4)\n"
"v_mul_f32 v190, s55, v190\n"
"v_mul_f32 v191, s55, v191\n"
"v_mfma_f32_16x16x32_bf16 v[32:35], a[160:163], v[192:195], v[32:35]\n"
"ds_read_b64_tr_b16 a[152:153], v241 offset:36352\n"
"ds_read_b64_tr_b16 a[154:155], v241 offset:36608\n"
"v_mfma_f32_16x16x32_bf16 v[32:35], a[164:167], v[204:207], v[32:35]\n"
"s_add_u32 m0, s52, 32512\n"
"ds_read_b64_tr_b16 a[156:157], v241 offset:39424\n"
"ds_read_b64_tr_b16 a[158:159], v241 offset:39680\n"
"v_mfma_f32_16x16x32_bf16 v[80:83], a[164:167], v[208:211], v[80:83]\n"
"buffer_load_dwordx4 v242, s[40:43], s70 offen offset:256 sc0 nt lds\n"
"v_mfma_f32_16x16x32_bf16 v[80:83], a[160:163], v[196:199], v[80:83]\n"
"v_exp_f32 v188, v188\n"
"v_mfma_f32_16x16x32_bf16 v[128:131], a[160:163], v[200:203], v[128:131]\n"
"v_exp_f32 v189, v189\n"
"v_mfma_f32_16x16x32_bf16 v[128:131], a[164:167], v[212:215], v[128:131]\n"
"s_waitcnt lgkmcnt(4)\n"
"v_exp_f32 v190, v190\n"
"v_mfma_f32_16x16x32_bf16 v[36:39], a[144:147], v[192:195], v[36:39]\n"
"ds_read_b64_tr_b16 a[160:161], v241 offset:36384\n"
"ds_read_b64_tr_b16 a[162:163], v241 offset:36640\n"
"v_mfma_f32_16x16x32_bf16 v[36:39], a[148:151], v[204:207], v[36:39]\n"
"ds_read_b64_tr_b16 a[164:165], v241 offset:39456\n"
"ds_read_b64_tr_b16 a[166:167], v241 offset:39712\n"
"v_mfma_f32_16x16x32_bf16 v[84:87], a[148:151], v[208:211], v[84:87]\n"
"v_exp_f32 v191, v191\n"
"v_mfma_f32_16x16x32_bf16 v[84:87], a[144:147], v[196:199], v[84:87]\n"
"v_mfma_f32_16x16x32_bf16 v[132:135], a[144:147], v[200:203], v[132:135]\n"
"v_mfma_f32_16x16x32_bf16 v[132:135], a[148:151], v[212:215], v[132:135]\n"
"s_waitcnt lgkmcnt(4)\n"
"v_mfma_f32_16x16x32_bf16 v[40:43], a[152:155], v[192:195], v[40:43]\n"
"ds_read_b128 a[144:147], v240 offset:0\n"
"ds_read_b128 a[148:151], v240 offset:512\n"
"v_mfma_f32_16x16x32_bf16 v[40:43], a[156:159], v[204:207], v[40:43]\n"
"v_mfma_f32_16x16x32_bf16 v[88:91], a[156:159], v[208:211], v[88:91]\n"
"v_mfma_f32_16x16x32_bf16 v[88:91], a[152:155], v[196:199], v[88:91]\n"
"v_mfma_f32_16x16x32_bf16 v[136:139], a[152:155], v[200:203], v[136:139]\n"
"v_mfma_f32_16x16x32_bf16 v[136:139], a[156:159], v[212:215], v[136:139]\n"
"s_waitcnt lgkmcnt(2)\n"
"v_mfma_f32_16x16x32_bf16 v[44:47], a[160:163], v[192:195], v[44:47]\n"
"ds_read_b128 a[152:155], v240 offset:1024\n"
"ds_read_b128 a[156:159], v240 offset:1536\n"
"v_mfma_f32_16x16x32_bf16 v[44:47], a[164:167], v[204:207], v[44:47]\n"
"v_cvt_pk_bf16_f32 v192, v144, v145\n"
"v_cvt_pk_bf16_f32 v193, v146, v147\n"
"v_mfma_f32_16x16x32_bf16 v[92:95], a[164:167], v[208:211], v[92:95]\n"
"v_cvt_pk_bf16_f32 v194, v156, v157\n"
"v_cvt_pk_bf16_f32 v195, v158, v159\n"
"v_mfma_f32_16x16x32_bf16 v[92:95], a[160:163], v[196:199], v[92:95]\n"
"v_cvt_pk_bf16_f32 v196, v148, v149\n"
"v_cvt_pk_bf16_f32 v197, v150, v151\n"
"v_mfma_f32_16x16x32_bf16 v[140:143], a[160:163], v[200:203], v[140:143]\n"
"v_cvt_pk_bf16_f32 v200, v152, v153\n"
"v_cvt_pk_bf16_f32 v201, v154, v155\n"
"v_mfma_f32_16x16x32_bf16 v[140:143], a[164:167], v[212:215], v[140:143]\n"
"s_waitcnt lgkmcnt(2)\n"
"v_cvt_pk_bf16_f32 v198, v160, v161\n"
"v_cvt_pk_bf16_f32 v199, v162, v163\n"
"v_mfma_f32_16x16x32_bf16 v[144:147], a[144:147], a[0:3], 0\n"
"ds_read_b128 a[160:163], v240 offset:2048\n"
"ds_read_b128 a[164:167], v240 offset:2560\n"
"v_mfma_f32_16x16x32_bf16 v[144:147], a[148:151], a[4:7], v[144:147]\n"
"v_cvt_pk_bf16_f32 v202, v164, v165\n"
"v_cvt_pk_bf16_f32 v203, v166, v167\n"
"v_mfma_f32_16x16x32_bf16 v[148:151], a[148:151], a[28:31], 0\n"
"v_cvt_pk_bf16_f32 v204, v168, v169\n"
"v_cvt_pk_bf16_f32 v205, v170, v171\n"
"v_mfma_f32_16x16x32_bf16 v[148:151], a[144:147], a[24:27], v[148:151]\n"
"v_cvt_pk_bf16_f32 v208, v172, v173\n"
"v_cvt_pk_bf16_f32 v209, v174, v175\n"
"v_mfma_f32_16x16x32_bf16 v[152:155], a[144:147], a[48:51], 0\n"
"v_cvt_pk_bf16_f32 v212, v176, v177\n"
"v_cvt_pk_bf16_f32 v213, v178, v179\n"
"v_mfma_f32_16x16x32_bf16 v[152:155], a[148:151], a[52:55], v[152:155]\n"
"s_waitcnt lgkmcnt(2)\n"
"v_cvt_pk_bf16_f32 v206, v180, v181\n"
"v_cvt_pk_bf16_f32 v207, v182, v183\n"
"v_mfma_f32_16x16x32_bf16 v[144:147], a[152:155], a[8:11], v[144:147]\n"
"ds_read_b128 a[144:147], v240 offset:256\n"
"ds_read_b128 a[148:151], v240 offset:768\n"
"v_mfma_f32_16x16x32_bf16 v[144:147], a[156:159], a[12:15], v[144:147]\n"
"v_cvt_pk_bf16_f32 v210, v184, v185\n"
"v_cvt_pk_bf16_f32 v211, v186, v187\n"
"v_mfma_f32_16x16x32_bf16 v[148:151], a[156:159], a[36:39], v[148:151]\n"
"v_cvt_pk_bf16_f32 v214, v188, v189\n"
"v_cvt_pk_bf16_f32 v215, v190, v191\n"
"v_mfma_f32_16x16x32_bf16 v[148:151], a[152:155], a[32:35], v[148:151]\n"
"v_mfma_f32_16x16x32_bf16 v[152:155], a[152:155], a[56:59], v[152:155]\n"
"v_mfma_f32_16x16x32_bf16 v[152:155], a[156:159], a[60:63], v[152:155]\n"
"s_waitcnt lgkmcnt(2)\n"
"v_mfma_f32_16x16x32_bf16 v[144:147], a[160:163], a[16:19], v[144:147]\n"
"ds_read_b128 a[152:155], v240 offset:1280\n"
"ds_read_b128 a[156:159], v240 offset:1792\n"
"v_mfma_f32_16x16x32_bf16 v[144:147], a[164:167], a[20:23], v[144:147]\n"
"v_mfma_f32_16x16x32_bf16 v[148:151], a[164:167], a[44:47], v[148:151]\n"
"v_mfma_f32_16x16x32_bf16 v[148:151], a[160:163], a[40:43], v[148:151]\n"
"v_mfma_f32_16x16x32_bf16 v[152:155], a[160:163], a[64:67], v[152:155]\n"
"v_mfma_f32_16x16x32_bf16 v[152:155], a[164:167], a[68:71], v[152:155]\n"
"s_waitcnt lgkmcnt(2)\n"
"v_mfma_f32_16x16x32_bf16 v[156:159], a[144:147], a[0:3], 0\n"
"ds_read_b128 a[160:163], v240 offset:2304\n"
"ds_read_b128 a[164:167], v240 offset:2816\n"
"v_mfma_f32_16x16x32_bf16 v[156:159], a[148:151], a[4:7], v[156:159]\n"
"v_mfma_f32_16x16x32_bf16 v[160:163], a[148:151], a[28:31], 0\n"
"v_mfma_f32_16x16x32_bf16 v[160:163], a[144:147], a[24:27], v[160:163]\n"
"v_mul_f32 v144, s55, v144\n"
"v_mul_f32 v145, s55, v145\n"
"v_mfma_f32_16x16x32_bf16 v[164:167], a[144:147], a[48:51], 0\n"
"v_mul_f32 v146, s55, v146\n"
"v_mul_f32 v147, s55, v147\n"
"v_mfma_f32_16x16x32_bf16 v[164:167], a[148:151], a[52:55], v[164:167]\n"
"s_waitcnt lgkmcnt(2)\n"
"v_exp_f32 v144, v144\n"
"v_mfma_f32_16x16x32_bf16 v[156:159], a[152:155], a[8:11], v[156:159]\n"
"ds_read_b128 a[144:147], v240 offset:3072\n"
"ds_read_b128 a[148:151], v240 offset:3584\n"
"v_mfma_f32_16x16x32_bf16 v[156:159], a[156:159], a[12:15], v[156:159]\n"
"v_exp_f32 v145, v145\n"
"v_mfma_f32_16x16x32_bf16 v[160:163], a[156:159], a[36:39], v[160:163]\n"
"v_exp_f32 v146, v146\n"
"v_mfma_f32_16x16x32_bf16 v[160:163], a[152:155], a[32:35], v[160:163]\n"
"v_exp_f32 v147, v147\n"
"v_mfma_f32_16x16x32_bf16 v[164:167], a[152:155], a[56:59], v[164:167]\n"
"v_mul_f32 v148, s55, v148\n"
"v_mul_f32 v149, s55, v149\n"
"v_mfma_f32_16x16x32_bf16 v[164:167], a[156:159], a[60:63], v[164:167]\n"
"s_waitcnt lgkmcnt(2)\n"
"v_mul_f32 v150, s55, v150\n"
"v_mul_f32 v151, s55, v151\n"
"v_mfma_f32_16x16x32_bf16 v[156:159], a[160:163], a[16:19], v[156:159]\n"
"ds_read_b128 a[152:155], v240 offset:4096\n"
"ds_read_b128 a[156:159], v240 offset:4608\n"
"v_mfma_f32_16x16x32_bf16 v[156:159], a[164:167], a[20:23], v[156:159]\n"
"v_exp_f32 v148, v148\n"
"v_mfma_f32_16x16x32_bf16 v[160:163], a[164:167], a[44:47], v[160:163]\n"
"v_exp_f32 v149, v149\n"
"v_mfma_f32_16x16x32_bf16 v[160:163], a[160:163], a[40:43], v[160:163]\n"
"v_exp_f32 v150, v150\n"
"v_mfma_f32_16x16x32_bf16 v[164:167], a[160:163], a[64:67], v[164:167]\n"
"v_exp_f32 v151, v151\n"
"v_mfma_f32_16x16x32_bf16 v[164:167], a[164:167], a[68:71], v[164:167]\n"
"s_waitcnt lgkmcnt(2)\n"
"v_mul_f32 v152, s55, v152\n"
"v_mul_f32 v153, s55, v153\n"
"v_mfma_f32_16x16x32_bf16 v[168:171], a[144:147], a[0:3], 0\n"
"ds_read_b128 a[160:163], v240 offset:5120\n"
"ds_read_b128 a[164:167], v240 offset:5632\n"
"v_mfma_f32_16x16x32_bf16 v[168:171], a[148:151], a[4:7], v[168:171]\n"
"v_mul_f32 v154, s55, v154\n"
"v_mul_f32 v155, s55, v155\n"
"v_mfma_f32_16x16x32_bf16 v[172:175], a[148:151], a[28:31], 0\n"
"v_exp_f32 v152, v152\n"
"v_mfma_f32_16x16x32_bf16 v[172:175], a[144:147], a[24:27], v[172:175]\n"
"v_exp_f32 v153, v153\n"
"v_mfma_f32_16x16x32_bf16 v[176:179], a[144:147], a[48:51], 0\n"
"v_exp_f32 v154, v154\n"
"v_mfma_f32_16x16x32_bf16 v[176:179], a[148:151], a[52:55], v[176:179]\n"
"s_waitcnt lgkmcnt(2)\n"
"v_exp_f32 v155, v155\n"
"v_mfma_f32_16x16x32_bf16 v[168:171], a[152:155], a[8:11], v[168:171]\n"
"ds_read_b128 a[144:147], v240 offset:3328\n"
"ds_read_b128 a[148:151], v240 offset:3840\n"
"v_mfma_f32_16x16x32_bf16 v[168:171], a[156:159], a[12:15], v[168:171]\n"
"v_mul_f32 v156, s55, v156\n"
"v_mul_f32 v157, s55, v157\n"
"v_mfma_f32_16x16x32_bf16 v[172:175], a[156:159], a[36:39], v[172:175]\n"
"v_mul_f32 v158, s55, v158\n"
"v_mul_f32 v159, s55, v159\n"
"v_mfma_f32_16x16x32_bf16 v[172:175], a[152:155], a[32:35], v[172:175]\n"
"v_exp_f32 v156, v156\n"
"v_mfma_f32_16x16x32_bf16 v[176:179], a[152:155], a[56:59], v[176:179]\n"
"v_exp_f32 v157, v157\n"
"v_mfma_f32_16x16x32_bf16 v[176:179], a[156:159], a[60:63], v[176:179]\n"
"s_waitcnt lgkmcnt(2)\n"
"v_exp_f32 v158, v158\n"
"v_mfma_f32_16x16x32_bf16 v[168:171], a[160:163], a[16:19], v[168:171]\n"
"ds_read_b128 a[152:155], v240 offset:4352\n"
"ds_read_b128 a[156:159], v240 offset:4864\n"
"v_mfma_f32_16x16x32_bf16 v[168:171], a[164:167], a[20:23], v[168:171]\n"
"v_exp_f32 v159, v159\n"
"v_mfma_f32_16x16x32_bf16 v[172:175], a[164:167], a[44:47], v[172:175]\n"
"v_mul_f32 v160, s55, v160\n"
"v_mul_f32 v161, s55, v161\n"
"v_mfma_f32_16x16x32_bf16 v[172:175], a[160:163], a[40:43], v[172:175]\n"
"v_mul_f32 v162, s55, v162\n"
"v_mul_f32 v163, s55, v163\n"
"v_mfma_f32_16x16x32_bf16 v[176:179], a[160:163], a[64:67], v[176:179]\n"
"v_exp_f32 v160, v160\n"
"v_mfma_f32_16x16x32_bf16 v[176:179], a[164:167], a[68:71], v[176:179]\n"
"s_waitcnt lgkmcnt(2)\n"
"v_exp_f32 v161, v161\n"
"v_mfma_f32_16x16x32_bf16 v[180:183], a[144:147], a[0:3], 0\n"
"ds_read_b128 a[160:163], v240 offset:5376\n"
"ds_read_b128 a[164:167], v240 offset:5888\n"
"v_mfma_f32_16x16x32_bf16 v[180:183], a[148:151], a[4:7], v[180:183]\n"
"v_exp_f32 v162, v162\n"
"v_mfma_f32_16x16x32_bf16 v[184:187], a[148:151], a[28:31], 0\n"
"v_exp_f32 v163, v163\n"
"v_mfma_f32_16x16x32_bf16 v[184:187], a[144:147], a[24:27], v[184:187]\n"
"s_waitcnt vmcnt(12)\n"
"v_mul_f32 v164, s55, v164\n"
"v_mul_f32 v165, s55, v165\n"
"v_mfma_f32_16x16x32_bf16 v[188:191], a[144:147], a[48:51], 0\n"
"s_barrier\n"
"v_mul_f32 v166, s55, v166\n"
"v_mul_f32 v167, s55, v167\n"
"v_mfma_f32_16x16x32_bf16 v[188:191], a[148:151], a[52:55], v[188:191]\n"
"s_waitcnt lgkmcnt(2)\n"
"v_exp_f32 v164, v164\n"
"v_mfma_f32_16x16x32_bf16 v[180:183], a[152:155], a[8:11], v[180:183]\n"
"s_mov_b32 s59, 196608\n"
"ds_read_b64_tr_b16 a[144:145], v241 offset:21504\n"
"ds_read_b64_tr_b16 a[146:147], v241 offset:21760\n"
"v_mfma_f32_16x16x32_bf16 v[180:183], a[156:159], a[12:15], v[180:183]\n"
"s_add_u32 m0, s52, 33792\n"
"ds_read_b64_tr_b16 a[148:149], v241 offset:24576\n"
"ds_read_b64_tr_b16 a[150:151], v241 offset:24832\n"
"v_mfma_f32_16x16x32_bf16 v[184:187], a[156:159], a[36:39], v[184:187]\n"
"buffer_load_dwordx4 v242, s[40:43], s59 offen offset:0 sc0 nt lds\n"
"v_mfma_f32_16x16x32_bf16 v[184:187], a[152:155], a[32:35], v[184:187]\n"
"ds_write_b128 v219, a[72:75] offset:0\n"
"v_mul_f32 v168, s55, v168\n"
"v_mfma_f32_16x16x32_bf16 v[188:191], a[152:155], a[56:59], v[188:191]\n"
"ds_write_b128 v219, a[76:79] offset:1152\n"
"v_mul_f32 v169, s55, v169\n"
"v_mfma_f32_16x16x32_bf16 v[188:191], a[156:159], a[60:63], v[188:191]\n"
"s_waitcnt lgkmcnt(6)\n"
"ds_read_b128 a[72:75], v221 offset:0\n"
"ds_read_b128 a[76:79], v221 offset:64\n"
"v_mfma_f32_16x16x32_bf16 v[180:183], a[160:163], a[16:19], v[180:183]\n"
"ds_read_b64_tr_b16 a[152:153], v241 offset:21536\n"
"ds_read_b64_tr_b16 a[154:155], v241 offset:21792\n"
"v_mfma_f32_16x16x32_bf16 v[180:183], a[164:167], a[20:23], v[180:183]\n"
"s_add_u32 m0, s52, 34688\n"
"ds_read_b64_tr_b16 a[156:157], v241 offset:24608\n"
"ds_read_b64_tr_b16 a[158:159], v241 offset:24864\n"
"v_mfma_f32_16x16x32_bf16 v[184:187], a[164:167], a[44:47], v[184:187]\n"
"buffer_load_dwordx4 v242, s[40:43], s59 offen offset:128 sc0 nt lds\n"
"v_mfma_f32_16x16x32_bf16 v[184:187], a[160:163], a[40:43], v[184:187]\n"
"ds_write_b128 v219, a[80:83] offset:0\n"
"v_mul_f32 v170, s55, v170\n"
"v_mfma_f32_16x16x32_bf16 v[188:191], a[160:163], a[64:67], v[188:191]\n"
"ds_write_b128 v219, a[84:87] offset:1152\n"
"v_mul_f32 v171, s55, v171\n"
"v_mfma_f32_16x16x32_bf16 v[188:191], a[164:167], a[68:71], v[188:191]\n"
"ds_read_b128 a[80:83], v221 offset:0\n"
"ds_read_b128 a[84:87], v221 offset:64\n"
"v_mfma_f32_16x16x32_bf16 a[240:243], a[252:255], v[192:195], a[240:243]\n"
"v_exp_f32 v165, v165\n"
"v_mfma_f32_16x16x32_bf16 a[240:243], a[252:255], v[204:207], a[240:243]\n"
"s_add_u32 m0, s52, 35584\n"
"v_exp_f32 v166, v166\n"
"v_mfma_f32_16x16x32_bf16 a[244:247], a[252:255], v[196:199], a[244:247]\n"
"buffer_load_dwordx4 v242, s[40:43], s59 offen offset:256 sc0 nt lds\n"
"v_mfma_f32_16x16x32_bf16 a[244:247], a[252:255], v[208:211], a[244:247]\n"
"ds_write_b128 v219, a[88:91] offset:0\n"
"v_mul_f32 v172, s55, v172\n"
"v_mfma_f32_16x16x32_bf16 a[248:251], a[252:255], v[200:203], a[248:251]\n"
"ds_write_b128 v219, a[92:95] offset:1152\n"
"v_mul_f32 v173, s55, v173\n"
"v_mfma_f32_16x16x32_bf16 a[248:251], a[252:255], v[212:215], a[248:251]\n"
"s_waitcnt lgkmcnt(14)\n"
"ds_read_b128 a[88:91], v221 offset:0\n"
"ds_read_b128 a[92:95], v221 offset:64\n"
"v_mfma_f32_16x16x32_bf16 v[0:3], a[144:147], v[192:195], v[0:3]\n"
"s_mov_b32 s70, 208896\n"
"ds_read_b64_tr_b16 a[160:161], v241 offset:22016\n"
"ds_read_b64_tr_b16 a[162:163], v241 offset:22272\n"
"v_mfma_f32_16x16x32_bf16 v[0:3], a[148:151], v[204:207], v[0:3]\n"
"s_add_u32 m0, s52, 36864\n"
"ds_read_b64_tr_b16 a[164:165], v241 offset:25088\n"
"ds_read_b64_tr_b16 a[166:167], v241 offset:25344\n"
"v_mfma_f32_16x16x32_bf16 v[48:51], a[148:151], v[208:211], v[48:51]\n"
"buffer_load_dwordx4 v242, s[40:43], s70 offen offset:0 sc0 nt lds\n"
"v_mfma_f32_16x16x32_bf16 v[48:51], a[144:147], v[196:199], v[48:51]\n"
"ds_write_b128 v219, a[96:99] offset:0\n"
"v_mul_f32 v174, s55, v174\n"
"v_mfma_f32_16x16x32_bf16 v[96:99], a[144:147], v[200:203], v[96:99]\n"
"ds_write_b128 v219, a[100:103] offset:1152\n"
"v_mul_f32 v175, s55, v175\n"
"v_mfma_f32_16x16x32_bf16 v[96:99], a[148:151], v[212:215], v[96:99]\n"
"s_waitcnt lgkmcnt(14)\n"
"ds_read_b128 a[96:99], v221 offset:0\n"
"ds_read_b128 a[100:103], v221 offset:64\n"
"v_mfma_f32_16x16x32_bf16 v[4:7], a[152:155], v[192:195], v[4:7]\n"
"ds_read_b64_tr_b16 a[144:145], v241 offset:22048\n"
"ds_read_b64_tr_b16 a[146:147], v241 offset:22304\n"
"v_mfma_f32_16x16x32_bf16 v[4:7], a[156:159], v[204:207], v[4:7]\n"
"s_add_u32 m0, s52, 37760\n"
"ds_read_b64_tr_b16 a[148:149], v241 offset:25120\n"
"ds_read_b64_tr_b16 a[150:151], v241 offset:25376\n"
"v_mfma_f32_16x16x32_bf16 v[52:55], a[156:159], v[208:211], v[52:55]\n"
"buffer_load_dwordx4 v242, s[40:43], s70 offen offset:128 sc0 nt lds\n"
"v_mfma_f32_16x16x32_bf16 v[52:55], a[152:155], v[196:199], v[52:55]\n"
"ds_write_b128 v219, a[104:107] offset:0\n"
"v_mul_f32 v176, s55, v176\n"
"v_mfma_f32_16x16x32_bf16 v[100:103], a[152:155], v[200:203], v[100:103]\n"
"ds_write_b128 v219, a[108:111] offset:1152\n"
"v_mul_f32 v177, s55, v177\n"
"v_mfma_f32_16x16x32_bf16 v[100:103], a[156:159], v[212:215], v[100:103]\n"
"s_waitcnt lgkmcnt(10)\n"
"ds_read_b128 a[104:107], v221 offset:0\n"
"ds_read_b128 a[108:111], v221 offset:64\n"
"v_mfma_f32_16x16x32_bf16 v[8:11], a[160:163], v[192:195], v[8:11]\n"
"ds_read_b64_tr_b16 a[152:153], v241 offset:22528\n"
"ds_read_b64_tr_b16 a[154:155], v241 offset:22784\n"
"v_mfma_f32_16x16x32_bf16 v[8:11], a[164:167], v[204:207], v[8:11]\n"
"s_add_u32 m0, s52, 38656\n"
"ds_read_b64_tr_b16 a[156:157], v241 offset:25600\n"
"ds_read_b64_tr_b16 a[158:159], v241 offset:25856\n"
"v_mfma_f32_16x16x32_bf16 v[56:59], a[164:167], v[208:211], v[56:59]\n"
"buffer_load_dwordx4 v242, s[40:43], s70 offen offset:256 sc0 nt lds\n"
"v_mfma_f32_16x16x32_bf16 v[56:59], a[160:163], v[196:199], v[56:59]\n"
"ds_write_b128 v219, a[112:115] offset:0\n"
"v_mul_f32 v178, s55, v178\n"
"v_mfma_f32_16x16x32_bf16 v[104:107], a[160:163], v[200:203], v[104:107]\n"
"ds_write_b128 v219, a[116:119] offset:1152\n"
"v_mul_f32 v179, s55, v179\n"
"v_mfma_f32_16x16x32_bf16 v[104:107], a[164:167], v[212:215], v[104:107]\n"
"s_waitcnt lgkmcnt(10)\n"
"ds_read_b128 a[112:115], v221 offset:0\n"
"ds_read_b128 a[116:119], v221 offset:64\n"
"v_mfma_f32_16x16x32_bf16 v[12:15], a[144:147], v[192:195], v[12:15]\n"
"s_mov_b32 s53, 245760\n"
"ds_read_b64_tr_b16 a[160:161], v241 offset:22560\n"
"ds_read_b64_tr_b16 a[162:163], v241 offset:22816\n"
"v_mfma_f32_16x16x32_bf16 v[12:15], a[148:151], v[204:207], v[12:15]\n"
"s_add_u32 m0, s52, 18432\n"
"ds_read_b64_tr_b16 a[164:165], v241 offset:25632\n"
"ds_read_b64_tr_b16 a[166:167], v241 offset:25888\n"
"v_mfma_f32_16x16x32_bf16 v[60:63], a[148:151], v[208:211], v[60:63]\n"
"buffer_load_dwordx4 v242, s[36:39], s53 offen offset:0 sc0 nt lds\n"
"v_mfma_f32_16x16x32_bf16 v[60:63], a[144:147], v[196:199], v[60:63]\n"
"ds_write_b128 v219, a[120:123] offset:0\n"
"v_mul_f32 v180, s55, v180\n"
"v_mfma_f32_16x16x32_bf16 v[108:111], a[144:147], v[200:203], v[108:111]\n"
"ds_write_b128 v219, a[124:127] offset:1152\n"
"v_mul_f32 v181, s55, v181\n"
"v_mfma_f32_16x16x32_bf16 v[108:111], a[148:151], v[212:215], v[108:111]\n"
"s_waitcnt lgkmcnt(10)\n"
"ds_read_b128 a[120:123], v221 offset:0\n"
"ds_read_b128 a[124:127], v221 offset:64\n"
"v_mfma_f32_16x16x32_bf16 v[16:19], a[152:155], v[192:195], v[16:19]\n"
"ds_read_b64_tr_b16 a[144:145], v241 offset:23040\n"
"ds_read_b64_tr_b16 a[146:147], v241 offset:23296\n"
"v_mfma_f32_16x16x32_bf16 v[16:19], a[156:159], v[204:207], v[16:19]\n"
"s_add_u32 m0, s52, 19328\n"
"ds_read_b64_tr_b16 a[148:149], v241 offset:26112\n"
"ds_read_b64_tr_b16 a[150:151], v241 offset:26368\n"
"v_mfma_f32_16x16x32_bf16 v[64:67], a[156:159], v[208:211], v[64:67]\n"
"buffer_load_dwordx4 v242, s[36:39], s53 offen offset:128 sc0 nt lds\n"
"v_mfma_f32_16x16x32_bf16 v[64:67], a[152:155], v[196:199], v[64:67]\n"
"ds_write_b128 v219, a[128:131] offset:0\n"
"v_mul_f32 v182, s55, v182\n"
"v_mfma_f32_16x16x32_bf16 v[112:115], a[152:155], v[200:203], v[112:115]\n"
"ds_write_b128 v219, a[132:135] offset:1152\n"
"v_mul_f32 v183, s55, v183\n"
"v_mfma_f32_16x16x32_bf16 v[112:115], a[156:159], v[212:215], v[112:115]\n"
"s_waitcnt lgkmcnt(10)\n"
"ds_read_b128 a[128:131], v221 offset:0\n"
"ds_read_b128 a[132:135], v221 offset:64\n"
"v_mfma_f32_16x16x32_bf16 v[20:23], a[160:163], v[192:195], v[20:23]\n"
"ds_read_b64_tr_b16 a[152:153], v241 offset:23072\n"
"ds_read_b64_tr_b16 a[154:155], v241 offset:23328\n"
"v_mfma_f32_16x16x32_bf16 v[20:23], a[164:167], v[204:207], v[20:23]\n"
"s_add_u32 m0, s52, 20224\n"
"ds_read_b64_tr_b16 a[156:157], v241 offset:26144\n"
"ds_read_b64_tr_b16 a[158:159], v241 offset:26400\n"
"v_mfma_f32_16x16x32_bf16 v[68:71], a[164:167], v[208:211], v[68:71]\n"
"buffer_load_dwordx4 v242, s[36:39], s53 offen offset:256 sc0 nt lds\n"
"v_mfma_f32_16x16x32_bf16 v[68:71], a[160:163], v[196:199], v[68:71]\n"
"ds_write_b128 v219, a[136:139] offset:0\n"
"v_mul_f32 v184, s55, v184\n"
"v_mfma_f32_16x16x32_bf16 v[116:119], a[160:163], v[200:203], v[116:119]\n"
"ds_write_b128 v219, a[140:143] offset:1152\n"
"v_mul_f32 v185, s55, v185\n"
"v_mfma_f32_16x16x32_bf16 v[116:119], a[164:167], v[212:215], v[116:119]\n"
"s_waitcnt lgkmcnt(10)\n"
"ds_read_b128 a[136:139], v221 offset:0\n"
"ds_read_b128 a[140:143], v221 offset:64\n"
"v_mfma_f32_16x16x32_bf16 v[24:27], a[144:147], v[192:195], v[24:27]\n"
"ds_read_b64_tr_b16 a[160:161], v241 offset:23552\n"
"ds_read_b64_tr_b16 a[162:163], v241 offset:23808\n"
"v_mfma_f32_16x16x32_bf16 v[24:27], a[148:151], v[204:207], v[24:27]\n"
"ds_read_b64_tr_b16 a[164:165], v241 offset:26624\n"
"ds_read_b64_tr_b16 a[166:167], v241 offset:26880\n"
"v_mfma_f32_16x16x32_bf16 v[72:75], a[148:151], v[208:211], v[72:75]\n"
"v_exp_f32 v167, v167\n"
"v_mfma_f32_16x16x32_bf16 v[72:75], a[144:147], v[196:199], v[72:75]\n"
"v_exp_f32 v168, v168\n"
"v_mfma_f32_16x16x32_bf16 v[120:123], a[144:147], v[200:203], v[120:123]\n"
"v_exp_f32 v169, v169\n"
"v_mfma_f32_16x16x32_bf16 v[120:123], a[148:151], v[212:215], v[120:123]\n"
"s_waitcnt lgkmcnt(8)\n"
"v_exp_f32 v170, v170\n"
"v_mfma_f32_16x16x32_bf16 v[28:31], a[152:155], v[192:195], v[28:31]\n"
"ds_read_b64_tr_b16 a[144:145], v241 offset:23584\n"
"ds_read_b64_tr_b16 a[146:147], v241 offset:23840\n"
"v_mfma_f32_16x16x32_bf16 v[28:31], a[156:159], v[204:207], v[28:31]\n"
"ds_read_b64_tr_b16 a[148:149], v241 offset:26656\n"
"ds_read_b64_tr_b16 a[150:151], v241 offset:26912\n"
"v_mfma_f32_16x16x32_bf16 v[76:79], a[156:159], v[208:211], v[76:79]\n"
"v_exp_f32 v171, v171\n"
"v_mfma_f32_16x16x32_bf16 v[76:79], a[152:155], v[196:199], v[76:79]\n"
"v_exp_f32 v172, v172\n"
"v_mfma_f32_16x16x32_bf16 v[124:127], a[152:155], v[200:203], v[124:127]\n"
"v_exp_f32 v173, v173\n"
"v_mfma_f32_16x16x32_bf16 v[124:127], a[156:159], v[212:215], v[124:127]\n"
"s_waitcnt lgkmcnt(4)\n"
"v_exp_f32 v174, v174\n"
"v_mfma_f32_16x16x32_bf16 v[32:35], a[160:163], v[192:195], v[32:35]\n"
"ds_read_b64_tr_b16 a[152:153], v241 offset:24064\n"
"ds_read_b64_tr_b16 a[154:155], v241 offset:24320\n"
"v_mfma_f32_16x16x32_bf16 v[32:35], a[164:167], v[204:207], v[32:35]\n"
"ds_read_b64_tr_b16 a[156:157], v241 offset:27136\n"
"ds_read_b64_tr_b16 a[158:159], v241 offset:27392\n"
"v_mfma_f32_16x16x32_bf16 v[80:83], a[164:167], v[208:211], v[80:83]\n"
"v_exp_f32 v175, v175\n"
"v_mfma_f32_16x16x32_bf16 v[80:83], a[160:163], v[196:199], v[80:83]\n"
"v_exp_f32 v176, v176\n"
"v_mfma_f32_16x16x32_bf16 v[128:131], a[160:163], v[200:203], v[128:131]\n"
"v_exp_f32 v177, v177\n"
"v_mfma_f32_16x16x32_bf16 v[128:131], a[164:167], v[212:215], v[128:131]\n"
"s_waitcnt lgkmcnt(4)\n"
"v_exp_f32 v178, v178\n"
"v_mfma_f32_16x16x32_bf16 v[36:39], a[144:147], v[192:195], v[36:39]\n"
"s_mov_b32 s54, 258048\n"
"ds_read_b64_tr_b16 a[160:161], v241 offset:24096\n"
"ds_read_b64_tr_b16 a[162:163], v241 offset:24352\n"
"v_mfma_f32_16x16x32_bf16 v[36:39], a[148:151], v[204:207], v[36:39]\n"
"s_add_u32 m0, s52, 0\n"
"ds_read_b64_tr_b16 a[164:165], v241 offset:27168\n"
"ds_read_b64_tr_b16 a[166:167], v241 offset:27424\n"
"v_mfma_f32_16x16x32_bf16 v[84:87], a[148:151], v[208:211], v[84:87]\n"
"buffer_load_dwordx4 v242, s[36:39], s54 offen offset:0 sc0 nt lds\n"
"v_mfma_f32_16x16x32_bf16 v[84:87], a[144:147], v[196:199], v[84:87]\n"
"v_exp_f32 v179, v179\n"
"v_mfma_f32_16x16x32_bf16 v[132:135], a[144:147], v[200:203], v[132:135]\n"
"v_exp_f32 v180, v180\n"
"v_mfma_f32_16x16x32_bf16 v[132:135], a[148:151], v[212:215], v[132:135]\n"
"s_waitcnt lgkmcnt(4)\n"
"v_exp_f32 v181, v181\n"
"v_mfma_f32_16x16x32_bf16 v[40:43], a[152:155], v[192:195], v[40:43]\n"
"ds_read_b128 a[144:147], v240 offset:6144\n"
"ds_read_b128 a[148:151], v240 offset:6656\n"
"v_mfma_f32_16x16x32_bf16 v[40:43], a[156:159], v[204:207], v[40:43]\n"
"s_add_u32 m0, s52, 896\n"
"v_exp_f32 v182, v182\n"
"v_mfma_f32_16x16x32_bf16 v[88:91], a[156:159], v[208:211], v[88:91]\n"
"buffer_load_dwordx4 v242, s[36:39], s54 offen offset:128 sc0 nt lds\n"
"v_mfma_f32_16x16x32_bf16 v[88:91], a[152:155], v[196:199], v[88:91]\n"
"v_exp_f32 v183, v183\n"
"v_mfma_f32_16x16x32_bf16 v[136:139], a[152:155], v[200:203], v[136:139]\n"
"v_mul_f32 v186, s55, v186\n"
"v_mul_f32 v187, s55, v187\n"
"v_mfma_f32_16x16x32_bf16 v[136:139], a[156:159], v[212:215], v[136:139]\n"
"s_waitcnt lgkmcnt(2)\n"
"v_exp_f32 v184, v184\n"
"v_mfma_f32_16x16x32_bf16 v[44:47], a[160:163], v[192:195], v[44:47]\n"
"ds_read_b128 a[152:155], v240 offset:7168\n"
"ds_read_b128 a[156:159], v240 offset:7680\n"
"v_mfma_f32_16x16x32_bf16 v[44:47], a[164:167], v[204:207], v[44:47]\n"
"s_add_u32 m0, s52, 1792\n"
"v_cvt_pk_bf16_f32 v192, v144, v145\n"
"v_cvt_pk_bf16_f32 v193, v146, v147\n"
"v_mfma_f32_16x16x32_bf16 v[92:95], a[164:167], v[208:211], v[92:95]\n"
"buffer_load_dwordx4 v242, s[36:39], s54 offen offset:256 sc0 nt lds\n"
"v_mfma_f32_16x16x32_bf16 v[92:95], a[160:163], v[196:199], v[92:95]\n"
"v_cvt_pk_bf16_f32 v196, v148, v149\n"
"v_cvt_pk_bf16_f32 v197, v150, v151\n"
"v_mfma_f32_16x16x32_bf16 v[140:143], a[160:163], v[200:203], v[140:143]\n"
"v_cvt_pk_bf16_f32 v200, v152, v153\n"
"v_cvt_pk_bf16_f32 v201, v154, v155\n"
"v_mfma_f32_16x16x32_bf16 v[140:143], a[164:167], v[212:215], v[140:143]\n"
"s_waitcnt lgkmcnt(2)\n"
"v_cvt_pk_bf16_f32 v194, v156, v157\n"
"v_cvt_pk_bf16_f32 v195, v158, v159\n"
"v_mfma_f32_16x16x32_bf16 v[144:147], a[144:147], a[72:75], 0\n"
"ds_read_b128 a[160:163], v240 offset:8192\n"
"ds_read_b128 a[164:167], v240 offset:8704\n"
"v_mfma_f32_16x16x32_bf16 v[144:147], a[148:151], a[76:79], v[144:147]\n"
"v_cvt_pk_bf16_f32 v198, v160, v161\n"
"v_cvt_pk_bf16_f32 v199, v162, v163\n"
"v_mfma_f32_16x16x32_bf16 v[148:151], a[148:151], a[100:103], 0\n"
"v_cvt_pk_bf16_f32 v202, v164, v165\n"
"v_cvt_pk_bf16_f32 v203, v166, v167\n"
"v_mfma_f32_16x16x32_bf16 v[148:151], a[144:147], a[96:99], v[148:151]\n"
"v_cvt_pk_bf16_f32 v204, v168, v169\n"
"v_cvt_pk_bf16_f32 v205, v170, v171\n"
"v_mfma_f32_16x16x32_bf16 v[152:155], a[144:147], a[120:123], 0\n"
"v_cvt_pk_bf16_f32 v208, v172, v173\n"
"v_cvt_pk_bf16_f32 v209, v174, v175\n"
"v_mfma_f32_16x16x32_bf16 v[152:155], a[148:151], a[124:127], v[152:155]\n"
"s_waitcnt lgkmcnt(2)\n"
"v_cvt_pk_bf16_f32 v212, v176, v177\n"
"v_cvt_pk_bf16_f32 v213, v178, v179\n"
"v_mfma_f32_16x16x32_bf16 v[144:147], a[152:155], a[80:83], v[144:147]\n"
"ds_read_b128 a[144:147], v240 offset:6400\n"
"ds_read_b128 a[148:151], v240 offset:6912\n"
"v_mfma_f32_16x16x32_bf16 v[144:147], a[156:159], a[84:87], v[144:147]\n"
"v_cvt_pk_bf16_f32 v206, v180, v181\n"
"v_cvt_pk_bf16_f32 v207, v182, v183\n"
"v_mfma_f32_16x16x32_bf16 v[148:151], a[156:159], a[108:111], v[148:151]\n"
"v_exp_f32 v185, v185\n"
"v_mfma_f32_16x16x32_bf16 v[148:151], a[152:155], a[104:107], v[148:151]\n"
"v_exp_f32 v186, v186\n"
"v_mfma_f32_16x16x32_bf16 v[152:155], a[152:155], a[128:131], v[152:155]\n"
"v_exp_f32 v187, v187\n"
"v_mfma_f32_16x16x32_bf16 v[152:155], a[156:159], a[132:135], v[152:155]\n"
"s_waitcnt lgkmcnt(2)\n"
"v_mul_f32 v188, s55, v188\n"
"v_mul_f32 v189, s55, v189\n"
"v_mfma_f32_16x16x32_bf16 v[144:147], a[160:163], a[88:91], v[144:147]\n"
"ds_read_b128 a[152:155], v240 offset:7424\n"
"ds_read_b128 a[156:159], v240 offset:7936\n"
"v_mfma_f32_16x16x32_bf16 v[144:147], a[164:167], a[92:95], v[144:147]\n"
"v_mul_f32 v190, s55, v190\n"
"v_mul_f32 v191, s55, v191\n"
"v_mfma_f32_16x16x32_bf16 v[148:151], a[164:167], a[116:119], v[148:151]\n"
"v_cvt_pk_bf16_f32 v210, v184, v185\n"
"v_cvt_pk_bf16_f32 v211, v186, v187\n"
"v_mfma_f32_16x16x32_bf16 v[148:151], a[160:163], a[112:115], v[148:151]\n"
"v_exp_f32 v188, v188\n"
"v_mfma_f32_16x16x32_bf16 v[152:155], a[160:163], a[136:139], v[152:155]\n"
"v_exp_f32 v189, v189\n"
"v_mfma_f32_16x16x32_bf16 v[152:155], a[164:167], a[140:143], v[152:155]\n"
"s_waitcnt lgkmcnt(2)\n"
"v_exp_f32 v190, v190\n"
"v_mfma_f32_16x16x32_bf16 v[156:159], a[144:147], a[72:75], 0\n"
"ds_read_b128 a[160:163], v240 offset:8448\n"
"ds_read_b128 a[164:167], v240 offset:8960\n"
"v_mfma_f32_16x16x32_bf16 v[156:159], a[148:151], a[76:79], v[156:159]\n"
"v_exp_f32 v191, v191\n"
"v_mfma_f32_16x16x32_bf16 v[160:163], a[148:151], a[100:103], 0\n"
"v_cvt_pk_bf16_f32 v214, v188, v189\n"
"v_cvt_pk_bf16_f32 v215, v190, v191\n"
"v_mfma_f32_16x16x32_bf16 v[160:163], a[144:147], a[96:99], v[160:163]\n"
"v_mul_f32 v144, s55, v144\n"
"v_mul_f32 v145, s55, v145\n"
"v_mfma_f32_16x16x32_bf16 v[164:167], a[144:147], a[120:123], 0\n"
"v_mul_f32 v146, s55, v146\n"
"v_mul_f32 v147, s55, v147\n"
"v_mfma_f32_16x16x32_bf16 v[164:167], a[148:151], a[124:127], v[164:167]\n"
"s_waitcnt lgkmcnt(2)\n"
"v_exp_f32 v144, v144\n"
"v_mfma_f32_16x16x32_bf16 v[156:159], a[152:155], a[80:83], v[156:159]\n"
"ds_read_b128 a[144:147], v240 offset:9216\n"
"ds_read_b128 a[148:151], v240 offset:9728\n"
"v_mfma_f32_16x16x32_bf16 v[156:159], a[156:159], a[84:87], v[156:159]\n"
"v_exp_f32 v145, v145\n"
"v_mfma_f32_16x16x32_bf16 v[160:163], a[156:159], a[108:111], v[160:163]\n"
"v_exp_f32 v146, v146\n"
"v_mfma_f32_16x16x32_bf16 v[160:163], a[152:155], a[104:107], v[160:163]\n"
"v_exp_f32 v147, v147\n"
"v_mfma_f32_16x16x32_bf16 v[164:167], a[152:155], a[128:131], v[164:167]\n"
"v_mul_f32 v148, s55, v148\n"
"v_mul_f32 v149, s55, v149\n"
"v_mfma_f32_16x16x32_bf16 v[164:167], a[156:159], a[132:135], v[164:167]\n"
"s_waitcnt lgkmcnt(2)\n"
"v_mul_f32 v150, s55, v150\n"
"v_mul_f32 v151, s55, v151\n"
"v_mfma_f32_16x16x32_bf16 v[156:159], a[160:163], a[88:91], v[156:159]\n"
"ds_read_b128 a[152:155], v240 offset:10240\n"
"ds_read_b128 a[156:159], v240 offset:10752\n"
"v_mfma_f32_16x16x32_bf16 v[156:159], a[164:167], a[92:95], v[156:159]\n"
"v_exp_f32 v148, v148\n"
"v_mfma_f32_16x16x32_bf16 v[160:163], a[164:167], a[116:119], v[160:163]\n"
"v_exp_f32 v149, v149\n"
"v_mfma_f32_16x16x32_bf16 v[160:163], a[160:163], a[112:115], v[160:163]\n"
"v_exp_f32 v150, v150\n"
"v_mfma_f32_16x16x32_bf16 v[164:167], a[160:163], a[136:139], v[164:167]\n"
"v_exp_f32 v151, v151\n"
"v_mfma_f32_16x16x32_bf16 v[164:167], a[164:167], a[140:143], v[164:167]\n"
"s_waitcnt lgkmcnt(2)\n"
"v_mul_f32 v152, s55, v152\n"
"v_mul_f32 v153, s55, v153\n"
"v_mfma_f32_16x16x32_bf16 v[168:171], a[144:147], a[72:75], 0\n"
"ds_read_b128 a[160:163], v240 offset:11264\n"
"ds_read_b128 a[164:167], v240 offset:11776\n"
"v_mfma_f32_16x16x32_bf16 v[168:171], a[148:151], a[76:79], v[168:171]\n"
"v_mul_f32 v154, s55, v154\n"
"v_mul_f32 v155, s55, v155\n"
"v_mfma_f32_16x16x32_bf16 v[172:175], a[148:151], a[100:103], 0\n"
"v_exp_f32 v152, v152\n"
"v_mfma_f32_16x16x32_bf16 v[172:175], a[144:147], a[96:99], v[172:175]\n"
"v_exp_f32 v153, v153\n"
"v_mfma_f32_16x16x32_bf16 v[176:179], a[144:147], a[120:123], 0\n"
"v_exp_f32 v154, v154\n"
"v_mfma_f32_16x16x32_bf16 v[176:179], a[148:151], a[124:127], v[176:179]\n"
"s_waitcnt lgkmcnt(2)\n"
"v_exp_f32 v155, v155\n"
"v_mfma_f32_16x16x32_bf16 v[168:171], a[152:155], a[80:83], v[168:171]\n"
"ds_read_b128 a[144:147], v240 offset:9472\n"
"ds_read_b128 a[148:151], v240 offset:9984\n"
"v_mfma_f32_16x16x32_bf16 v[168:171], a[156:159], a[84:87], v[168:171]\n"
"v_mul_f32 v156, s55, v156\n"
"v_mul_f32 v157, s55, v157\n"
"v_mfma_f32_16x16x32_bf16 v[172:175], a[156:159], a[108:111], v[172:175]\n"
"v_mul_f32 v158, s55, v158\n"
"v_mul_f32 v159, s55, v159\n"
"v_mfma_f32_16x16x32_bf16 v[172:175], a[152:155], a[104:107], v[172:175]\n"
"v_exp_f32 v156, v156\n"
"v_mfma_f32_16x16x32_bf16 v[176:179], a[152:155], a[128:131], v[176:179]\n"
"v_exp_f32 v157, v157\n"
"v_mfma_f32_16x16x32_bf16 v[176:179], a[156:159], a[132:135], v[176:179]\n"
"s_waitcnt lgkmcnt(2)\n"
"v_exp_f32 v158, v158\n"
"v_mfma_f32_16x16x32_bf16 v[168:171], a[160:163], a[88:91], v[168:171]\n"
"ds_read_b128 a[152:155], v240 offset:10496\n"
"ds_read_b128 a[156:159], v240 offset:11008\n"
"v_mfma_f32_16x16x32_bf16 v[168:171], a[164:167], a[92:95], v[168:171]\n"
"v_exp_f32 v159, v159\n"
"v_mfma_f32_16x16x32_bf16 v[172:175], a[164:167], a[116:119], v[172:175]\n"
"v_mul_f32 v160, s55, v160\n"
"v_mul_f32 v161, s55, v161\n"
"v_mfma_f32_16x16x32_bf16 v[172:175], a[160:163], a[112:115], v[172:175]\n"
"v_mul_f32 v162, s55, v162\n"
"v_mul_f32 v163, s55, v163\n"
"v_mfma_f32_16x16x32_bf16 v[176:179], a[160:163], a[136:139], v[176:179]\n"
"v_exp_f32 v160, v160\n"
"v_mfma_f32_16x16x32_bf16 v[176:179], a[164:167], a[140:143], v[176:179]\n"
"s_waitcnt lgkmcnt(2)\n"
"v_exp_f32 v161, v161\n"
"v_mfma_f32_16x16x32_bf16 v[180:183], a[144:147], a[72:75], 0\n"
"ds_read_b128 a[160:163], v240 offset:11520\n"
"ds_read_b128 a[164:167], v240 offset:12032\n"
"v_mfma_f32_16x16x32_bf16 v[180:183], a[148:151], a[76:79], v[180:183]\n"
"v_exp_f32 v162, v162\n"
"v_mfma_f32_16x16x32_bf16 v[184:187], a[148:151], a[100:103], 0\n"
"v_exp_f32 v163, v163\n"
"v_mfma_f32_16x16x32_bf16 v[184:187], a[144:147], a[96:99], v[184:187]\n"
"s_waitcnt vmcnt(12)\n"
"v_mul_f32 v164, s55, v164\n"
"v_mul_f32 v165, s55, v165\n"
"v_mfma_f32_16x16x32_bf16 v[188:191], a[144:147], a[120:123], 0\n"
"s_barrier\n"
"v_mul_f32 v166, s55, v166\n"
"v_mul_f32 v167, s55, v167\n"
"v_mfma_f32_16x16x32_bf16 v[188:191], a[148:151], a[124:127], v[188:191]\n"
"s_waitcnt lgkmcnt(2)\n"
"v_exp_f32 v164, v164\n"
"v_mfma_f32_16x16x32_bf16 v[180:183], a[152:155], a[80:83], v[180:183]\n"
"s_mov_b32 s53, 270336\n"
"ds_read_b64_tr_b16 a[144:145], v241 offset:27648\n"
"ds_read_b64_tr_b16 a[146:147], v241 offset:27904\n"
"v_mfma_f32_16x16x32_bf16 v[180:183], a[156:159], a[84:87], v[180:183]\n"
"s_add_u32 m0, s52, 3072\n"
"ds_read_b64_tr_b16 a[148:149], v241 offset:30720\n"
"ds_read_b64_tr_b16 a[150:151], v241 offset:30976\n"
"v_mfma_f32_16x16x32_bf16 v[184:187], a[156:159], a[108:111], v[184:187]\n"
"buffer_load_dwordx4 v242, s[36:39], s53 offen offset:0 sc0 nt lds\n"
"v_mfma_f32_16x16x32_bf16 v[184:187], a[152:155], a[104:107], v[184:187]\n"
"v_exp_f32 v165, v165\n"
"v_mfma_f32_16x16x32_bf16 v[188:191], a[152:155], a[128:131], v[188:191]\n"
"v_exp_f32 v166, v166\n"
"v_mfma_f32_16x16x32_bf16 v[188:191], a[156:159], a[132:135], v[188:191]\n"
"s_waitcnt lgkmcnt(4)\n"
"v_exp_f32 v167, v167\n"
"v_mfma_f32_16x16x32_bf16 v[180:183], a[160:163], a[88:91], v[180:183]\n"
"ds_read_b64_tr_b16 a[152:153], v241 offset:27680\n"
"ds_read_b64_tr_b16 a[154:155], v241 offset:27936\n"
"v_mfma_f32_16x16x32_bf16 v[180:183], a[164:167], a[92:95], v[180:183]\n"
"s_add_u32 m0, s52, 3968\n"
"ds_read_b64_tr_b16 a[156:157], v241 offset:30752\n"
"ds_read_b64_tr_b16 a[158:159], v241 offset:31008\n"
"v_mfma_f32_16x16x32_bf16 v[184:187], a[164:167], a[116:119], v[184:187]\n"
"buffer_load_dwordx4 v242, s[36:39], s53 offen offset:128 sc0 nt lds\n"
"v_mfma_f32_16x16x32_bf16 v[184:187], a[160:163], a[112:115], v[184:187]\n"
"v_mul_f32 v168, s55, v168\n"
"v_mul_f32 v169, s55, v169\n"
"v_mfma_f32_16x16x32_bf16 v[188:191], a[160:163], a[136:139], v[188:191]\n"
"v_mul_f32 v170, s55, v170\n"
"v_mul_f32 v171, s55, v171\n"
"v_mfma_f32_16x16x32_bf16 v[188:191], a[164:167], a[140:143], v[188:191]\n"
"v_exp_f32 v168, v168\n"
"v_mfma_f32_16x16x32_bf16 a[240:243], a[252:255], v[192:195], a[240:243]\n"
"v_exp_f32 v169, v169\n"
"v_mfma_f32_16x16x32_bf16 a[240:243], a[252:255], v[204:207], a[240:243]\n"
"s_add_u32 m0, s52, 4864\n"
"v_exp_f32 v170, v170\n"
"v_mfma_f32_16x16x32_bf16 a[244:247], a[252:255], v[196:199], a[244:247]\n"
"buffer_load_dwordx4 v242, s[36:39], s53 offen offset:256 sc0 nt lds\n"
"v_mfma_f32_16x16x32_bf16 a[244:247], a[252:255], v[208:211], a[244:247]\n"
"v_exp_f32 v171, v171\n"
"v_mfma_f32_16x16x32_bf16 a[248:251], a[252:255], v[200:203], a[248:251]\n"
"v_mul_f32 v172, s55, v172\n"
"v_mul_f32 v173, s55, v173\n"
"v_mfma_f32_16x16x32_bf16 a[248:251], a[252:255], v[212:215], a[248:251]\n"
"s_waitcnt lgkmcnt(4)\n"
"v_mul_f32 v174, s55, v174\n"
"v_mul_f32 v175, s55, v175\n"
"v_mfma_f32_16x16x32_bf16 v[0:3], a[144:147], v[192:195], v[0:3]\n"
"s_mov_b32 s54, 282624\n"
"ds_read_b64_tr_b16 a[160:161], v241 offset:28160\n"
"ds_read_b64_tr_b16 a[162:163], v241 offset:28416\n"
"v_mfma_f32_16x16x32_bf16 v[0:3], a[148:151], v[204:207], v[0:3]\n"
"s_add_u32 m0, s52, 6144\n"
"ds_read_b64_tr_b16 a[164:165], v241 offset:31232\n"
"ds_read_b64_tr_b16 a[166:167], v241 offset:31488\n"
"v_mfma_f32_16x16x32_bf16 v[48:51], a[148:151], v[208:211], v[48:51]\n"
"buffer_load_dwordx4 v242, s[36:39], s54 offen offset:0 sc0 nt lds\n"
"v_mfma_f32_16x16x32_bf16 v[48:51], a[144:147], v[196:199], v[48:51]\n"
"v_accvgpr_read_b32 v232, a240\n"
"v_subrev_u32 v246, s57, v232\n"
"v_mfma_f32_16x16x32_bf16 v[96:99], a[144:147], v[200:203], v[96:99]\n"
"v_max_u32 v238, v238, v246\n"
"v_mul_f32 v176, s55, v176\n"
"v_mfma_f32_16x16x32_bf16 v[96:99], a[148:151], v[212:215], v[96:99]\n"
"s_waitcnt lgkmcnt(4)\n"
"v_rcp_f32 v232, v232\n"
"v_mfma_f32_16x16x32_bf16 v[4:7], a[152:155], v[192:195], v[4:7]\n"
"ds_read_b64_tr_b16 a[144:145], v241 offset:28192\n"
"ds_read_b64_tr_b16 a[146:147], v241 offset:28448\n"
"v_mfma_f32_16x16x32_bf16 v[4:7], a[156:159], v[204:207], v[4:7]\n"
"s_add_u32 m0, s52, 7040\n"
"ds_read_b64_tr_b16 a[148:149], v241 offset:31264\n"
"ds_read_b64_tr_b16 a[150:151], v241 offset:31520\n"
"v_mfma_f32_16x16x32_bf16 v[52:55], a[156:159], v[208:211], v[52:55]\n"
"buffer_load_dwordx4 v242, s[36:39], s54 offen offset:128 sc0 nt lds\n"
"v_mfma_f32_16x16x32_bf16 v[52:55], a[152:155], v[196:199], v[52:55]\n"
"v_accvgpr_read_b32 v233, a244\n"
"v_subrev_u32 v247, s57, v233\n"
"v_mfma_f32_16x16x32_bf16 v[100:103], a[152:155], v[200:203], v[100:103]\n"
"v_max_u32 v238, v238, v247\n"
"v_accvgpr_read_b32 v234, a248\n"
"v_mfma_f32_16x16x32_bf16 v[100:103], a[156:159], v[212:215], v[100:103]\n"
"s_waitcnt lgkmcnt(4)\n"
"v_rcp_f32 v233, v233\n"
"v_mfma_f32_16x16x32_bf16 v[8:11], a[160:163], v[192:195], v[8:11]\n"
"ds_read_b64_tr_b16 a[152:153], v241 offset:28672\n"
"ds_read_b64_tr_b16 a[154:155], v241 offset:28928\n"
"v_mfma_f32_16x16x32_bf16 v[8:11], a[164:167], v[204:207], v[8:11]\n"
"s_add_u32 m0, s52, 7936\n"
"ds_read_b64_tr_b16 a[156:157], v241 offset:31744\n"
"ds_read_b64_tr_b16 a[158:159], v241 offset:32000\n"
"v_mfma_f32_16x16x32_bf16 v[56:59], a[164:167], v[208:211], v[56:59]\n"
"buffer_load_dwordx4 v242, s[36:39], s54 offen offset:256 sc0 nt lds\n"
"v_mfma_f32_16x16x32_bf16 v[56:59], a[160:163], v[196:199], v[56:59]\n"
"v_subrev_u32 v248, s57, v234\n"
"v_max_u32 v238, v238, v248\n"
"v_mfma_f32_16x16x32_bf16 v[104:107], a[160:163], v[200:203], v[104:107]\n"
"v_rcp_f32 v234, v234\n"
"v_mfma_f32_16x16x32_bf16 v[104:107], a[164:167], v[212:215], v[104:107]\n"
"s_waitcnt lgkmcnt(4)\n"
"v_exp_f32 v172, v172\n"
"v_mfma_f32_16x16x32_bf16 v[12:15], a[144:147], v[192:195], v[12:15]\n"
"s_mov_b32 s59, 221184\n"
"ds_read_b64_tr_b16 a[160:161], v241 offset:28704\n"
"ds_read_b64_tr_b16 a[162:163], v241 offset:28960\n"
"v_mfma_f32_16x16x32_bf16 v[12:15], a[148:151], v[204:207], v[12:15]\n"
"s_add_u32 m0, s52, 21504\n"
"ds_read_b64_tr_b16 a[164:165], v241 offset:31776\n"
"ds_read_b64_tr_b16 a[166:167], v241 offset:32032\n"
"v_mfma_f32_16x16x32_bf16 v[60:63], a[148:151], v[208:211], v[60:63]\n"
"buffer_load_dwordx4 v242, s[40:43], s59 offen offset:0 sc0 nt lds\n"
"v_mfma_f32_16x16x32_bf16 v[60:63], a[144:147], v[196:199], v[60:63]\n"
"v_exp_f32 v173, v173\n"
"v_mfma_f32_16x16x32_bf16 v[108:111], a[144:147], v[200:203], v[108:111]\n"
"v_exp_f32 v174, v174\n"
"v_mfma_f32_16x16x32_bf16 v[108:111], a[148:151], v[212:215], v[108:111]\n"
"s_waitcnt lgkmcnt(4)\n"
"v_exp_f32 v175, v175\n"
"v_mfma_f32_16x16x32_bf16 v[16:19], a[152:155], v[192:195], v[16:19]\n"
"ds_read_b64_tr_b16 a[144:145], v241 offset:29184\n"
"ds_read_b64_tr_b16 a[146:147], v241 offset:29440\n"
"v_mfma_f32_16x16x32_bf16 v[16:19], a[156:159], v[204:207], v[16:19]\n"
"s_add_u32 m0, s52, 22400\n"
"ds_read_b64_tr_b16 a[148:149], v241 offset:32256\n"
"ds_read_b64_tr_b16 a[150:151], v241 offset:32512\n"
"v_mfma_f32_16x16x32_bf16 v[64:67], a[156:159], v[208:211], v[64:67]\n"
"buffer_load_dwordx4 v242, s[40:43], s59 offen offset:128 sc0 nt lds\n"
"v_mfma_f32_16x16x32_bf16 v[64:67], a[152:155], v[196:199], v[64:67]\n"
"v_mul_f32 v177, s55, v177\n"
"v_mul_f32 v178, s55, v178\n"
"v_mfma_f32_16x16x32_bf16 v[112:115], a[152:155], v[200:203], v[112:115]\n"
"v_mul_f32 v179, s55, v179\n"
"v_mul_f32 v0, v232, v0\n"
"v_mfma_f32_16x16x32_bf16 v[112:115], a[156:159], v[212:215], v[112:115]\n"
"s_waitcnt lgkmcnt(4)\n"
"v_exp_f32 v176, v176\n"
"v_mfma_f32_16x16x32_bf16 v[20:23], a[160:163], v[192:195], v[20:23]\n"
"ds_read_b64_tr_b16 a[152:153], v241 offset:29216\n"
"ds_read_b64_tr_b16 a[154:155], v241 offset:29472\n"
"v_mfma_f32_16x16x32_bf16 v[20:23], a[164:167], v[204:207], v[20:23]\n"
"s_add_u32 m0, s52, 23296\n"
"ds_read_b64_tr_b16 a[156:157], v241 offset:32288\n"
"ds_read_b64_tr_b16 a[158:159], v241 offset:32544\n"
"v_mfma_f32_16x16x32_bf16 v[68:71], a[164:167], v[208:211], v[68:71]\n"
"buffer_load_dwordx4 v242, s[40:43], s59 offen offset:256 sc0 nt lds\n"
"v_mfma_f32_16x16x32_bf16 v[68:71], a[160:163], v[196:199], v[68:71]\n"
"v_exp_f32 v177, v177\n"
"v_mfma_f32_16x16x32_bf16 v[116:119], a[160:163], v[200:203], v[116:119]\n"
"v_exp_f32 v178, v178\n"
"v_mfma_f32_16x16x32_bf16 v[116:119], a[164:167], v[212:215], v[116:119]\n"
"s_waitcnt lgkmcnt(4)\n"
"v_exp_f32 v179, v179\n"
"v_mfma_f32_16x16x32_bf16 v[24:27], a[144:147], v[192:195], v[24:27]\n"
"s_mov_b32 s70, 233472\n"
"ds_read_b64_tr_b16 a[160:161], v241 offset:29696\n"
"ds_read_b64_tr_b16 a[162:163], v241 offset:29952\n"
"v_mfma_f32_16x16x32_bf16 v[24:27], a[148:151], v[204:207], v[24:27]\n"
"s_add_u32 m0, s52, 24576\n"
"ds_read_b64_tr_b16 a[164:165], v241 offset:32768\n"
"ds_read_b64_tr_b16 a[166:167], v241 offset:33024\n"
"v_mfma_f32_16x16x32_bf16 v[72:75], a[148:151], v[208:211], v[72:75]\n"
"buffer_load_dwordx4 v242, s[40:43], s70 offen offset:0 sc0 nt lds\n"
"v_mfma_f32_16x16x32_bf16 v[72:75], a[144:147], v[196:199], v[72:75]\n"
"v_mul_f32 v1, v232, v1\n"
"v_mul_f32 v2, v232, v2\n"
"v_mfma_f32_16x16x32_bf16 v[120:123], a[144:147], v[200:203], v[120:123]\n"
"v_mul_f32 v3, v232, v3\n"
"v_mul_f32 v4, v232, v4\n"
"v_mfma_f32_16x16x32_bf16 v[120:123], a[148:151], v[212:215], v[120:123]\n"
"s_waitcnt lgkmcnt(4)\n"
"v_mul_f32 v5, v232, v5\n"
"v_mul_f32 v6, v232, v6\n"
"v_mfma_f32_16x16x32_bf16 v[28:31], a[152:155], v[192:195], v[28:31]\n"
"ds_read_b64_tr_b16 a[144:145], v241 offset:29728\n"
"ds_read_b64_tr_b16 a[146:147], v241 offset:29984\n"
"v_mfma_f32_16x16x32_bf16 v[28:31], a[156:159], v[204:207], v[28:31]\n"
"s_add_u32 m0, s52, 25472\n"
"ds_read_b64_tr_b16 a[148:149], v241 offset:32800\n"
"ds_read_b64_tr_b16 a[150:151], v241 offset:33056\n"
"v_mfma_f32_16x16x32_bf16 v[76:79], a[156:159], v[208:211], v[76:79]\n"
"buffer_load_dwordx4 v242, s[40:43], s70 offen offset:128 sc0 nt lds\n"
"v_mfma_f32_16x16x32_bf16 v[76:79], a[152:155], v[196:199], v[76:79]\n"
"v_mul_f32 v7, v232, v7\n"
"v_cvt_pk_bf16_f32 v0, v0, v1\n"
"v_mfma_f32_16x16x32_bf16 v[124:127], a[152:155], v[200:203], v[124:127]\n"
"v_cvt_pk_bf16_f32 v1, v2, v3\n"
"v_cvt_pk_bf16_f32 v2, v4, v5\n"
"v_mfma_f32_16x16x32_bf16 v[124:127], a[156:159], v[212:215], v[124:127]\n"
"s_waitcnt lgkmcnt(4)\n"
"v_cvt_pk_bf16_f32 v3, v6, v7\n"
"v_mul_f32 v180, s55, v180\n"
"v_mfma_f32_16x16x32_bf16 v[32:35], a[160:163], v[192:195], v[32:35]\n"
"ds_read_b64_tr_b16 a[152:153], v241 offset:30208\n"
"ds_read_b64_tr_b16 a[154:155], v241 offset:30464\n"
"v_mfma_f32_16x16x32_bf16 v[32:35], a[164:167], v[204:207], v[32:35]\n"
"s_add_u32 m0, s52, 26368\n"
"ds_read_b64_tr_b16 a[156:157], v241 offset:33280\n"
"ds_read_b64_tr_b16 a[158:159], v241 offset:33536\n"
"v_mfma_f32_16x16x32_bf16 v[80:83], a[164:167], v[208:211], v[80:83]\n"
"buffer_load_dwordx4 v242, s[40:43], s70 offen offset:256 sc0 nt lds\n"
"v_mfma_f32_16x16x32_bf16 v[80:83], a[160:163], v[196:199], v[80:83]\n"
"v_permlane16_swap_b32 v0, v2\n"
"v_mfma_f32_16x16x32_bf16 v[128:131], a[160:163], v[200:203], v[128:131]\n"
"v_permlane16_swap_b32 v1, v3\n"
"v_mfma_f32_16x16x32_bf16 v[128:131], a[164:167], v[212:215], v[128:131]\n"
"s_waitcnt lgkmcnt(4)\n"
"v_mul_f32 v181, s55, v181\n"
"v_mul_f32 v182, s55, v182\n"
"v_mfma_f32_16x16x32_bf16 v[36:39], a[144:147], v[192:195], v[36:39]\n"
"ds_read_b64_tr_b16 a[160:161], v241 offset:30240\n"
"ds_read_b64_tr_b16 a[162:163], v241 offset:30496\n"
"v_mfma_f32_16x16x32_bf16 v[36:39], a[148:151], v[204:207], v[36:39]\n"
"ds_read_b64_tr_b16 a[164:165], v241 offset:33312\n"
"ds_read_b64_tr_b16 a[166:167], v241 offset:33568\n"
"v_mfma_f32_16x16x32_bf16 v[84:87], a[148:151], v[208:211], v[84:87]\n"
"v_mul_f32 v183, s55, v183\n"
"v_mul_f32 v184, s55, v184\n"
"v_mfma_f32_16x16x32_bf16 v[84:87], a[144:147], v[196:199], v[84:87]\n"
"v_exp_f32 v180, v180\n"
"v_mfma_f32_16x16x32_bf16 v[132:135], a[144:147], v[200:203], v[132:135]\n"
"v_exp_f32 v181, v181\n"
"v_mfma_f32_16x16x32_bf16 v[132:135], a[148:151], v[212:215], v[132:135]\n"
"s_waitcnt lgkmcnt(4)\n"
"v_exp_f32 v182, v182\n"
"v_mfma_f32_16x16x32_bf16 v[40:43], a[152:155], v[192:195], v[40:43]\n"
"ds_read_b128 a[144:147], v240 offset:12288\n"
"ds_read_b128 a[148:151], v240 offset:12800\n"
"v_mfma_f32_16x16x32_bf16 v[40:43], a[156:159], v[204:207], v[40:43]\n"
"v_exp_f32 v183, v183\n"
"v_mfma_f32_16x16x32_bf16 v[88:91], a[156:159], v[208:211], v[88:91]\n"
"v_mul_f32 v185, s55, v185\n"
"v_mul_f32 v186, s55, v186\n"
"v_mfma_f32_16x16x32_bf16 v[88:91], a[152:155], v[196:199], v[88:91]\n"
"v_mul_f32 v187, s55, v187\n"
"v_mul_f32 v188, s55, v188\n"
"v_mfma_f32_16x16x32_bf16 v[136:139], a[152:155], v[200:203], v[136:139]\n"
"v_exp_f32 v184, v184\n"
"v_mfma_f32_16x16x32_bf16 v[136:139], a[156:159], v[212:215], v[136:139]\n"
"s_waitcnt lgkmcnt(2)\n"
"v_exp_f32 v185, v185\n"
"v_mfma_f32_16x16x32_bf16 v[44:47], a[160:163], v[192:195], v[44:47]\n"
"ds_read_b128 a[152:155], v240 offset:13312\n"
"ds_read_b128 a[156:159], v240 offset:13824\n"
"v_mfma_f32_16x16x32_bf16 v[44:47], a[164:167], v[204:207], v[44:47]\n"
"v_cvt_pk_bf16_f32 v192, v144, v145\n"
"v_cvt_pk_bf16_f32 v193, v146, v147\n"
"v_mfma_f32_16x16x32_bf16 v[92:95], a[164:167], v[208:211], v[92:95]\n"
"v_cvt_pk_bf16_f32 v194, v156, v157\n"
"v_cvt_pk_bf16_f32 v195, v158, v159\n"
"v_mfma_f32_16x16x32_bf16 v[92:95], a[160:163], v[196:199], v[92:95]\n"
"v_cvt_pk_bf16_f32 v196, v148, v149\n"
"v_cvt_pk_bf16_f32 v197, v150, v151\n"
"v_mfma_f32_16x16x32_bf16 v[140:143], a[160:163], v[200:203], v[140:143]\n"
"v_cvt_pk_bf16_f32 v200, v152, v153\n"
"v_cvt_pk_bf16_f32 v201, v154, v155\n"
"v_mfma_f32_16x16x32_bf16 v[140:143], a[164:167], v[212:215], v[140:143]\n"
"s_waitcnt lgkmcnt(2)\n"
"v_cvt_pk_bf16_f32 v198, v160, v161\n"
"v_cvt_pk_bf16_f32 v199, v162, v163\n"
"v_mfma_f32_16x16x32_bf16 v[144:147], a[144:147], a[72:75], 0\n"
"ds_read_b128 a[160:163], v240 offset:14336\n"
"ds_read_b128 a[164:167], v240 offset:14848\n"
"v_mfma_f32_16x16x32_bf16 v[144:147], a[148:151], a[76:79], v[144:147]\n"
"v_cvt_pk_bf16_f32 v202, v164, v165\n"
"v_cvt_pk_bf16_f32 v203, v166, v167\n"
"v_mfma_f32_16x16x32_bf16 v[148:151], a[148:151], a[100:103], 0\n"
"v_cvt_pk_bf16_f32 v204, v168, v169\n"
"v_cvt_pk_bf16_f32 v205, v170, v171\n"
"v_mfma_f32_16x16x32_bf16 v[148:151], a[144:147], a[96:99], v[148:151]\n"
"v_cvt_pk_bf16_f32 v208, v172, v173\n"
"v_cvt_pk_bf16_f32 v209, v174, v175\n"
"v_mfma_f32_16x16x32_bf16 v[152:155], a[144:147], a[120:123], 0\n"
"v_cvt_pk_bf16_f32 v212, v176, v177\n"
"v_cvt_pk_bf16_f32 v213, v178, v179\n"
"v_mfma_f32_16x16x32_bf16 v[152:155], a[148:151], a[124:127], v[152:155]\n"
"s_waitcnt lgkmcnt(2)\n"
"v_cvt_pk_bf16_f32 v206, v180, v181\n"
"v_cvt_pk_bf16_f32 v207, v182, v183\n"
"v_mfma_f32_16x16x32_bf16 v[144:147], a[152:155], a[80:83], v[144:147]\n"
"ds_read_b128 a[144:147], v240 offset:12544\n"
"ds_read_b128 a[148:151], v240 offset:13056\n"
"v_mfma_f32_16x16x32_bf16 v[144:147], a[156:159], a[84:87], v[144:147]\n"
"v_exp_f32 v186, v186\n"
"v_mfma_f32_16x16x32_bf16 v[148:151], a[156:159], a[108:111], v[148:151]\n"
"v_exp_f32 v187, v187\n"
"v_mfma_f32_16x16x32_bf16 v[148:151], a[152:155], a[104:107], v[148:151]\n"
"v_mul_f32 v189, s55, v189\n"
"v_mul_f32 v190, s55, v190\n"
"v_mfma_f32_16x16x32_bf16 v[152:155], a[152:155], a[128:131], v[152:155]\n"
"v_mul_f32 v191, s55, v191\n"
"v_cvt_pk_bf16_f32 v210, v184, v185\n"
"v_mfma_f32_16x16x32_bf16 v[152:155], a[156:159], a[132:135], v[152:155]\n"
"s_waitcnt lgkmcnt(2)\n"
"v_cvt_pk_bf16_f32 v211, v186, v187\n"
"v_mul_f32 v8, v232, v8\n"
"v_mfma_f32_16x16x32_bf16 v[144:147], a[160:163], a[88:91], v[144:147]\n"
"ds_read_b128 a[152:155], v240 offset:13568\n"
"ds_read_b128 a[156:159], v240 offset:14080\n"
"v_mfma_f32_16x16x32_bf16 v[144:147], a[164:167], a[92:95], v[144:147]\n"
"v_exp_f32 v188, v188\n"
"v_mfma_f32_16x16x32_bf16 v[148:151], a[164:167], a[116:119], v[148:151]\n"
"v_exp_f32 v189, v189\n"
"v_mfma_f32_16x16x32_bf16 v[148:151], a[160:163], a[112:115], v[148:151]\n"
"v_exp_f32 v190, v190\n"
"v_mfma_f32_16x16x32_bf16 v[152:155], a[160:163], a[136:139], v[152:155]\n"
"v_exp_f32 v191, v191\n"
"v_mfma_f32_16x16x32_bf16 v[152:155], a[164:167], a[140:143], v[152:155]\n"
"s_waitcnt lgkmcnt(2)\n"
"v_cvt_pk_bf16_f32 v214, v188, v189\n"
"v_cvt_pk_bf16_f32 v215, v190, v191\n"
"v_mfma_f32_16x16x32_bf16 v[156:159], a[144:147], a[72:75], 0\n"
"ds_read_b128 a[160:163], v240 offset:14592\n"
"ds_read_b128 a[164:167], v240 offset:15104\n"
"v_mfma_f32_16x16x32_bf16 v[156:159], a[148:151], a[76:79], v[156:159]\n"
"v_mul_f32 v9, v232, v9\n"
"v_mul_f32 v10, v232, v10\n"
"v_mfma_f32_16x16x32_bf16 v[160:163], a[148:151], a[100:103], 0\n"
"v_mul_f32 v11, v232, v11\n"
"v_mul_f32 v12, v232, v12\n"
"v_mfma_f32_16x16x32_bf16 v[160:163], a[144:147], a[96:99], v[160:163]\n"
"v_mul_f32 v13, v232, v13\n"
"v_mul_f32 v14, v232, v14\n"
"v_mfma_f32_16x16x32_bf16 v[164:167], a[144:147], a[120:123], 0\n"
"v_mul_f32 v15, v232, v15\n"
"v_cvt_pk_bf16_f32 v8, v8, v9\n"
"v_mfma_f32_16x16x32_bf16 v[164:167], a[148:151], a[124:127], v[164:167]\n"
"s_waitcnt lgkmcnt(2)\n"
"v_cvt_pk_bf16_f32 v9, v10, v11\n"
"v_cvt_pk_bf16_f32 v10, v12, v13\n"
"v_mfma_f32_16x16x32_bf16 v[156:159], a[152:155], a[80:83], v[156:159]\n"
"ds_read_b128 a[144:147], v240 offset:15360\n"
"ds_read_b128 a[148:151], v240 offset:15872\n"
"v_mfma_f32_16x16x32_bf16 v[156:159], a[156:159], a[84:87], v[156:159]\n"
"v_cvt_pk_bf16_f32 v11, v14, v15\n"
"v_mul_f32 v16, v232, v16\n"
"v_mfma_f32_16x16x32_bf16 v[160:163], a[156:159], a[108:111], v[160:163]\n"
"v_mul_f32 v17, v232, v17\n"
"v_mul_f32 v18, v232, v18\n"
"v_mfma_f32_16x16x32_bf16 v[160:163], a[152:155], a[104:107], v[160:163]\n"
"v_permlane16_swap_b32 v8, v10\n"
"v_mfma_f32_16x16x32_bf16 v[164:167], a[152:155], a[128:131], v[164:167]\n"
"v_permlane16_swap_b32 v9, v11\n"
"v_mfma_f32_16x16x32_bf16 v[164:167], a[156:159], a[132:135], v[164:167]\n"
"s_waitcnt lgkmcnt(2)\n"
"v_mul_f32 v19, v232, v19\n"
"v_mul_f32 v20, v232, v20\n"
"v_mfma_f32_16x16x32_bf16 v[156:159], a[160:163], a[88:91], v[156:159]\n"
"ds_read_b128 a[152:155], v240 offset:16384\n"
"ds_read_b128 a[156:159], v240 offset:16896\n"
"v_mfma_f32_16x16x32_bf16 v[156:159], a[164:167], a[92:95], v[156:159]\n"
"v_mul_f32 v21, v232, v21\n"
"v_mul_f32 v22, v232, v22\n"
"v_mfma_f32_16x16x32_bf16 v[160:163], a[164:167], a[116:119], v[160:163]\n"
"v_mul_f32 v23, v232, v23\n"
"v_cvt_pk_bf16_f32 v16, v16, v17\n"
"v_mfma_f32_16x16x32_bf16 v[160:163], a[160:163], a[112:115], v[160:163]\n"
"v_cvt_pk_bf16_f32 v17, v18, v19\n"
"v_cvt_pk_bf16_f32 v18, v20, v21\n"
"v_mfma_f32_16x16x32_bf16 v[164:167], a[160:163], a[136:139], v[164:167]\n"
"v_cvt_pk_bf16_f32 v19, v22, v23\n"
"v_mul_f32 v144, s55, v144\n"
"v_mfma_f32_16x16x32_bf16 v[164:167], a[164:167], a[140:143], v[164:167]\n"
"s_waitcnt lgkmcnt(2)\n"
"v_mul_f32 v145, s55, v145\n"
"v_mul_f32 v146, s55, v146\n"
"v_mfma_f32_16x16x32_bf16 v[168:171], a[144:147], a[72:75], 0\n"
"ds_read_b128 a[160:163], v240 offset:17408\n"
"ds_read_b128 a[164:167], v240 offset:17920\n"
"v_mfma_f32_16x16x32_bf16 v[168:171], a[148:151], a[76:79], v[168:171]\n"
"v_permlane16_swap_b32 v16, v18\n"
"v_mfma_f32_16x16x32_bf16 v[172:175], a[148:151], a[100:103], 0\n"
"v_permlane16_swap_b32 v17, v19\n"
"v_mfma_f32_16x16x32_bf16 v[172:175], a[144:147], a[96:99], v[172:175]\n"
"v_mul_f32 v147, s55, v147\n"
"v_mul_f32 v148, s55, v148\n"
"v_mfma_f32_16x16x32_bf16 v[176:179], a[144:147], a[120:123], 0\n"
"v_exp_f32 v144, v144\n"
"v_mfma_f32_16x16x32_bf16 v[176:179], a[148:151], a[124:127], v[176:179]\n"
"s_waitcnt lgkmcnt(2)\n"
"v_exp_f32 v145, v145\n"
"v_mfma_f32_16x16x32_bf16 v[168:171], a[152:155], a[80:83], v[168:171]\n"
"ds_read_b128 a[144:147], v240 offset:15616\n"
"ds_read_b128 a[148:151], v240 offset:16128\n"
"v_mfma_f32_16x16x32_bf16 v[168:171], a[156:159], a[84:87], v[168:171]\n"
"v_exp_f32 v146, v146\n"
"v_mfma_f32_16x16x32_bf16 v[172:175], a[156:159], a[108:111], v[172:175]\n"
"v_exp_f32 v147, v147\n"
"v_mfma_f32_16x16x32_bf16 v[172:175], a[152:155], a[104:107], v[172:175]\n"
"v_mul_f32 v149, s55, v149\n"
"v_mul_f32 v150, s55, v150\n"
"v_mfma_f32_16x16x32_bf16 v[176:179], a[152:155], a[128:131], v[176:179]\n"
"v_mul_f32 v151, s55, v151\n"
"v_mul_f32 v152, s55, v152\n"
"v_mfma_f32_16x16x32_bf16 v[176:179], a[156:159], a[132:135], v[176:179]\n"
"s_waitcnt lgkmcnt(2)\n"
"v_exp_f32 v148, v148\n"
"v_mfma_f32_16x16x32_bf16 v[168:171], a[160:163], a[88:91], v[168:171]\n"
"ds_read_b128 a[152:155], v240 offset:16640\n"
"ds_read_b128 a[156:159], v240 offset:17152\n"
"v_mfma_f32_16x16x32_bf16 v[168:171], a[164:167], a[92:95], v[168:171]\n"
"v_exp_f32 v149, v149\n"
"v_mfma_f32_16x16x32_bf16 v[172:175], a[164:167], a[116:119], v[172:175]\n"
"v_exp_f32 v150, v150\n"
"v_mfma_f32_16x16x32_bf16 v[172:175], a[160:163], a[112:115], v[172:175]\n"
"v_exp_f32 v151, v151\n"
"v_mfma_f32_16x16x32_bf16 v[176:179], a[160:163], a[136:139], v[176:179]\n"
"v_mul_f32 v153, s55, v153\n"
"v_mul_f32 v154, s55, v154\n"
"v_mfma_f32_16x16x32_bf16 v[176:179], a[164:167], a[140:143], v[176:179]\n"
"s_waitcnt lgkmcnt(2)\n"
"v_mul_f32 v155, s55, v155\n"
"v_mul_f32 v156, s55, v156\n"
"v_mfma_f32_16x16x32_bf16 v[180:183], a[144:147], a[72:75], 0\n"
"ds_read_b128 a[160:163], v240 offset:17664\n"
"ds_read_b128 a[164:167], v240 offset:18176\n"
"v_mfma_f32_16x16x32_bf16 v[180:183], a[148:151], a[76:79], v[180:183]\n"
"v_exp_f32 v152, v152\n"
"v_mfma_f32_16x16x32_bf16 v[184:187], a[148:151], a[100:103], 0\n"
"v_exp_f32 v153, v153\n"
"v_mfma_f32_16x16x32_bf16 v[184:187], a[144:147], a[96:99], v[184:187]\n"
"s_waitcnt vmcnt(12)\n"
"v_exp_f32 v154, v154\n"
"v_mfma_f32_16x16x32_bf16 v[188:191], a[144:147], a[120:123], 0\n"
"s_barrier\n"
"v_exp_f32 v155, v155\n"
"v_mfma_f32_16x16x32_bf16 v[188:191], a[148:151], a[124:127], v[188:191]\n"
"s_waitcnt lgkmcnt(2)\n"
"v_mul_f32 v157, s55, v157\n"
"v_mul_f32 v158, s55, v158\n"
"v_mfma_f32_16x16x32_bf16 v[180:183], a[152:155], a[80:83], v[180:183]\n"
"s_mov_b32 s53, 294912\n"
"ds_read_b64_tr_b16 a[144:145], v241 offset:33792\n"
"ds_read_b64_tr_b16 a[146:147], v241 offset:34048\n"
"v_mfma_f32_16x16x32_bf16 v[180:183], a[156:159], a[84:87], v[180:183]\n"
"s_add_u32 m0, s52, 9216\n"
"ds_read_b64_tr_b16 a[148:149], v241 offset:36864\n"
"ds_read_b64_tr_b16 a[150:151], v241 offset:37120\n"
"v_mfma_f32_16x16x32_bf16 v[184:187], a[156:159], a[108:111], v[184:187]\n"
"buffer_load_dwordx4 v242, s[36:39], s53 offen offset:0 sc0 nt lds\n"
"v_mfma_f32_16x16x32_bf16 v[184:187], a[152:155], a[104:107], v[184:187]\n"
"v_mul_f32 v159, s55, v159\n"
"v_mul_f32 v160, s55, v160\n"
"v_mfma_f32_16x16x32_bf16 v[188:191], a[152:155], a[128:131], v[188:191]\n"
"v_exp_f32 v156, v156\n"
"v_mfma_f32_16x16x32_bf16 v[188:191], a[156:159], a[132:135], v[188:191]\n"
"s_waitcnt lgkmcnt(4)\n"
"v_exp_f32 v157, v157\n"
"v_mfma_f32_16x16x32_bf16 v[180:183], a[160:163], a[88:91], v[180:183]\n"
"ds_read_b64_tr_b16 a[152:153], v241 offset:33824\n"
"ds_read_b64_tr_b16 a[154:155], v241 offset:34080\n"
"v_mfma_f32_16x16x32_bf16 v[180:183], a[164:167], a[92:95], v[180:183]\n"
"s_add_u32 m0, s52, 10112\n"
"ds_read_b64_tr_b16 a[156:157], v241 offset:36896\n"
"ds_read_b64_tr_b16 a[158:159], v241 offset:37152\n"
"v_mfma_f32_16x16x32_bf16 v[184:187], a[164:167], a[116:119], v[184:187]\n"
"buffer_load_dwordx4 v242, s[36:39], s53 offen offset:128 sc0 nt lds\n"
"v_mfma_f32_16x16x32_bf16 v[184:187], a[160:163], a[112:115], v[184:187]\n"
"v_exp_f32 v158, v158\n"
"v_mfma_f32_16x16x32_bf16 v[188:191], a[160:163], a[136:139], v[188:191]\n"
"v_exp_f32 v159, v159\n"
"v_mfma_f32_16x16x32_bf16 v[188:191], a[164:167], a[140:143], v[188:191]\n"
"v_mul_f32 v161, s55, v161\n"
"v_mul_f32 v162, s55, v162\n"
"v_mfma_f32_16x16x32_bf16 a[240:243], a[252:255], v[192:195], 0\n"
"v_mul_f32 v163, s55, v163\n"
"v_mul_f32 v164, s55, v164\n"
"v_mfma_f32_16x16x32_bf16 a[240:243], a[252:255], v[204:207], a[240:243]\n"
"s_add_u32 m0, s52, 11008\n"
"v_exp_f32 v160, v160\n"
"v_mfma_f32_16x16x32_bf16 a[244:247], a[252:255], v[196:199], 0\n"
"buffer_load_dwordx4 v242, s[36:39], s53 offen offset:256 sc0 nt lds\n"
"v_mfma_f32_16x16x32_bf16 a[244:247], a[252:255], v[208:211], a[244:247]\n"
"v_exp_f32 v161, v161\n"
"v_mfma_f32_16x16x32_bf16 a[248:251], a[252:255], v[200:203], 0\n"
"v_exp_f32 v162, v162\n"
"v_mfma_f32_16x16x32_bf16 a[248:251], a[252:255], v[212:215], a[248:251]\n"
"s_waitcnt lgkmcnt(4)\n"
"v_exp_f32 v163, v163\n"
"v_mfma_f32_16x16x32_bf16 a[0:3], a[144:147], v[192:195], 0\n"
"s_mov_b32 s54, 307200\n"
"ds_read_b64_tr_b16 a[160:161], v241 offset:34304\n"
"ds_read_b64_tr_b16 a[162:163], v241 offset:34560\n"
"v_mfma_f32_16x16x32_bf16 a[0:3], a[148:151], v[204:207], a[0:3]\n"
"s_add_u32 m0, s52, 12288\n"
"ds_read_b64_tr_b16 a[164:165], v241 offset:37376\n"
"ds_read_b64_tr_b16 a[166:167], v241 offset:37632\n"
"v_mfma_f32_16x16x32_bf16 a[48:51], a[148:151], v[208:211], 0\n"
"buffer_load_dwordx4 v242, s[36:39], s54 offen offset:0 sc0 nt lds\n"
"v_mfma_f32_16x16x32_bf16 a[48:51], a[144:147], v[196:199], a[48:51]\n"
"v_mul_f32 v165, s55, v165\n"
"v_mul_f32 v166, s55, v166\n"
"v_mfma_f32_16x16x32_bf16 a[192:195], a[144:147], v[200:203], 0\n"
"v_mul_f32 v167, s55, v167\n"
"v_mul_f32 v24, v232, v24\n"
"v_mfma_f32_16x16x32_bf16 a[192:195], a[148:151], v[212:215], a[192:195]\n"
"s_waitcnt lgkmcnt(4)\n"
"v_exp_f32 v164, v164\n"
"v_mfma_f32_16x16x32_bf16 a[4:7], a[152:155], v[192:195], 0\n"
"ds_read_b64_tr_b16 a[144:145], v241 offset:34336\n"
"ds_read_b64_tr_b16 a[146:147], v241 offset:34592\n"
"v_mfma_f32_16x16x32_bf16 a[4:7], a[156:159], v[204:207], a[4:7]\n"
"s_add_u32 m0, s52, 13184\n"
"ds_read_b64_tr_b16 a[148:149], v241 offset:37408\n"
"ds_read_b64_tr_b16 a[150:151], v241 offset:37664\n"
"v_mfma_f32_16x16x32_bf16 a[52:55], a[156:159], v[208:211], 0\n"
"buffer_load_dwordx4 v242, s[36:39], s54 offen offset:128 sc0 nt lds\n"
"v_mfma_f32_16x16x32_bf16 a[52:55], a[152:155], v[196:199], a[52:55]\n"
"v_exp_f32 v165, v165\n"
"v_mfma_f32_16x16x32_bf16 a[196:199], a[152:155], v[200:203], 0\n"
"v_exp_f32 v166, v166\n"
"v_mfma_f32_16x16x32_bf16 a[196:199], a[156:159], v[212:215], a[196:199]\n"
"s_waitcnt lgkmcnt(4)\n"
"v_exp_f32 v167, v167\n"
"v_mfma_f32_16x16x32_bf16 a[8:11], a[160:163], v[192:195], 0\n"
"ds_read_b64_tr_b16 a[152:153], v241 offset:34816\n"
"ds_read_b64_tr_b16 a[154:155], v241 offset:35072\n"
"v_mfma_f32_16x16x32_bf16 a[8:11], a[164:167], v[204:207], a[8:11]\n"
"s_add_u32 m0, s52, 14080\n"
"ds_read_b64_tr_b16 a[156:157], v241 offset:37888\n"
"ds_read_b64_tr_b16 a[158:159], v241 offset:38144\n"
"v_mfma_f32_16x16x32_bf16 a[56:59], a[164:167], v[208:211], 0\n"
"buffer_load_dwordx4 v242, s[36:39], s54 offen offset:256 sc0 nt lds\n"
"v_mfma_f32_16x16x32_bf16 a[56:59], a[160:163], v[196:199], a[56:59]\n"
"v_mul_f32 v25, v232, v25\n"
"v_mul_f32 v26, v232, v26\n"
"v_mfma_f32_16x16x32_bf16 a[200:203], a[160:163], v[200:203], 0\n"
"v_mul_f32 v27, v232, v27\n"
"v_mul_f32 v28, v232, v28\n"
"v_mfma_f32_16x16x32_bf16 a[200:203], a[164:167], v[212:215], a[200:203]\n"
"s_waitcnt lgkmcnt(4)\n"
"v_mul_f32 v29, v232, v29\n"
"v_mul_f32 v30, v232, v30\n"
"v_mfma_f32_16x16x32_bf16 a[12:15], a[144:147], v[192:195], 0\n"
"s_mov_b32 s59, 245760\n"
"ds_read_b64_tr_b16 a[160:161], v241 offset:34848\n"
"ds_read_b64_tr_b16 a[162:163], v241 offset:35104\n"
"v_mfma_f32_16x16x32_bf16 a[12:15], a[148:151], v[204:207], a[12:15]\n"
"s_add_u32 m0, s52, 27648\n"
"ds_read_b64_tr_b16 a[164:165], v241 offset:37920\n"
"ds_read_b64_tr_b16 a[166:167], v241 offset:38176\n"
"v_mfma_f32_16x16x32_bf16 a[60:63], a[148:151], v[208:211], 0\n"
"buffer_load_dwordx4 v242, s[40:43], s59 offen offset:0 sc0 nt lds\n"
"v_mfma_f32_16x16x32_bf16 a[60:63], a[144:147], v[196:199], a[60:63]\n"
"v_mul_f32 v31, v232, v31\n"
"v_mul_f32 v168, s55, v168\n"
"v_mfma_f32_16x16x32_bf16 a[204:207], a[144:147], v[200:203], 0\n"
"v_mul_f32 v169, s55, v169\n"
"v_mul_f32 v170, s55, v170\n"
"v_mfma_f32_16x16x32_bf16 a[204:207], a[148:151], v[212:215], a[204:207]\n"
"s_waitcnt lgkmcnt(4)\n"
"v_mul_f32 v171, s55, v171\n"
"v_cvt_pk_bf16_f32 v24, v24, v25\n"
"v_mfma_f32_16x16x32_bf16 a[16:19], a[152:155], v[192:195], 0\n"
"ds_read_b64_tr_b16 a[144:145], v241 offset:35328\n"
"ds_read_b64_tr_b16 a[146:147], v241 offset:35584\n"
"v_mfma_f32_16x16x32_bf16 a[16:19], a[156:159], v[204:207], a[16:19]\n"
"s_add_u32 m0, s52, 28544\n"
"ds_read_b64_tr_b16 a[148:149], v241 offset:38400\n"
"ds_read_b64_tr_b16 a[150:151], v241 offset:38656\n"
"v_mfma_f32_16x16x32_bf16 a[64:67], a[156:159], v[208:211], 0\n"
"buffer_load_dwordx4 v242, s[40:43], s59 offen offset:128 sc0 nt lds\n"
"v_mfma_f32_16x16x32_bf16 a[64:67], a[152:155], v[196:199], a[64:67]\n"
"v_cvt_pk_bf16_f32 v25, v26, v27\n"
"v_cvt_pk_bf16_f32 v26, v28, v29\n"
"v_mfma_f32_16x16x32_bf16 a[208:211], a[152:155], v[200:203], 0\n"
"v_cvt_pk_bf16_f32 v27, v30, v31\n"
"v_mul_f32 v172, s55, v172\n"
"v_mfma_f32_16x16x32_bf16 a[208:211], a[156:159], v[212:215], a[208:211]\n"
"s_waitcnt lgkmcnt(4)\n"
"v_exp_f32 v168, v168\n"
"v_mfma_f32_16x16x32_bf16 a[20:23], a[160:163], v[192:195], 0\n"
"ds_read_b64_tr_b16 a[152:153], v241 offset:35360\n"
"ds_read_b64_tr_b16 a[154:155], v241 offset:35616\n"
"v_mfma_f32_16x16x32_bf16 a[20:23], a[164:167], v[204:207], a[20:23]\n"
"s_add_u32 m0, s52, 29440\n"
"ds_read_b64_tr_b16 a[156:157], v241 offset:38432\n"
"ds_read_b64_tr_b16 a[158:159], v241 offset:38688\n"
"v_mfma_f32_16x16x32_bf16 a[68:71], a[164:167], v[208:211], 0\n"
"buffer_load_dwordx4 v242, s[40:43], s59 offen offset:256 sc0 nt lds\n"
"v_mfma_f32_16x16x32_bf16 a[68:71], a[160:163], v[196:199], a[68:71]\n"
"v_exp_f32 v169, v169\n"
"v_mfma_f32_16x16x32_bf16 a[212:215], a[160:163], v[200:203], 0\n"
"v_exp_f32 v170, v170\n"
"v_mfma_f32_16x16x32_bf16 a[212:215], a[164:167], v[212:215], a[212:215]\n"
"s_waitcnt lgkmcnt(4)\n"
"v_exp_f32 v171, v171\n"
"v_mfma_f32_16x16x32_bf16 a[24:27], a[144:147], v[192:195], 0\n"
"s_mov_b32 s70, 258048\n"
"ds_read_b64_tr_b16 a[160:161], v241 offset:35840\n"
"ds_read_b64_tr_b16 a[162:163], v241 offset:36096\n"
"v_mfma_f32_16x16x32_bf16 a[24:27], a[148:151], v[204:207], a[24:27]\n"
"s_add_u32 m0, s52, 30720\n"
"ds_read_b64_tr_b16 a[164:165], v241 offset:38912\n"
"ds_read_b64_tr_b16 a[166:167], v241 offset:39168\n"
"v_mfma_f32_16x16x32_bf16 a[168:171], a[148:151], v[208:211], 0\n"
"buffer_load_dwordx4 v242, s[40:43], s70 offen offset:0 sc0 nt lds\n"
"v_mfma_f32_16x16x32_bf16 a[168:171], a[144:147], v[196:199], a[168:171]\n"
"v_mul_f32 v173, s55, v173\n"
"v_mul_f32 v174, s55, v174\n"
"v_mfma_f32_16x16x32_bf16 a[216:219], a[144:147], v[200:203], 0\n"
"v_mul_f32 v175, s55, v175\n"
"v_mul_f32 v176, s55, v176\n"
"v_mfma_f32_16x16x32_bf16 a[216:219], a[148:151], v[212:215], a[216:219]\n"
"s_waitcnt lgkmcnt(4)\n"
"v_permlane16_swap_b32 v24, v26\n"
"v_mfma_f32_16x16x32_bf16 a[28:31], a[152:155], v[192:195], 0\n"
"ds_read_b64_tr_b16 a[144:145], v241 offset:35872\n"
"ds_read_b64_tr_b16 a[146:147], v241 offset:36128\n"
"v_mfma_f32_16x16x32_bf16 a[28:31], a[156:159], v[204:207], a[28:31]\n"
"s_add_u32 m0, s52, 31616\n"
"ds_read_b64_tr_b16 a[148:149], v241 offset:38944\n"
"ds_read_b64_tr_b16 a[150:151], v241 offset:39200\n"
"v_mfma_f32_16x16x32_bf16 a[172:175], a[156:159], v[208:211], 0\n"
"buffer_load_dwordx4 v242, s[40:43], s70 offen offset:128 sc0 nt lds\n"
"v_mfma_f32_16x16x32_bf16 a[172:175], a[152:155], v[196:199], a[172:175]\n"
"v_permlane16_swap_b32 v25, v27\n"
"v_mfma_f32_16x16x32_bf16 a[220:223], a[152:155], v[200:203], 0\n"
"v_exp_f32 v172, v172\n"
"v_mfma_f32_16x16x32_bf16 a[220:223], a[156:159], v[212:215], a[220:223]\n"
"s_waitcnt lgkmcnt(4)\n"
"v_exp_f32 v173, v173\n"
"v_mfma_f32_16x16x32_bf16 a[32:35], a[160:163], v[192:195], 0\n"
"ds_read_b64_tr_b16 a[152:153], v241 offset:36352\n"
"ds_read_b64_tr_b16 a[154:155], v241 offset:36608\n"
"v_mfma_f32_16x16x32_bf16 a[32:35], a[164:167], v[204:207], a[32:35]\n"
"s_add_u32 m0, s52, 32512\n"
"ds_read_b64_tr_b16 a[156:157], v241 offset:39424\n"
"ds_read_b64_tr_b16 a[158:159], v241 offset:39680\n"
"v_mfma_f32_16x16x32_bf16 a[176:179], a[164:167], v[208:211], 0\n"
"buffer_load_dwordx4 v242, s[40:43], s70 offen offset:256 sc0 nt lds\n"
"v_mfma_f32_16x16x32_bf16 a[176:179], a[160:163], v[196:199], a[176:179]\n"
"v_exp_f32 v174, v174\n"
"v_mfma_f32_16x16x32_bf16 a[224:227], a[160:163], v[200:203], 0\n"
"v_exp_f32 v175, v175\n"
"v_mfma_f32_16x16x32_bf16 a[224:227], a[164:167], v[212:215], a[224:227]\n"
"s_waitcnt lgkmcnt(4)\n"
"v_mul_f32 v177, s55, v177\n"
"v_mul_f32 v178, s55, v178\n"
"v_mfma_f32_16x16x32_bf16 a[36:39], a[144:147], v[192:195], 0\n"
"ds_read_b64_tr_b16 a[160:161], v241 offset:36384\n"
"ds_read_b64_tr_b16 a[162:163], v241 offset:36640\n"
"v_mfma_f32_16x16x32_bf16 a[36:39], a[148:151], v[204:207], a[36:39]\n"
"ds_read_b64_tr_b16 a[164:165], v241 offset:39456\n"
"ds_read_b64_tr_b16 a[166:167], v241 offset:39712\n"
"v_mfma_f32_16x16x32_bf16 a[180:183], a[148:151], v[208:211], 0\n"
"v_mul_f32 v179, s55, v179\n"
"v_mul_f32 v180, s55, v180\n"
"v_mfma_f32_16x16x32_bf16 a[180:183], a[144:147], v[196:199], a[180:183]\n"
"v_exp_f32 v176, v176\n"
"v_mfma_f32_16x16x32_bf16 a[228:231], a[144:147], v[200:203], 0\n"
"v_exp_f32 v177, v177\n"
"v_mfma_f32_16x16x32_bf16 a[228:231], a[148:151], v[212:215], a[228:231]\n"
"s_waitcnt lgkmcnt(4)\n"
"v_exp_f32 v178, v178\n"
"v_mfma_f32_16x16x32_bf16 a[40:43], a[152:155], v[192:195], 0\n"
"ds_read_b128 a[144:147], v240 offset:18432\n"
"ds_read_b128 a[148:151], v240 offset:18944\n"
"v_mfma_f32_16x16x32_bf16 a[40:43], a[156:159], v[204:207], a[40:43]\n"
"v_exp_f32 v179, v179\n"
"v_mfma_f32_16x16x32_bf16 a[184:187], a[156:159], v[208:211], 0\n"
"v_mul_f32 v181, s55, v181\n"
"v_mul_f32 v182, s55, v182\n"
"v_mfma_f32_16x16x32_bf16 a[184:187], a[152:155], v[196:199], a[184:187]\n"
"v_mul_f32 v183, s55, v183\n"
"v_mul_f32 v184, s55, v184\n"
"v_mfma_f32_16x16x32_bf16 a[232:235], a[152:155], v[200:203], 0\n"
"v_exp_f32 v180, v180\n"
"v_mfma_f32_16x16x32_bf16 a[232:235], a[156:159], v[212:215], a[232:235]\n"
"s_waitcnt lgkmcnt(2)\n"
"v_exp_f32 v181, v181\n"
"v_mfma_f32_16x16x32_bf16 a[44:47], a[160:163], v[192:195], 0\n"
"ds_read_b128 a[152:155], v240 offset:19456\n"
"ds_read_b128 a[156:159], v240 offset:19968\n"
"v_mfma_f32_16x16x32_bf16 a[44:47], a[164:167], v[204:207], a[44:47]\n"
"v_cvt_pk_bf16_f32 v192, v144, v145\n"
"v_cvt_pk_bf16_f32 v193, v146, v147\n"
"v_mfma_f32_16x16x32_bf16 a[188:191], a[164:167], v[208:211], 0\n"
"v_cvt_pk_bf16_f32 v194, v156, v157\n"
"v_cvt_pk_bf16_f32 v195, v158, v159\n"
"v_mfma_f32_16x16x32_bf16 a[188:191], a[160:163], v[196:199], a[188:191]\n"
"v_cvt_pk_bf16_f32 v196, v148, v149\n"
"v_cvt_pk_bf16_f32 v197, v150, v151\n"
"v_mfma_f32_16x16x32_bf16 a[236:239], a[160:163], v[200:203], 0\n"
"v_cvt_pk_bf16_f32 v200, v152, v153\n"
"v_cvt_pk_bf16_f32 v201, v154, v155\n"
"v_mfma_f32_16x16x32_bf16 a[236:239], a[164:167], v[212:215], a[236:239]\n"
"s_waitcnt lgkmcnt(2)\n"
"v_cvt_pk_bf16_f32 v198, v160, v161\n"
"v_cvt_pk_bf16_f32 v199, v162, v163\n"
"v_mfma_f32_16x16x32_bf16 v[144:147], a[144:147], a[72:75], 0\n"
"ds_read_b128 a[160:163], v240 offset:20480\n"
"ds_read_b128 a[164:167], v240 offset:20992\n"
"v_mfma_f32_16x16x32_bf16 v[144:147], a[148:151], a[76:79], v[144:147]\n"
"v_cvt_pk_bf16_f32 v202, v164, v165\n"
"v_cvt_pk_bf16_f32 v203, v166, v167\n"
"v_mfma_f32_16x16x32_bf16 v[148:151], a[148:151], a[100:103], 0\n"
"v_cvt_pk_bf16_f32 v204, v168, v169\n"
"v_cvt_pk_bf16_f32 v205, v170, v171\n"
"v_mfma_f32_16x16x32_bf16 v[148:151], a[144:147], a[96:99], v[148:151]\n"
"v_cvt_pk_bf16_f32 v208, v172, v173\n"
"v_cvt_pk_bf16_f32 v209, v174, v175\n"
"v_mfma_f32_16x16x32_bf16 v[152:155], a[144:147], a[120:123], 0\n"
"buffer_store_dwordx4 v[0:3], v244, s[48:51], s60 offen offset:0\n"
"v_mfma_f32_16x16x32_bf16 v[152:155], a[148:151], a[124:127], v[152:155]\n"
"s_waitcnt lgkmcnt(2)\n"
"v_cvt_pk_bf16_f32 v212, v176, v177\n"
"v_cvt_pk_bf16_f32 v213, v178, v179\n"
"v_mfma_f32_16x16x32_bf16 v[144:147], a[152:155], a[80:83], v[144:147]\n"
"ds_read_b128 a[144:147], v240 offset:18688\n"
"ds_read_b128 a[148:151], v240 offset:19200\n"
"v_mfma_f32_16x16x32_bf16 v[144:147], a[156:159], a[84:87], v[144:147]\n"
"v_exp_f32 v182, v182\n"
"v_mfma_f32_16x16x32_bf16 v[148:151], a[156:159], a[108:111], v[148:151]\n"
"v_exp_f32 v183, v183\n"
"v_mfma_f32_16x16x32_bf16 v[148:151], a[152:155], a[104:107], v[148:151]\n"
"v_mul_f32 v185, s55, v185\n"
"v_mul_f32 v186, s55, v186\n"
"v_mfma_f32_16x16x32_bf16 v[152:155], a[152:155], a[128:131], v[152:155]\n"
"v_mul_f32 v187, s55, v187\n"
"v_cvt_pk_bf16_f32 v206, v180, v181\n"
"v_mfma_f32_16x16x32_bf16 v[152:155], a[156:159], a[132:135], v[152:155]\n"
"s_waitcnt lgkmcnt(2)\n"
"v_cvt_pk_bf16_f32 v207, v182, v183\n"
"v_mul_f32 v188, s55, v188\n"
"v_mfma_f32_16x16x32_bf16 v[144:147], a[160:163], a[88:91], v[144:147]\n"
"ds_read_b128 a[152:155], v240 offset:19712\n"
"ds_read_b128 a[156:159], v240 offset:20224\n"
"v_mfma_f32_16x16x32_bf16 v[144:147], a[164:167], a[92:95], v[144:147]\n"
"v_exp_f32 v184, v184\n"
"v_mfma_f32_16x16x32_bf16 v[148:151], a[164:167], a[116:119], v[148:151]\n"
"v_exp_f32 v185, v185\n"
"v_mfma_f32_16x16x32_bf16 v[148:151], a[160:163], a[112:115], v[148:151]\n"
"v_exp_f32 v186, v186\n"
"v_mfma_f32_16x16x32_bf16 v[152:155], a[160:163], a[136:139], v[152:155]\n"
"v_exp_f32 v187, v187\n"
"v_mfma_f32_16x16x32_bf16 v[152:155], a[164:167], a[140:143], v[152:155]\n"
"s_waitcnt lgkmcnt(2)\n"
"v_mul_f32 v189, s55, v189\n"
"v_mul_f32 v190, s55, v190\n"
"v_mfma_f32_16x16x32_bf16 v[156:159], a[144:147], a[72:75], 0\n"
"ds_read_b128 a[160:163], v240 offset:20736\n"
"ds_read_b128 a[164:167], v240 offset:21248\n"
"v_mfma_f32_16x16x32_bf16 v[156:159], a[148:151], a[76:79], v[156:159]\n"
"v_mul_f32 v191, s55, v191\n"
"v_cvt_pk_bf16_f32 v210, v184, v185\n"
"v_mfma_f32_16x16x32_bf16 v[160:163], a[148:151], a[100:103], 0\n"
"v_cvt_pk_bf16_f32 v211, v186, v187\n"
"v_mul_f32 v32, v232, v32\n"
"v_mfma_f32_16x16x32_bf16 v[160:163], a[144:147], a[96:99], v[160:163]\n"
"v_exp_f32 v188, v188\n"
"v_mfma_f32_16x16x32_bf16 v[164:167], a[144:147], a[120:123], 0\n"
"buffer_store_dwordx4 v[8:11], v244, s[48:51], s60 offen offset:64\n"
"v_mfma_f32_16x16x32_bf16 v[164:167], a[148:151], a[124:127], v[164:167]\n"
"s_waitcnt lgkmcnt(2)\n"
"v_exp_f32 v189, v189\n"
"v_mfma_f32_16x16x32_bf16 v[156:159], a[152:155], a[80:83], v[156:159]\n"
"ds_read_b128 a[144:147], v240 offset:0\n"
"ds_read_b128 a[148:151], v240 offset:512\n"
"v_mfma_f32_16x16x32_bf16 v[156:159], a[156:159], a[84:87], v[156:159]\n"
"v_exp_f32 v190, v190\n"
"v_mfma_f32_16x16x32_bf16 v[160:163], a[156:159], a[108:111], v[160:163]\n"
"v_exp_f32 v191, v191\n"
"v_mfma_f32_16x16x32_bf16 v[160:163], a[152:155], a[104:107], v[160:163]\n"
"v_cvt_pk_bf16_f32 v214, v188, v189\n"
"v_cvt_pk_bf16_f32 v215, v190, v191\n"
"v_mfma_f32_16x16x32_bf16 v[164:167], a[152:155], a[128:131], v[164:167]\n"
"v_mul_f32 v33, v232, v33\n"
"v_mul_f32 v34, v232, v34\n"
"v_mfma_f32_16x16x32_bf16 v[164:167], a[156:159], a[132:135], v[164:167]\n"
"s_waitcnt lgkmcnt(2)\n"
"v_mul_f32 v35, v232, v35\n"
"v_mul_f32 v36, v232, v36\n"
"v_mfma_f32_16x16x32_bf16 v[156:159], a[160:163], a[88:91], v[156:159]\n"
"ds_read_b128 a[152:155], v240 offset:1024\n"
"ds_read_b128 a[156:159], v240 offset:1536\n"
"v_mfma_f32_16x16x32_bf16 v[156:159], a[164:167], a[92:95], v[156:159]\n"
"v_mul_f32 v37, v232, v37\n"
"v_mul_f32 v38, v232, v38\n"
"v_mfma_f32_16x16x32_bf16 v[160:163], a[164:167], a[116:119], v[160:163]\n"
"v_mul_f32 v39, v232, v39\n"
"v_cvt_pk_bf16_f32 v32, v32, v33\n"
"v_mfma_f32_16x16x32_bf16 v[160:163], a[160:163], a[112:115], v[160:163]\n"
"v_cvt_pk_bf16_f32 v33, v34, v35\n"
"v_cvt_pk_bf16_f32 v34, v36, v37\n"
"v_mfma_f32_16x16x32_bf16 v[164:167], a[160:163], a[136:139], v[164:167]\n"
"v_cvt_pk_bf16_f32 v35, v38, v39\n"
"v_mul_f32 v40, v232, v40\n"
"v_mfma_f32_16x16x32_bf16 v[164:167], a[164:167], a[140:143], v[164:167]\n"
"s_waitcnt lgkmcnt(2)\n"
"v_mul_f32 v41, v232, v41\n"
"v_mul_f32 v42, v232, v42\n"
"v_mfma_f32_16x16x32_bf16 v[168:171], a[144:147], a[72:75], 0\n"
"ds_read_b128 a[160:163], v240 offset:2048\n"
"ds_read_b128 a[164:167], v240 offset:2560\n"
"v_mfma_f32_16x16x32_bf16 v[168:171], a[148:151], a[76:79], v[168:171]\n"
"v_permlane16_swap_b32 v32, v34\n"
"v_mfma_f32_16x16x32_bf16 v[172:175], a[148:151], a[100:103], 0\n"
"v_permlane16_swap_b32 v33, v35\n"
"v_mfma_f32_16x16x32_bf16 v[172:175], a[144:147], a[96:99], v[172:175]\n"
"v_mul_f32 v43, v232, v43\n"
"v_mul_f32 v44, v232, v44\n"
"v_mfma_f32_16x16x32_bf16 v[176:179], a[144:147], a[120:123], 0\n"
"buffer_store_dwordx4 v[16:19], v244, s[48:51], s60 offen offset:128\n"
"v_mfma_f32_16x16x32_bf16 v[176:179], a[148:151], a[124:127], v[176:179]\n"
"s_waitcnt lgkmcnt(2)\n"
"v_mul_f32 v45, v232, v45\n"
"v_mul_f32 v46, v232, v46\n"
"v_mfma_f32_16x16x32_bf16 v[168:171], a[152:155], a[80:83], v[168:171]\n"
"ds_read_b128 a[144:147], v240 offset:256\n"
"ds_read_b128 a[148:151], v240 offset:768\n"
"v_mfma_f32_16x16x32_bf16 v[168:171], a[156:159], a[84:87], v[168:171]\n"
"v_mul_f32 v47, v232, v47\n"
"v_cvt_pk_bf16_f32 v40, v40, v41\n"
"v_mfma_f32_16x16x32_bf16 v[172:175], a[156:159], a[108:111], v[172:175]\n"
"v_cvt_pk_bf16_f32 v41, v42, v43\n"
"v_cvt_pk_bf16_f32 v42, v44, v45\n"
"v_mfma_f32_16x16x32_bf16 v[172:175], a[152:155], a[104:107], v[172:175]\n"
"v_cvt_pk_bf16_f32 v43, v46, v47\n"
"v_mul_f32 v144, s55, v144\n"
"v_mfma_f32_16x16x32_bf16 v[176:179], a[152:155], a[128:131], v[176:179]\n"
"v_mul_f32 v145, s55, v145\n"
"v_mul_f32 v146, s55, v146\n"
"v_mfma_f32_16x16x32_bf16 v[176:179], a[156:159], a[132:135], v[176:179]\n"
"s_waitcnt lgkmcnt(2)\n"
"v_permlane16_swap_b32 v40, v42\n"
"v_mfma_f32_16x16x32_bf16 v[168:171], a[160:163], a[88:91], v[168:171]\n"
"ds_read_b128 a[152:155], v240 offset:1280\n"
"ds_read_b128 a[156:159], v240 offset:1792\n"
"v_mfma_f32_16x16x32_bf16 v[168:171], a[164:167], a[92:95], v[168:171]\n"
"v_permlane16_swap_b32 v41, v43\n"
"v_mfma_f32_16x16x32_bf16 v[172:175], a[164:167], a[116:119], v[172:175]\n"
"v_mul_f32 v147, s55, v147\n"
"v_mul_f32 v148, s55, v148\n"
"v_mfma_f32_16x16x32_bf16 v[172:175], a[160:163], a[112:115], v[172:175]\n"
"v_exp_f32 v144, v144\n"
"v_mfma_f32_16x16x32_bf16 v[176:179], a[160:163], a[136:139], v[176:179]\n"
"v_exp_f32 v145, v145\n"
"v_mfma_f32_16x16x32_bf16 v[176:179], a[164:167], a[140:143], v[176:179]\n"
"s_waitcnt lgkmcnt(2)\n"
"v_exp_f32 v146, v146\n"
"v_mfma_f32_16x16x32_bf16 v[180:183], a[144:147], a[72:75], 0\n"
"ds_read_b128 a[160:163], v240 offset:2304\n"
"ds_read_b128 a[164:167], v240 offset:2816\n"
"v_mfma_f32_16x16x32_bf16 v[180:183], a[148:151], a[76:79], v[180:183]\n"
"v_exp_f32 v147, v147\n"
"v_mfma_f32_16x16x32_bf16 v[184:187], a[148:151], a[100:103], 0\n"
"v_mul_f32 v149, s55, v149\n"
"v_mul_f32 v150, s55, v150\n"
"v_mfma_f32_16x16x32_bf16 v[184:187], a[144:147], a[96:99], v[184:187]\n"
"s_waitcnt vmcnt(15)\n"
"v_mul_f32 v151, s55, v151\n"
"v_mul_f32 v152, s55, v152\n"
"v_mfma_f32_16x16x32_bf16 v[188:191], a[144:147], a[120:123], 0\n"
"s_barrier\n"
"buffer_store_dwordx4 v[24:27], v244, s[48:51], s60 offen offset:192\n"
"v_mfma_f32_16x16x32_bf16 v[188:191], a[148:151], a[124:127], v[188:191]\n"
"s_waitcnt lgkmcnt(2)\n"
"v_exp_f32 v148, v148\n"
"v_mfma_f32_16x16x32_bf16 v[180:183], a[152:155], a[80:83], v[180:183]\n"
"s_mov_b32 s53, 319488\n"
"ds_read_b64_tr_b16 a[144:145], v241 offset:21504\n"
"ds_read_b64_tr_b16 a[146:147], v241 offset:21760\n"
"v_mfma_f32_16x16x32_bf16 v[180:183], a[156:159], a[84:87], v[180:183]\n"
"s_add_u32 m0, s52, 15360\n"
"ds_read_b64_tr_b16 a[148:149], v241 offset:24576\n"
"ds_read_b64_tr_b16 a[150:151], v241 offset:24832\n"
"v_mfma_f32_16x16x32_bf16 v[184:187], a[156:159], a[108:111], v[184:187]\n"
"buffer_load_dwordx4 v242, s[36:39], s53 offen offset:0 sc0 nt lds\n"
"v_mfma_f32_16x16x32_bf16 v[184:187], a[152:155], a[104:107], v[184:187]\n"
"v_exp_f32 v149, v149\n"
"v_mfma_f32_16x16x32_bf16 v[188:191], a[152:155], a[128:131], v[188:191]\n"
"v_exp_f32 v150, v150\n"
"v_mfma_f32_16x16x32_bf16 v[188:191], a[156:159], a[132:135], v[188:191]\n"
"s_waitcnt lgkmcnt(4)\n"
"v_exp_f32 v151, v151\n"
"v_mfma_f32_16x16x32_bf16 v[180:183], a[160:163], a[88:91], v[180:183]\n"
"ds_read_b64_tr_b16 a[152:153], v241 offset:21536\n"
"ds_read_b64_tr_b16 a[154:155], v241 offset:21792\n"
"v_mfma_f32_16x16x32_bf16 v[180:183], a[164:167], a[92:95], v[180:183]\n"
"s_add_u32 m0, s52, 16256\n"
"ds_read_b64_tr_b16 a[156:157], v241 offset:24608\n"
"ds_read_b64_tr_b16 a[158:159], v241 offset:24864\n"
"v_mfma_f32_16x16x32_bf16 v[184:187], a[164:167], a[116:119], v[184:187]\n"
"buffer_load_dwordx4 v242, s[36:39], s53 offen offset:128 sc0 nt lds\n"
"v_mfma_f32_16x16x32_bf16 v[184:187], a[160:163], a[112:115], v[184:187]\n"
"v_mul_f32 v153, s55, v153\n"
"v_mul_f32 v154, s55, v154\n"
"v_mfma_f32_16x16x32_bf16 v[188:191], a[160:163], a[136:139], v[188:191]\n"
"v_mul_f32 v155, s55, v155\n"
"v_mul_f32 v156, s55, v156\n"
"v_mfma_f32_16x16x32_bf16 v[188:191], a[164:167], a[140:143], v[188:191]\n"
"v_exp_f32 v152, v152\n"
"v_mfma_f32_16x16x32_bf16 a[240:243], a[252:255], v[192:195], a[240:243]\n"
"v_exp_f32 v153, v153\n"
"v_mfma_f32_16x16x32_bf16 a[240:243], a[252:255], v[204:207], a[240:243]\n"
"s_add_u32 m0, s52, 17152\n"
"v_exp_f32 v154, v154\n"
"v_mfma_f32_16x16x32_bf16 a[244:247], a[252:255], v[196:199], a[244:247]\n"
"buffer_load_dwordx4 v242, s[36:39], s53 offen offset:256 sc0 nt lds\n"
"v_mfma_f32_16x16x32_bf16 a[244:247], a[252:255], v[208:211], a[244:247]\n"
"v_exp_f32 v155, v155\n"
"v_mfma_f32_16x16x32_bf16 a[248:251], a[252:255], v[200:203], a[248:251]\n"
"v_mul_f32 v157, s55, v157\n"
"v_mul_f32 v158, s55, v158\n"
"v_mfma_f32_16x16x32_bf16 a[248:251], a[252:255], v[212:215], a[248:251]\n"
"s_waitcnt lgkmcnt(4)\n"
"v_mul_f32 v159, s55, v159\n"
"v_mul_f32 v160, s55, v160\n"
"v_mfma_f32_16x16x32_bf16 a[0:3], a[144:147], v[192:195], a[0:3]\n"
"s_mov_b32 s54, 331776\n"
"ds_read_b64_tr_b16 a[160:161], v241 offset:22016\n"
"ds_read_b64_tr_b16 a[162:163], v241 offset:22272\n"
"v_mfma_f32_16x16x32_bf16 a[0:3], a[148:151], v[204:207], a[0:3]\n"
"s_add_u32 m0, s52, 18432\n"
"ds_read_b64_tr_b16 a[164:165], v241 offset:25088\n"
"ds_read_b64_tr_b16 a[166:167], v241 offset:25344\n"
"v_mfma_f32_16x16x32_bf16 a[48:51], a[148:151], v[208:211], a[48:51]\n"
"buffer_load_dwordx4 v242, s[36:39], s54 offen offset:0 sc0 nt lds\n"
"v_mfma_f32_16x16x32_bf16 a[48:51], a[144:147], v[196:199], a[48:51]\n"
"v_exp_f32 v156, v156\n"
"v_mfma_f32_16x16x32_bf16 a[192:195], a[144:147], v[200:203], a[192:195]\n"
"v_exp_f32 v157, v157\n"
"v_mfma_f32_16x16x32_bf16 a[192:195], a[148:151], v[212:215], a[192:195]\n"
"s_waitcnt lgkmcnt(4)\n"
"v_exp_f32 v158, v158\n"
"v_mfma_f32_16x16x32_bf16 a[4:7], a[152:155], v[192:195], a[4:7]\n"
"ds_read_b64_tr_b16 a[144:145], v241 offset:22048\n"
"ds_read_b64_tr_b16 a[146:147], v241 offset:22304\n"
"v_mfma_f32_16x16x32_bf16 a[4:7], a[156:159], v[204:207], a[4:7]\n"
"s_add_u32 m0, s52, 19328\n"
"ds_read_b64_tr_b16 a[148:149], v241 offset:25120\n"
"ds_read_b64_tr_b16 a[150:151], v241 offset:25376\n"
"v_mfma_f32_16x16x32_bf16 a[52:55], a[156:159], v[208:211], a[52:55]\n"
"buffer_load_dwordx4 v242, s[36:39], s54 offen offset:128 sc0 nt lds\n"
"v_mfma_f32_16x16x32_bf16 a[52:55], a[152:155], v[196:199], a[52:55]\n"
"v_exp_f32 v159, v159\n"
"v_mfma_f32_16x16x32_bf16 a[196:199], a[152:155], v[200:203], a[196:199]\n"
"v_mul_f32 v161, s55, v161\n"
"v_mul_f32 v162, s55, v162\n"
"v_mfma_f32_16x16x32_bf16 a[196:199], a[156:159], v[212:215], a[196:199]\n"
"s_waitcnt lgkmcnt(4)\n"
"v_mul_f32 v163, s55, v163\n"
"v_mul_f32 v164, s55, v164\n"
"v_mfma_f32_16x16x32_bf16 a[8:11], a[160:163], v[192:195], a[8:11]\n"
"ds_read_b64_tr_b16 a[152:153], v241 offset:22528\n"
"ds_read_b64_tr_b16 a[154:155], v241 offset:22784\n"
"v_mfma_f32_16x16x32_bf16 a[8:11], a[164:167], v[204:207], a[8:11]\n"
"s_add_u32 m0, s52, 20224\n"
"ds_read_b64_tr_b16 a[156:157], v241 offset:25600\n"
"ds_read_b64_tr_b16 a[158:159], v241 offset:25856\n"
"v_mfma_f32_16x16x32_bf16 a[56:59], a[164:167], v[208:211], a[56:59]\n"
"buffer_load_dwordx4 v242, s[36:39], s54 offen offset:256 sc0 nt lds\n"
"v_mfma_f32_16x16x32_bf16 a[56:59], a[160:163], v[196:199], a[56:59]\n"
"v_exp_f32 v160, v160\n"
"v_mfma_f32_16x16x32_bf16 a[200:203], a[160:163], v[200:203], a[200:203]\n"
"v_exp_f32 v161, v161\n"
"v_mfma_f32_16x16x32_bf16 a[200:203], a[164:167], v[212:215], a[200:203]\n"
"s_waitcnt lgkmcnt(4)\n"
"v_exp_f32 v162, v162\n"
"v_mfma_f32_16x16x32_bf16 a[12:15], a[144:147], v[192:195], a[12:15]\n"
"s_mov_b32 s59, 270336\n"
"ds_read_b64_tr_b16 a[160:161], v241 offset:22560\n"
"ds_read_b64_tr_b16 a[162:163], v241 offset:22816\n"
"v_mfma_f32_16x16x32_bf16 a[12:15], a[148:151], v[204:207], a[12:15]\n"
"s_add_u32 m0, s52, 33792\n"
"ds_read_b64_tr_b16 a[164:165], v241 offset:25632\n"
"ds_read_b64_tr_b16 a[166:167], v241 offset:25888\n"
"v_mfma_f32_16x16x32_bf16 a[60:63], a[148:151], v[208:211], a[60:63]\n"
"buffer_load_dwordx4 v242, s[40:43], s59 offen offset:0 sc0 nt lds\n"
"v_mfma_f32_16x16x32_bf16 a[60:63], a[144:147], v[196:199], a[60:63]\n"
"v_exp_f32 v163, v163\n"
"v_mfma_f32_16x16x32_bf16 a[204:207], a[144:147], v[200:203], a[204:207]\n"
"v_mul_f32 v165, s55, v165\n"
"v_mul_f32 v166, s55, v166\n"
"v_mfma_f32_16x16x32_bf16 a[204:207], a[148:151], v[212:215], a[204:207]\n"
"s_waitcnt lgkmcnt(4)\n"
"v_mul_f32 v167, s55, v167\n"
"v_mul_f32 v48, v233, v48\n"
"v_mfma_f32_16x16x32_bf16 a[16:19], a[152:155], v[192:195], a[16:19]\n"
"ds_read_b64_tr_b16 a[144:145], v241 offset:23040\n"
"ds_read_b64_tr_b16 a[146:147], v241 offset:23296\n"
"v_mfma_f32_16x16x32_bf16 a[16:19], a[156:159], v[204:207], a[16:19]\n"
"s_add_u32 m0, s52, 34688\n"
"ds_read_b64_tr_b16 a[148:149], v241 offset:26112\n"
"ds_read_b64_tr_b16 a[150:151], v241 offset:26368\n"
"v_mfma_f32_16x16x32_bf16 a[64:67], a[156:159], v[208:211], a[64:67]\n"
"buffer_load_dwordx4 v242, s[40:43], s59 offen offset:128 sc0 nt lds\n"
"v_mfma_f32_16x16x32_bf16 a[64:67], a[152:155], v[196:199], a[64:67]\n"
"v_mul_f32 v49, v233, v49\n"
"v_mul_f32 v50, v233, v50\n"
"v_mfma_f32_16x16x32_bf16 a[208:211], a[152:155], v[200:203], a[208:211]\n"
"v_mul_f32 v51, v233, v51\n"
"v_mul_f32 v52, v233, v52\n"
"v_mfma_f32_16x16x32_bf16 a[208:211], a[156:159], v[212:215], a[208:211]\n"
"s_waitcnt lgkmcnt(4)\n"
"v_mul_f32 v53, v233, v53\n"
"v_mul_f32 v54, v233, v54\n"
"v_mfma_f32_16x16x32_bf16 a[20:23], a[160:163], v[192:195], a[20:23]\n"
"ds_read_b64_tr_b16 a[152:153], v241 offset:23072\n"
"ds_read_b64_tr_b16 a[154:155], v241 offset:23328\n"
"v_mfma_f32_16x16x32_bf16 a[20:23], a[164:167], v[204:207], a[20:23]\n"
"s_add_u32 m0, s52, 35584\n"
"ds_read_b64_tr_b16 a[156:157], v241 offset:26144\n"
"ds_read_b64_tr_b16 a[158:159], v241 offset:26400\n"
"v_mfma_f32_16x16x32_bf16 a[68:71], a[164:167], v[208:211], a[68:71]\n"
"buffer_load_dwordx4 v242, s[40:43], s59 offen offset:256 sc0 nt lds\n"
"v_mfma_f32_16x16x32_bf16 a[68:71], a[160:163], v[196:199], a[68:71]\n"
"v_mul_f32 v55, v233, v55\n"
"v_cvt_pk_bf16_f32 v48, v48, v49\n"
"v_mfma_f32_16x16x32_bf16 a[212:215], a[160:163], v[200:203], a[212:215]\n"
"v_exp_f32 v164, v164\n"
"v_mfma_f32_16x16x32_bf16 a[212:215], a[164:167], v[212:215], a[212:215]\n"
"s_waitcnt lgkmcnt(4)\n"
"v_exp_f32 v165, v165\n"
"v_mfma_f32_16x16x32_bf16 a[24:27], a[144:147], v[192:195], a[24:27]\n"
"s_mov_b32 s70, 282624\n"
"ds_read_b64_tr_b16 a[160:161], v241 offset:23552\n"
"ds_read_b64_tr_b16 a[162:163], v241 offset:23808\n"
"v_mfma_f32_16x16x32_bf16 a[24:27], a[148:151], v[204:207], a[24:27]\n"
"s_add_u32 m0, s52, 36864\n"
"ds_read_b64_tr_b16 a[164:165], v241 offset:26624\n"
"ds_read_b64_tr_b16 a[166:167], v241 offset:26880\n"
"v_mfma_f32_16x16x32_bf16 a[168:171], a[148:151], v[208:211], a[168:171]\n"
"buffer_load_dwordx4 v242, s[40:43], s70 offen offset:0 sc0 nt lds\n"
"v_mfma_f32_16x16x32_bf16 a[168:171], a[144:147], v[196:199], a[168:171]\n"
"v_exp_f32 v166, v166\n"
"v_mfma_f32_16x16x32_bf16 a[216:219], a[144:147], v[200:203], a[216:219]\n"
"v_exp_f32 v167, v167\n"
"v_mfma_f32_16x16x32_bf16 a[216:219], a[148:151], v[212:215], a[216:219]\n"
"s_waitcnt lgkmcnt(4)\n"
"v_cvt_pk_bf16_f32 v49, v50, v51\n"
"v_cvt_pk_bf16_f32 v50, v52, v53\n"
"v_mfma_f32_16x16x32_bf16 a[28:31], a[152:155], v[192:195], a[28:31]\n"
"ds_read_b64_tr_b16 a[144:145], v241 offset:23584\n"
"ds_read_b64_tr_b16 a[146:147], v241 offset:23840\n"
"v_mfma_f32_16x16x32_bf16 a[28:31], a[156:159], v[204:207], a[28:31]\n"
"s_add_u32 m0, s52, 37760\n"
"ds_read_b64_tr_b16 a[148:149], v241 offset:26656\n"
"ds_read_b64_tr_b16 a[150:151], v241 offset:26912\n"
"v_mfma_f32_16x16x32_bf16 a[172:175], a[156:159], v[208:211], a[172:175]\n"
"buffer_load_dwordx4 v242, s[40:43], s70 offen offset:128 sc0 nt lds\n"
"v_mfma_f32_16x16x32_bf16 a[172:175], a[152:155], v[196:199], a[172:175]\n"
"v_cvt_pk_bf16_f32 v51, v54, v55\n"
"v_mul_f32 v168, s55, v168\n"
"v_mfma_f32_16x16x32_bf16 a[220:223], a[152:155], v[200:203], a[220:223]\n"
"v_mul_f32 v169, s55, v169\n"
"v_mul_f32 v170, s55, v170\n"
"v_mfma_f32_16x16x32_bf16 a[220:223], a[156:159], v[212:215], a[220:223]\n"
"s_waitcnt lgkmcnt(4)\n"
"v_permlane16_swap_b32 v48, v50\n"
"v_mfma_f32_16x16x32_bf16 a[32:35], a[160:163], v[192:195], a[32:35]\n"
"ds_read_b64_tr_b16 a[152:153], v241 offset:24064\n"
"ds_read_b64_tr_b16 a[154:155], v241 offset:24320\n"
"v_mfma_f32_16x16x32_bf16 a[32:35], a[164:167], v[204:207], a[32:35]\n"
"s_add_u32 m0, s52, 38656\n"
"ds_read_b64_tr_b16 a[156:157], v241 offset:27136\n"
"ds_read_b64_tr_b16 a[158:159], v241 offset:27392\n"
"v_mfma_f32_16x16x32_bf16 a[176:179], a[164:167], v[208:211], a[176:179]\n"
"buffer_load_dwordx4 v242, s[40:43], s70 offen offset:256 sc0 nt lds\n"
"v_mfma_f32_16x16x32_bf16 a[176:179], a[160:163], v[196:199], a[176:179]\n"
"v_permlane16_swap_b32 v49, v51\n"
"v_mfma_f32_16x16x32_bf16 a[224:227], a[160:163], v[200:203], a[224:227]\n"
"v_mul_f32 v171, s55, v171\n"
"v_mul_f32 v172, s55, v172\n"
"v_mfma_f32_16x16x32_bf16 a[224:227], a[164:167], v[212:215], a[224:227]\n"
"s_waitcnt lgkmcnt(4)\n"
"v_exp_f32 v168, v168\n"
"v_mfma_f32_16x16x32_bf16 a[36:39], a[144:147], v[192:195], a[36:39]\n"
"ds_read_b64_tr_b16 a[160:161], v241 offset:24096\n"
"ds_read_b64_tr_b16 a[162:163], v241 offset:24352\n"
"v_mfma_f32_16x16x32_bf16 a[36:39], a[148:151], v[204:207], a[36:39]\n"
"ds_read_b64_tr_b16 a[164:165], v241 offset:27168\n"
"ds_read_b64_tr_b16 a[166:167], v241 offset:27424\n"
"v_mfma_f32_16x16x32_bf16 a[180:183], a[148:151], v[208:211], a[180:183]\n"
"v_exp_f32 v169, v169\n"
"v_mfma_f32_16x16x32_bf16 a[180:183], a[144:147], v[196:199], a[180:183]\n"
"v_exp_f32 v170, v170\n"
"v_mfma_f32_16x16x32_bf16 a[228:231], a[144:147], v[200:203], a[228:231]\n"
"v_exp_f32 v171, v171\n"
"v_mfma_f32_16x16x32_bf16 a[228:231], a[148:151], v[212:215], a[228:231]\n"
"s_waitcnt lgkmcnt(4)\n"
"v_mul_f32 v173, s55, v173\n"
"v_mul_f32 v174, s55, v174\n"
"v_mfma_f32_16x16x32_bf16 a[40:43], a[152:155], v[192:195], a[40:43]\n"
"ds_read_b128 a[144:147], v240 offset:3072\n"
"ds_read_b128 a[148:151], v240 offset:3584\n"
"v_mfma_f32_16x16x32_bf16 a[40:43], a[156:159], v[204:207], a[40:43]\n"
"v_mul_f32 v175, s55, v175\n"
"v_mul_f32 v176, s55, v176\n"
"v_mfma_f32_16x16x32_bf16 a[184:187], a[156:159], v[208:211], a[184:187]\n"
"v_exp_f32 v172, v172\n"
"v_mfma_f32_16x16x32_bf16 a[184:187], a[152:155], v[196:199], a[184:187]\n"
"v_exp_f32 v173, v173\n"
"v_mfma_f32_16x16x32_bf16 a[232:235], a[152:155], v[200:203], a[232:235]\n"
"v_exp_f32 v174, v174\n"
"v_mfma_f32_16x16x32_bf16 a[232:235], a[156:159], v[212:215], a[232:235]\n"
"s_waitcnt lgkmcnt(2)\n"
"v_exp_f32 v175, v175\n"
"v_mfma_f32_16x16x32_bf16 a[44:47], a[160:163], v[192:195], a[44:47]\n"
"ds_read_b128 a[152:155], v240 offset:4096\n"
"ds_read_b128 a[156:159], v240 offset:4608\n"
"v_mfma_f32_16x16x32_bf16 a[44:47], a[164:167], v[204:207], a[44:47]\n"
"v_cvt_pk_bf16_f32 v192, v144, v145\n"
"v_cvt_pk_bf16_f32 v193, v146, v147\n"
"v_mfma_f32_16x16x32_bf16 a[188:191], a[164:167], v[208:211], a[188:191]\n"
"v_cvt_pk_bf16_f32 v194, v156, v157\n"
"v_cvt_pk_bf16_f32 v195, v158, v159\n"
"v_mfma_f32_16x16x32_bf16 a[188:191], a[160:163], v[196:199], a[188:191]\n"
"v_cvt_pk_bf16_f32 v196, v148, v149\n"
"v_cvt_pk_bf16_f32 v197, v150, v151\n"
"v_mfma_f32_16x16x32_bf16 a[236:239], a[160:163], v[200:203], a[236:239]\n"
"v_cvt_pk_bf16_f32 v200, v152, v153\n"
"v_cvt_pk_bf16_f32 v201, v154, v155\n"
"v_mfma_f32_16x16x32_bf16 a[236:239], a[164:167], v[212:215], a[236:239]\n"
"s_waitcnt lgkmcnt(2)\n"
"v_cvt_pk_bf16_f32 v198, v160, v161\n"
"v_cvt_pk_bf16_f32 v199, v162, v163\n"
"v_mfma_f32_16x16x32_bf16 v[144:147], a[144:147], a[72:75], 0\n"
"ds_read_b128 a[160:163], v240 offset:5120\n"
"ds_read_b128 a[164:167], v240 offset:5632\n"
"v_mfma_f32_16x16x32_bf16 v[144:147], a[148:151], a[76:79], v[144:147]\n"
"v_cvt_pk_bf16_f32 v202, v164, v165\n"
"v_cvt_pk_bf16_f32 v203, v166, v167\n"
"v_mfma_f32_16x16x32_bf16 v[148:151], a[148:151], a[100:103], 0\n"
"v_cvt_pk_bf16_f32 v204, v168, v169\n"
"v_cvt_pk_bf16_f32 v205, v170, v171\n"
"v_mfma_f32_16x16x32_bf16 v[148:151], a[144:147], a[96:99], v[148:151]\n"
"v_mul_f32 v177, s55, v177\n"
"v_mul_f32 v178, s55, v178\n"
"v_mfma_f32_16x16x32_bf16 v[152:155], a[144:147], a[120:123], 0\n"
"buffer_store_dwordx4 v[32:35], v244, s[48:51], s60 offen offset:256\n"
"v_mfma_f32_16x16x32_bf16 v[152:155], a[148:151], a[124:127], v[152:155]\n"
"s_waitcnt lgkmcnt(2)\n"
"v_mul_f32 v179, s55, v179\n"
"v_cvt_pk_bf16_f32 v208, v172, v173\n"
"v_mfma_f32_16x16x32_bf16 v[144:147], a[152:155], a[80:83], v[144:147]\n"
"ds_read_b128 a[144:147], v240 offset:3328\n"
"ds_read_b128 a[148:151], v240 offset:3840\n"
"v_mfma_f32_16x16x32_bf16 v[144:147], a[156:159], a[84:87], v[144:147]\n"
"v_cvt_pk_bf16_f32 v209, v174, v175\n"
"v_mul_f32 v180, s55, v180\n"
"v_mfma_f32_16x16x32_bf16 v[148:151], a[156:159], a[108:111], v[148:151]\n"
"v_exp_f32 v176, v176\n"
"v_mfma_f32_16x16x32_bf16 v[148:151], a[152:155], a[104:107], v[148:151]\n"
"v_exp_f32 v177, v177\n"
"v_mfma_f32_16x16x32_bf16 v[152:155], a[152:155], a[128:131], v[152:155]\n"
"v_exp_f32 v178, v178\n"
"v_mfma_f32_16x16x32_bf16 v[152:155], a[156:159], a[132:135], v[152:155]\n"
"s_waitcnt lgkmcnt(2)\n"
"v_exp_f32 v179, v179\n"
"v_mfma_f32_16x16x32_bf16 v[144:147], a[160:163], a[88:91], v[144:147]\n"
"ds_read_b128 a[152:155], v240 offset:4352\n"
"ds_read_b128 a[156:159], v240 offset:4864\n"
"v_mfma_f32_16x16x32_bf16 v[144:147], a[164:167], a[92:95], v[144:147]\n"
"v_cvt_pk_bf16_f32 v212, v176, v177\n"
"v_cvt_pk_bf16_f32 v213, v178, v179\n"
"v_mfma_f32_16x16x32_bf16 v[148:151], a[164:167], a[116:119], v[148:151]\n"
"v_mul_f32 v181, s55, v181\n"
"v_mul_f32 v182, s55, v182\n"
"v_mfma_f32_16x16x32_bf16 v[148:151], a[160:163], a[112:115], v[148:151]\n"
"v_mul_f32 v183, s55, v183\n"
"v_mul_f32 v184, s55, v184\n"
"v_mfma_f32_16x16x32_bf16 v[152:155], a[160:163], a[136:139], v[152:155]\n"
"v_exp_f32 v180, v180\n"
"v_mfma_f32_16x16x32_bf16 v[152:155], a[164:167], a[140:143], v[152:155]\n"
"s_waitcnt lgkmcnt(2)\n"
"v_exp_f32 v181, v181\n"
"v_mfma_f32_16x16x32_bf16 v[156:159], a[144:147], a[72:75], 0\n"
"ds_read_b128 a[160:163], v240 offset:5376\n"
"ds_read_b128 a[164:167], v240 offset:5888\n"
"v_mfma_f32_16x16x32_bf16 v[156:159], a[148:151], a[76:79], v[156:159]\n"
"v_exp_f32 v182, v182\n"
"v_mfma_f32_16x16x32_bf16 v[160:163], a[148:151], a[100:103], 0\n"
"v_exp_f32 v183, v183\n"
"v_mfma_f32_16x16x32_bf16 v[160:163], a[144:147], a[96:99], v[160:163]\n"
"v_mul_f32 v185, s55, v185\n"
"v_mul_f32 v186, s55, v186\n"
"v_mfma_f32_16x16x32_bf16 v[164:167], a[144:147], a[120:123], 0\n"
"buffer_store_dwordx4 v[40:43], v244, s[48:51], s60 offen offset:320\n"
"v_mfma_f32_16x16x32_bf16 v[164:167], a[148:151], a[124:127], v[164:167]\n"
"s_waitcnt lgkmcnt(2)\n"
"v_mul_f32 v187, s55, v187\n"
"v_cvt_pk_bf16_f32 v206, v180, v181\n"
"v_mfma_f32_16x16x32_bf16 v[156:159], a[152:155], a[80:83], v[156:159]\n"
"ds_read_b128 a[144:147], v240 offset:6144\n"
"ds_read_b128 a[148:151], v240 offset:6656\n"
"v_mfma_f32_16x16x32_bf16 v[156:159], a[156:159], a[84:87], v[156:159]\n"
"v_cvt_pk_bf16_f32 v207, v182, v183\n"
"v_mul_f32 v188, s55, v188\n"
"v_mfma_f32_16x16x32_bf16 v[160:163], a[156:159], a[108:111], v[160:163]\n"
"v_exp_f32 v184, v184\n"
"v_mfma_f32_16x16x32_bf16 v[160:163], a[152:155], a[104:107], v[160:163]\n"
"v_exp_f32 v185, v185\n"
"v_mfma_f32_16x16x32_bf16 v[164:167], a[152:155], a[128:131], v[164:167]\n"
"v_exp_f32 v186, v186\n"
"v_mfma_f32_16x16x32_bf16 v[164:167], a[156:159], a[132:135], v[164:167]\n"
"s_waitcnt lgkmcnt(2)\n"
"v_exp_f32 v187, v187\n"
"v_mfma_f32_16x16x32_bf16 v[156:159], a[160:163], a[88:91], v[156:159]\n"
"ds_read_b128 a[152:155], v240 offset:7168\n"
"ds_read_b128 a[156:159], v240 offset:7680\n"
"v_mfma_f32_16x16x32_bf16 v[156:159], a[164:167], a[92:95], v[156:159]\n"
"v_mul_f32 v189, s55, v189\n"
"v_mul_f32 v190, s55, v190\n"
"v_mfma_f32_16x16x32_bf16 v[160:163], a[164:167], a[116:119], v[160:163]\n"
"v_mul_f32 v191, s55, v191\n"
"v_cvt_pk_bf16_f32 v210, v184, v185\n"
"v_mfma_f32_16x16x32_bf16 v[160:163], a[160:163], a[112:115], v[160:163]\n"
"v_cvt_pk_bf16_f32 v211, v186, v187\n"
"v_mul_f32 v56, v233, v56\n"
"v_mfma_f32_16x16x32_bf16 v[164:167], a[160:163], a[136:139], v[164:167]\n"
"v_exp_f32 v188, v188\n"
"v_mfma_f32_16x16x32_bf16 v[164:167], a[164:167], a[140:143], v[164:167]\n"
"s_waitcnt lgkmcnt(2)\n"
"v_exp_f32 v189, v189\n"
"v_mfma_f32_16x16x32_bf16 v[168:171], a[144:147], a[72:75], 0\n"
"ds_read_b128 a[160:163], v240 offset:8192\n"
"ds_read_b128 a[164:167], v240 offset:8704\n"
"v_mfma_f32_16x16x32_bf16 v[168:171], a[148:151], a[76:79], v[168:171]\n"
"v_exp_f32 v190, v190\n"
"v_mfma_f32_16x16x32_bf16 v[172:175], a[148:151], a[100:103], 0\n"
"v_exp_f32 v191, v191\n"
"v_mfma_f32_16x16x32_bf16 v[172:175], a[144:147], a[96:99], v[172:175]\n"
"v_mul_f32 v57, v233, v57\n"
"v_mul_f32 v58, v233, v58\n"
"v_mfma_f32_16x16x32_bf16 v[176:179], a[144:147], a[120:123], 0\n"
"buffer_store_dwordx4 v[48:51], v244, s[48:51], s61 offen offset:0\n"
"v_mfma_f32_16x16x32_bf16 v[176:179], a[148:151], a[124:127], v[176:179]\n"
"s_waitcnt lgkmcnt(2)\n"
"v_mul_f32 v59, v233, v59\n"
"v_mul_f32 v60, v233, v60\n"
"v_mfma_f32_16x16x32_bf16 v[168:171], a[152:155], a[80:83], v[168:171]\n"
"ds_read_b128 a[144:147], v240 offset:6400\n"
"ds_read_b128 a[148:151], v240 offset:6912\n"
"v_mfma_f32_16x16x32_bf16 v[168:171], a[156:159], a[84:87], v[168:171]\n"
"v_mul_f32 v61, v233, v61\n"
"v_mul_f32 v62, v233, v62\n"
"v_mfma_f32_16x16x32_bf16 v[172:175], a[156:159], a[108:111], v[172:175]\n"
"v_mul_f32 v63, v233, v63\n"
"v_cvt_pk_bf16_f32 v56, v56, v57\n"
"v_mfma_f32_16x16x32_bf16 v[172:175], a[152:155], a[104:107], v[172:175]\n"
"v_cvt_pk_bf16_f32 v57, v58, v59\n"
"v_cvt_pk_bf16_f32 v58, v60, v61\n"
"v_mfma_f32_16x16x32_bf16 v[176:179], a[152:155], a[128:131], v[176:179]\n"
"v_cvt_pk_bf16_f32 v59, v62, v63\n"
"v_cvt_pk_bf16_f32 v214, v188, v189\n"
"v_mfma_f32_16x16x32_bf16 v[176:179], a[156:159], a[132:135], v[176:179]\n"
"s_waitcnt lgkmcnt(2)\n"
"v_cvt_pk_bf16_f32 v215, v190, v191\n"
"v_mul_f32 v64, v233, v64\n"
"v_mfma_f32_16x16x32_bf16 v[168:171], a[160:163], a[88:91], v[168:171]\n"
"ds_read_b128 a[152:155], v240 offset:7424\n"
"ds_read_b128 a[156:159], v240 offset:7936\n"
"v_mfma_f32_16x16x32_bf16 v[168:171], a[164:167], a[92:95], v[168:171]\n"
"v_permlane16_swap_b32 v56, v58\n"
"v_mfma_f32_16x16x32_bf16 v[172:175], a[164:167], a[116:119], v[172:175]\n"
"v_permlane16_swap_b32 v57, v59\n"
"v_mfma_f32_16x16x32_bf16 v[172:175], a[160:163], a[112:115], v[172:175]\n"
"v_mul_f32 v65, v233, v65\n"
"v_mul_f32 v66, v233, v66\n"
"v_mfma_f32_16x16x32_bf16 v[176:179], a[160:163], a[136:139], v[176:179]\n"
"v_mul_f32 v67, v233, v67\n"
"v_mul_f32 v68, v233, v68\n"
"v_mfma_f32_16x16x32_bf16 v[176:179], a[164:167], a[140:143], v[176:179]\n"
"s_waitcnt lgkmcnt(2)\n"
"v_mul_f32 v69, v233, v69\n"
"v_mul_f32 v70, v233, v70\n"
"v_mfma_f32_16x16x32_bf16 v[180:183], a[144:147], a[72:75], 0\n"
"ds_read_b128 a[160:163], v240 offset:8448\n"
"ds_read_b128 a[164:167], v240 offset:8960\n"
"v_mfma_f32_16x16x32_bf16 v[180:183], a[148:151], a[76:79], v[180:183]\n"
"v_mul_f32 v71, v233, v71\n"
"v_cvt_pk_bf16_f32 v64, v64, v65\n"
"v_mfma_f32_16x16x32_bf16 v[184:187], a[148:151], a[100:103], 0\n"
"v_cvt_pk_bf16_f32 v65, v66, v67\n"
"v_cvt_pk_bf16_f32 v66, v68, v69\n"
"v_mfma_f32_16x16x32_bf16 v[184:187], a[144:147], a[96:99], v[184:187]\n"
"s_waitcnt vmcnt(19)\n"
"v_cvt_pk_bf16_f32 v67, v70, v71\n"
"v_mul_f32 v144, s55, v144\n"
"v_mfma_f32_16x16x32_bf16 v[188:191], a[144:147], a[120:123], 0\n"
"s_barrier\n"
"buffer_store_dwordx4 v[56:59], v244, s[48:51], s61 offen offset:64\n"
"v_mfma_f32_16x16x32_bf16 v[188:191], a[148:151], a[124:127], v[188:191]\n"
"s_waitcnt lgkmcnt(2)\n"
"v_permlane16_swap_b32 v64, v66\n"
"v_mfma_f32_16x16x32_bf16 v[180:183], a[152:155], a[80:83], v[180:183]\n"
"s_mov_b32 s53, 344064\n"
"ds_read_b64_tr_b16 a[144:145], v241 offset:27648\n"
"ds_read_b64_tr_b16 a[146:147], v241 offset:27904\n"
"v_mfma_f32_16x16x32_bf16 v[180:183], a[156:159], a[84:87], v[180:183]\n"
"s_add_u32 m0, s52, 0\n"
"ds_read_b64_tr_b16 a[148:149], v241 offset:30720\n"
"ds_read_b64_tr_b16 a[150:151], v241 offset:30976\n"
"v_mfma_f32_16x16x32_bf16 v[184:187], a[156:159], a[108:111], v[184:187]\n"
"buffer_load_dwordx4 v242, s[36:39], s53 offen offset:0 sc0 nt lds\n"
"v_mfma_f32_16x16x32_bf16 v[184:187], a[152:155], a[104:107], v[184:187]\n"
"v_permlane16_swap_b32 v65, v67\n"
"v_mfma_f32_16x16x32_bf16 v[188:191], a[152:155], a[128:131], v[188:191]\n"
"v_mul_f32 v145, s55, v145\n"
"v_mul_f32 v146, s55, v146\n"
"v_mfma_f32_16x16x32_bf16 v[188:191], a[156:159], a[132:135], v[188:191]\n"
"s_waitcnt lgkmcnt(4)\n"
"v_mul_f32 v147, s55, v147\n"
"v_mul_f32 v148, s55, v148\n"
"v_mfma_f32_16x16x32_bf16 v[180:183], a[160:163], a[88:91], v[180:183]\n"
"ds_read_b64_tr_b16 a[152:153], v241 offset:27680\n"
"ds_read_b64_tr_b16 a[154:155], v241 offset:27936\n"
"v_mfma_f32_16x16x32_bf16 v[180:183], a[164:167], a[92:95], v[180:183]\n"
"s_add_u32 m0, s52, 896\n"
"ds_read_b64_tr_b16 a[156:157], v241 offset:30752\n"
"ds_read_b64_tr_b16 a[158:159], v241 offset:31008\n"
"v_mfma_f32_16x16x32_bf16 v[184:187], a[164:167], a[116:119], v[184:187]\n"
"buffer_load_dwordx4 v242, s[36:39], s53 offen offset:128 sc0 nt lds\n"
"v_mfma_f32_16x16x32_bf16 v[184:187], a[160:163], a[112:115], v[184:187]\n"
"v_exp_f32 v144, v144\n"
"v_mfma_f32_16x16x32_bf16 v[188:191], a[160:163], a[136:139], v[188:191]\n"
"v_exp_f32 v145, v145\n"
"v_mfma_f32_16x16x32_bf16 v[188:191], a[164:167], a[140:143], v[188:191]\n"
"v_exp_f32 v146, v146\n"
"v_mfma_f32_16x16x32_bf16 a[240:243], a[252:255], v[192:195], a[240:243]\n"
"v_exp_f32 v147, v147\n"
"v_mfma_f32_16x16x32_bf16 a[240:243], a[252:255], v[204:207], a[240:243]\n"
"s_add_u32 m0, s52, 1792\n"
"v_mul_f32 v149, s55, v149\n"
"v_mul_f32 v150, s55, v150\n"
"v_mfma_f32_16x16x32_bf16 a[244:247], a[252:255], v[196:199], a[244:247]\n"
"buffer_load_dwordx4 v242, s[36:39], s53 offen offset:256 sc0 nt lds\n"
"v_mfma_f32_16x16x32_bf16 a[244:247], a[252:255], v[208:211], a[244:247]\n"
"v_mul_f32 v151, s55, v151\n"
"v_mul_f32 v152, s55, v152\n"
"v_mfma_f32_16x16x32_bf16 a[248:251], a[252:255], v[200:203], a[248:251]\n"
"v_exp_f32 v148, v148\n"
"v_mfma_f32_16x16x32_bf16 a[248:251], a[252:255], v[212:215], a[248:251]\n"
"s_waitcnt lgkmcnt(4)\n"
"v_exp_f32 v149, v149\n"
"v_mfma_f32_16x16x32_bf16 a[0:3], a[144:147], v[192:195], a[0:3]\n"
"s_mov_b32 s54, 356352\n"
"ds_read_b64_tr_b16 a[160:161], v241 offset:28160\n"
"ds_read_b64_tr_b16 a[162:163], v241 offset:28416\n"
"v_mfma_f32_16x16x32_bf16 a[0:3], a[148:151], v[204:207], a[0:3]\n"
"s_add_u32 m0, s52, 3072\n"
"ds_read_b64_tr_b16 a[164:165], v241 offset:31232\n"
"ds_read_b64_tr_b16 a[166:167], v241 offset:31488\n"
"v_mfma_f32_16x16x32_bf16 a[48:51], a[148:151], v[208:211], a[48:51]\n"
"buffer_load_dwordx4 v242, s[36:39], s54 offen offset:0 sc0 nt lds\n"
"v_mfma_f32_16x16x32_bf16 a[48:51], a[144:147], v[196:199], a[48:51]\n"
"v_exp_f32 v150, v150\n"
"v_mfma_f32_16x16x32_bf16 a[192:195], a[144:147], v[200:203], a[192:195]\n"
"v_exp_f32 v151, v151\n"
"v_mfma_f32_16x16x32_bf16 a[192:195], a[148:151], v[212:215], a[192:195]\n"
"s_waitcnt lgkmcnt(4)\n"
"v_mul_f32 v153, s55, v153\n"
"v_mul_f32 v154, s55, v154\n"
"v_mfma_f32_16x16x32_bf16 a[4:7], a[152:155], v[192:195], a[4:7]\n"
"ds_read_b64_tr_b16 a[144:145], v241 offset:28192\n"
"ds_read_b64_tr_b16 a[146:147], v241 offset:28448\n"
"v_mfma_f32_16x16x32_bf16 a[4:7], a[156:159], v[204:207], a[4:7]\n"
"s_add_u32 m0, s52, 3968\n"
"ds_read_b64_tr_b16 a[148:149], v241 offset:31264\n"
"ds_read_b64_tr_b16 a[150:151], v241 offset:31520\n"
"v_mfma_f32_16x16x32_bf16 a[52:55], a[156:159], v[208:211], a[52:55]\n"
"buffer_load_dwordx4 v242, s[36:39], s54 offen offset:128 sc0 nt lds\n"
"v_mfma_f32_16x16x32_bf16 a[52:55], a[152:155], v[196:199], a[52:55]\n"
"v_mul_f32 v155, s55, v155\n"
"v_mul_f32 v72, v233, v72\n"
"v_mfma_f32_16x16x32_bf16 a[196:199], a[152:155], v[200:203], a[196:199]\n"
"v_exp_f32 v152, v152\n"
"v_mfma_f32_16x16x32_bf16 a[196:199], a[156:159], v[212:215], a[196:199]\n"
"s_waitcnt lgkmcnt(4)\n"
"v_exp_f32 v153, v153\n"
"v_mfma_f32_16x16x32_bf16 a[8:11], a[160:163], v[192:195], a[8:11]\n"
"ds_read_b64_tr_b16 a[152:153], v241 offset:28672\n"
"ds_read_b64_tr_b16 a[154:155], v241 offset:28928\n"
"v_mfma_f32_16x16x32_bf16 a[8:11], a[164:167], v[204:207], a[8:11]\n"
"s_add_u32 m0, s52, 4864\n"
"ds_read_b64_tr_b16 a[156:157], v241 offset:31744\n"
"ds_read_b64_tr_b16 a[158:159], v241 offset:32000\n"
"v_mfma_f32_16x16x32_bf16 a[56:59], a[164:167], v[208:211], a[56:59]\n"
"buffer_load_dwordx4 v242, s[36:39], s54 offen offset:256 sc0 nt lds\n"
"v_mfma_f32_16x16x32_bf16 a[56:59], a[160:163], v[196:199], a[56:59]\n"
"v_exp_f32 v154, v154\n"
"v_mfma_f32_16x16x32_bf16 a[200:203], a[160:163], v[200:203], a[200:203]\n"
"v_exp_f32 v155, v155\n"
"v_mfma_f32_16x16x32_bf16 a[200:203], a[164:167], v[212:215], a[200:203]\n"
"s_waitcnt lgkmcnt(4)\n"
"v_mul_f32 v73, v233, v73\n"
"v_mul_f32 v74, v233, v74\n"
"v_mfma_f32_16x16x32_bf16 a[12:15], a[144:147], v[192:195], a[12:15]\n"
"s_mov_b32 s59, 294912\n"
"ds_read_b64_tr_b16 a[160:161], v241 offset:28704\n"
"ds_read_b64_tr_b16 a[162:163], v241 offset:28960\n"
"v_mfma_f32_16x16x32_bf16 a[12:15], a[148:151], v[204:207], a[12:15]\n"
"s_add_u32 m0, s52, 21504\n"
"ds_read_b64_tr_b16 a[164:165], v241 offset:31776\n"
"ds_read_b64_tr_b16 a[166:167], v241 offset:32032\n"
"v_mfma_f32_16x16x32_bf16 a[60:63], a[148:151], v[208:211], a[60:63]\n"
"buffer_load_dwordx4 v242, s[40:43], s59 offen offset:0 sc0 nt lds\n"
"v_mfma_f32_16x16x32_bf16 a[60:63], a[144:147], v[196:199], a[60:63]\n"
"v_mul_f32 v75, v233, v75\n"
"v_mul_f32 v76, v233, v76\n"
"v_mfma_f32_16x16x32_bf16 a[204:207], a[144:147], v[200:203], a[204:207]\n"
"v_mul_f32 v77, v233, v77\n"
"v_mul_f32 v78, v233, v78\n"
"v_mfma_f32_16x16x32_bf16 a[204:207], a[148:151], v[212:215], a[204:207]\n"
"s_waitcnt lgkmcnt(4)\n"
"v_mul_f32 v79, v233, v79\n"
"v_cvt_pk_bf16_f32 v72, v72, v73\n"
"v_mfma_f32_16x16x32_bf16 a[16:19], a[152:155], v[192:195], a[16:19]\n"
"ds_read_b64_tr_b16 a[144:145], v241 offset:29184\n"
"ds_read_b64_tr_b16 a[146:147], v241 offset:29440\n"
"v_mfma_f32_16x16x32_bf16 a[16:19], a[156:159], v[204:207], a[16:19]\n"
"s_add_u32 m0, s52, 22400\n"
"ds_read_b64_tr_b16 a[148:149], v241 offset:32256\n"
"ds_read_b64_tr_b16 a[150:151], v241 offset:32512\n"
"v_mfma_f32_16x16x32_bf16 a[64:67], a[156:159], v[208:211], a[64:67]\n"
"buffer_load_dwordx4 v242, s[40:43], s59 offen offset:128 sc0 nt lds\n"
"v_mfma_f32_16x16x32_bf16 a[64:67], a[152:155], v[196:199], a[64:67]\n"
"v_cvt_pk_bf16_f32 v73, v74, v75\n"
"v_cvt_pk_bf16_f32 v74, v76, v77\n"
"v_mfma_f32_16x16x32_bf16 a[208:211], a[152:155], v[200:203], a[208:211]\n"
"v_cvt_pk_bf16_f32 v75, v78, v79\n"
"v_mul_f32 v156, s55, v156\n"
"v_mfma_f32_16x16x32_bf16 a[208:211], a[156:159], v[212:215], a[208:211]\n"
"s_waitcnt lgkmcnt(4)\n"
"v_mul_f32 v157, s55, v157\n"
"v_mul_f32 v158, s55, v158\n"
"v_mfma_f32_16x16x32_bf16 a[20:23], a[160:163], v[192:195], a[20:23]\n"
"ds_read_b64_tr_b16 a[152:153], v241 offset:29216\n"
"ds_read_b64_tr_b16 a[154:155], v241 offset:29472\n"
"v_mfma_f32_16x16x32_bf16 a[20:23], a[164:167], v[204:207], a[20:23]\n"
"s_add_u32 m0, s52, 23296\n"
"ds_read_b64_tr_b16 a[156:157], v241 offset:32288\n"
"ds_read_b64_tr_b16 a[158:159], v241 offset:32544\n"
"v_mfma_f32_16x16x32_bf16 a[68:71], a[164:167], v[208:211], a[68:71]\n"
"buffer_load_dwordx4 v242, s[40:43], s59 offen offset:256 sc0 nt lds\n"
"v_mfma_f32_16x16x32_bf16 a[68:71], a[160:163], v[196:199], a[68:71]\n"
"v_permlane16_swap_b32 v72, v74\n"
"v_mfma_f32_16x16x32_bf16 a[212:215], a[160:163], v[200:203], a[212:215]\n"
"v_permlane16_swap_b32 v73, v75\n"
"v_mfma_f32_16x16x32_bf16 a[212:215], a[164:167], v[212:215], a[212:215]\n"
"s_waitcnt lgkmcnt(4)\n"
"v_mul_f32 v159, s55, v159\n"
"v_mul_f32 v160, s55, v160\n"
"v_mfma_f32_16x16x32_bf16 a[24:27], a[144:147], v[192:195], a[24:27]\n"
"s_mov_b32 s70, 307200\n"
"ds_read_b64_tr_b16 a[160:161], v241 offset:29696\n"
"ds_read_b64_tr_b16 a[162:163], v241 offset:29952\n"
"v_mfma_f32_16x16x32_bf16 a[24:27], a[148:151], v[204:207], a[24:27]\n"
"s_add_u32 m0, s52, 24576\n"
"ds_read_b64_tr_b16 a[164:165], v241 offset:32768\n"
"ds_read_b64_tr_b16 a[166:167], v241 offset:33024\n"
"v_mfma_f32_16x16x32_bf16 a[168:171], a[148:151], v[208:211], a[168:171]\n"
"buffer_load_dwordx4 v242, s[40:43], s70 offen offset:0 sc0 nt lds\n"
"v_mfma_f32_16x16x32_bf16 a[168:171], a[144:147], v[196:199], a[168:171]\n"
"v_exp_f32 v156, v156\n"
"v_mfma_f32_16x16x32_bf16 a[216:219], a[144:147], v[200:203], a[216:219]\n"
"v_exp_f32 v157, v157\n"
"v_mfma_f32_16x16x32_bf16 a[216:219], a[148:151], v[212:215], a[216:219]\n"
"s_waitcnt lgkmcnt(4)\n"
"v_exp_f32 v158, v158\n"
"v_mfma_f32_16x16x32_bf16 a[28:31], a[152:155], v[192:195], a[28:31]\n"
"ds_read_b64_tr_b16 a[144:145], v241 offset:29728\n"
"ds_read_b64_tr_b16 a[146:147], v241 offset:29984\n"
"v_mfma_f32_16x16x32_bf16 a[28:31], a[156:159], v[204:207], a[28:31]\n"
"s_add_u32 m0, s52, 25472\n"
"ds_read_b64_tr_b16 a[148:149], v241 offset:32800\n"
"ds_read_b64_tr_b16 a[150:151], v241 offset:33056\n"
"v_mfma_f32_16x16x32_bf16 a[172:175], a[156:159], v[208:211], a[172:175]\n"
"buffer_load_dwordx4 v242, s[40:43], s70 offen offset:128 sc0 nt lds\n"
"v_mfma_f32_16x16x32_bf16 a[172:175], a[152:155], v[196:199], a[172:175]\n"
"v_exp_f32 v159, v159\n"
"v_mfma_f32_16x16x32_bf16 a[220:223], a[152:155], v[200:203], a[220:223]\n"
"v_mul_f32 v161, s55, v161\n"
"v_mul_f32 v162, s55, v162\n"
"v_mfma_f32_16x16x32_bf16 a[220:223], a[156:159], v[212:215], a[220:223]\n"
"s_waitcnt lgkmcnt(4)\n"
"v_mul_f32 v163, s55, v163\n"
"v_mul_f32 v164, s55, v164\n"
"v_mfma_f32_16x16x32_bf16 a[32:35], a[160:163], v[192:195], a[32:35]\n"
"ds_read_b64_tr_b16 a[152:153], v241 offset:30208\n"
"ds_read_b64_tr_b16 a[154:155], v241 offset:30464\n"
"v_mfma_f32_16x16x32_bf16 a[32:35], a[164:167], v[204:207], a[32:35]\n"
"s_add_u32 m0, s52, 26368\n"
"ds_read_b64_tr_b16 a[156:157], v241 offset:33280\n"
"ds_read_b64_tr_b16 a[158:159], v241 offset:33536\n"
"v_mfma_f32_16x16x32_bf16 a[176:179], a[164:167], v[208:211], a[176:179]\n"
"buffer_load_dwordx4 v242, s[40:43], s70 offen offset:256 sc0 nt lds\n"
"v_mfma_f32_16x16x32_bf16 a[176:179], a[160:163], v[196:199], a[176:179]\n"
"v_exp_f32 v160, v160\n"
"v_mfma_f32_16x16x32_bf16 a[224:227], a[160:163], v[200:203], a[224:227]\n"
"v_exp_f32 v161, v161\n"
"v_mfma_f32_16x16x32_bf16 a[224:227], a[164:167], v[212:215], a[224:227]\n"
"s_waitcnt lgkmcnt(4)\n"
"v_exp_f32 v162, v162\n"
"v_mfma_f32_16x16x32_bf16 a[36:39], a[144:147], v[192:195], a[36:39]\n"
"ds_read_b64_tr_b16 a[160:161], v241 offset:30240\n"
"ds_read_b64_tr_b16 a[162:163], v241 offset:30496\n"
"v_mfma_f32_16x16x32_bf16 a[36:39], a[148:151], v[204:207], a[36:39]\n"
"ds_read_b64_tr_b16 a[164:165], v241 offset:33312\n"
"ds_read_b64_tr_b16 a[166:167], v241 offset:33568\n"
"v_mfma_f32_16x16x32_bf16 a[180:183], a[148:151], v[208:211], a[180:183]\n"
"v_exp_f32 v163, v163\n"
"v_mfma_f32_16x16x32_bf16 a[180:183], a[144:147], v[196:199], a[180:183]\n"
"v_mul_f32 v165, s55, v165\n"
"v_mul_f32 v166, s55, v166\n"
"v_mfma_f32_16x16x32_bf16 a[228:231], a[144:147], v[200:203], a[228:231]\n"
"v_mul_f32 v167, s55, v167\n"
"v_mul_f32 v168, s55, v168\n"
"v_mfma_f32_16x16x32_bf16 a[228:231], a[148:151], v[212:215], a[228:231]\n"
"s_waitcnt lgkmcnt(4)\n"
"v_exp_f32 v164, v164\n"
"v_mfma_f32_16x16x32_bf16 a[40:43], a[152:155], v[192:195], a[40:43]\n"
"ds_read_b128 a[144:147], v240 offset:9216\n"
"ds_read_b128 a[148:151], v240 offset:9728\n"
"v_mfma_f32_16x16x32_bf16 a[40:43], a[156:159], v[204:207], a[40:43]\n"
"v_exp_f32 v165, v165\n"
"v_mfma_f32_16x16x32_bf16 a[184:187], a[156:159], v[208:211], a[184:187]\n"
"v_exp_f32 v166, v166\n"
"v_mfma_f32_16x16x32_bf16 a[184:187], a[152:155], v[196:199], a[184:187]\n"
"v_exp_f32 v167, v167\n"
"v_mfma_f32_16x16x32_bf16 a[232:235], a[152:155], v[200:203], a[232:235]\n"
"v_mul_f32 v169, s55, v169\n"
"v_mul_f32 v170, s55, v170\n"
"v_mfma_f32_16x16x32_bf16 a[232:235], a[156:159], v[212:215], a[232:235]\n"
"s_waitcnt lgkmcnt(2)\n"
"v_mul_f32 v171, s55, v171\n"
"v_mul_f32 v172, s55, v172\n"
"v_mfma_f32_16x16x32_bf16 a[44:47], a[160:163], v[192:195], a[44:47]\n"
"ds_read_b128 a[152:155], v240 offset:10240\n"
"ds_read_b128 a[156:159], v240 offset:10752\n"
"v_mfma_f32_16x16x32_bf16 a[44:47], a[164:167], v[204:207], a[44:47]\n"
"v_cvt_pk_bf16_f32 v192, v144, v145\n"
"v_cvt_pk_bf16_f32 v193, v146, v147\n"
"v_mfma_f32_16x16x32_bf16 a[188:191], a[164:167], v[208:211], a[188:191]\n"
"v_cvt_pk_bf16_f32 v194, v156, v157\n"
"v_cvt_pk_bf16_f32 v195, v158, v159\n"
"v_mfma_f32_16x16x32_bf16 a[188:191], a[160:163], v[196:199], a[188:191]\n"
"v_cvt_pk_bf16_f32 v196, v148, v149\n"
"v_cvt_pk_bf16_f32 v197, v150, v151\n"
"v_mfma_f32_16x16x32_bf16 a[236:239], a[160:163], v[200:203], a[236:239]\n"
"v_cvt_pk_bf16_f32 v200, v152, v153\n"
"v_cvt_pk_bf16_f32 v201, v154, v155\n"
"v_mfma_f32_16x16x32_bf16 a[236:239], a[164:167], v[212:215], a[236:239]\n"
"s_waitcnt lgkmcnt(2)\n"
"v_cvt_pk_bf16_f32 v198, v160, v161\n"
"v_cvt_pk_bf16_f32 v199, v162, v163\n"
"v_mfma_f32_16x16x32_bf16 v[144:147], a[144:147], a[72:75], 0\n"
"ds_read_b128 a[160:163], v240 offset:11264\n"
"ds_read_b128 a[164:167], v240 offset:11776\n"
"v_mfma_f32_16x16x32_bf16 v[144:147], a[148:151], a[76:79], v[144:147]\n"
"v_cvt_pk_bf16_f32 v202, v164, v165\n"
"v_cvt_pk_bf16_f32 v203, v166, v167\n"
"v_mfma_f32_16x16x32_bf16 v[148:151], a[148:151], a[100:103], 0\n"
"v_exp_f32 v168, v168\n"
"v_mfma_f32_16x16x32_bf16 v[148:151], a[144:147], a[96:99], v[148:151]\n"
"v_exp_f32 v169, v169\n"
"v_mfma_f32_16x16x32_bf16 v[152:155], a[144:147], a[120:123], 0\n"
"buffer_store_dwordx4 v[64:67], v244, s[48:51], s61 offen offset:128\n"
"v_mfma_f32_16x16x32_bf16 v[152:155], a[148:151], a[124:127], v[152:155]\n"
"s_waitcnt lgkmcnt(2)\n"
"v_exp_f32 v170, v170\n"
"v_mfma_f32_16x16x32_bf16 v[144:147], a[152:155], a[80:83], v[144:147]\n"
"ds_read_b128 a[144:147], v240 offset:9472\n"
"ds_read_b128 a[148:151], v240 offset:9984\n"
"v_mfma_f32_16x16x32_bf16 v[144:147], a[156:159], a[84:87], v[144:147]\n"
"v_exp_f32 v171, v171\n"
"v_mfma_f32_16x16x32_bf16 v[148:151], a[156:159], a[108:111], v[148:151]\n"
"v_mul_f32 v173, s55, v173\n"
"v_mul_f32 v174, s55, v174\n"
"v_mfma_f32_16x16x32_bf16 v[148:151], a[152:155], a[104:107], v[148:151]\n"
"v_mul_f32 v175, s55, v175\n"
"v_cvt_pk_bf16_f32 v204, v168, v169\n"
"v_mfma_f32_16x16x32_bf16 v[152:155], a[152:155], a[128:131], v[152:155]\n"
"v_cvt_pk_bf16_f32 v205, v170, v171\n"
"v_mul_f32 v176, s55, v176\n"
"v_mfma_f32_16x16x32_bf16 v[152:155], a[156:159], a[132:135], v[152:155]\n"
"s_waitcnt lgkmcnt(2)\n"
"v_exp_f32 v172, v172\n"
"v_mfma_f32_16x16x32_bf16 v[144:147], a[160:163], a[88:91], v[144:147]\n"
"ds_read_b128 a[152:155], v240 offset:10496\n"
"ds_read_b128 a[156:159], v240 offset:11008\n"
"v_mfma_f32_16x16x32_bf16 v[144:147], a[164:167], a[92:95], v[144:147]\n"
"v_exp_f32 v173, v173\n"
"v_mfma_f32_16x16x32_bf16 v[148:151], a[164:167], a[116:119], v[148:151]\n"
"v_exp_f32 v174, v174\n"
"v_mfma_f32_16x16x32_bf16 v[148:151], a[160:163], a[112:115], v[148:151]\n"
"v_exp_f32 v175, v175\n"
"v_mfma_f32_16x16x32_bf16 v[152:155], a[160:163], a[136:139], v[152:155]\n"
"v_mul_f32 v177, s55, v177\n"
"v_mul_f32 v178, s55, v178\n"
"v_mfma_f32_16x16x32_bf16 v[152:155], a[164:167], a[140:143], v[152:155]\n"
"s_waitcnt lgkmcnt(2)\n"
"v_mul_f32 v179, s55, v179\n"
"v_cvt_pk_bf16_f32 v208, v172, v173\n"
"v_mfma_f32_16x16x32_bf16 v[156:159], a[144:147], a[72:75], 0\n"
"ds_read_b128 a[160:163], v240 offset:11520\n"
"ds_read_b128 a[164:167], v240 offset:12032\n"
"v_mfma_f32_16x16x32_bf16 v[156:159], a[148:151], a[76:79], v[156:159]\n"
"v_cvt_pk_bf16_f32 v209, v174, v175\n"
"v_mul_f32 v80, v233, v80\n"
"v_mfma_f32_16x16x32_bf16 v[160:163], a[148:151], a[100:103], 0\n"
"v_exp_f32 v176, v176\n"
"v_mfma_f32_16x16x32_bf16 v[160:163], a[144:147], a[96:99], v[160:163]\n"
"v_exp_f32 v177, v177\n"
"v_mfma_f32_16x16x32_bf16 v[164:167], a[144:147], a[120:123], 0\n"
"buffer_store_dwordx4 v[72:75], v244, s[48:51], s61 offen offset:192\n"
"v_mfma_f32_16x16x32_bf16 v[164:167], a[148:151], a[124:127], v[164:167]\n"
"s_waitcnt lgkmcnt(2)\n"
"v_exp_f32 v178, v178\n"
"v_mfma_f32_16x16x32_bf16 v[156:159], a[152:155], a[80:83], v[156:159]\n"
"ds_read_b128 a[144:147], v240 offset:12288\n"
"ds_read_b128 a[148:151], v240 offset:12800\n"
"v_mfma_f32_16x16x32_bf16 v[156:159], a[156:159], a[84:87], v[156:159]\n"
"v_exp_f32 v179, v179\n"
"v_mfma_f32_16x16x32_bf16 v[160:163], a[156:159], a[108:111], v[160:163]\n"
"v_mul_f32 v81, v233, v81\n"
"v_mul_f32 v82, v233, v82\n"
"v_mfma_f32_16x16x32_bf16 v[160:163], a[152:155], a[104:107], v[160:163]\n"
"v_mul_f32 v83, v233, v83\n"
"v_mul_f32 v84, v233, v84\n"
"v_mfma_f32_16x16x32_bf16 v[164:167], a[152:155], a[128:131], v[164:167]\n"
"v_mul_f32 v85, v233, v85\n"
"v_mul_f32 v86, v233, v86\n"
"v_mfma_f32_16x16x32_bf16 v[164:167], a[156:159], a[132:135], v[164:167]\n"
"s_waitcnt lgkmcnt(2)\n"
"v_mul_f32 v87, v233, v87\n"
"v_cvt_pk_bf16_f32 v80, v80, v81\n"
"v_mfma_f32_16x16x32_bf16 v[156:159], a[160:163], a[88:91], v[156:159]\n"
"ds_read_b128 a[152:155], v240 offset:13312\n"
"ds_read_b128 a[156:159], v240 offset:13824\n"
"v_mfma_f32_16x16x32_bf16 v[156:159], a[164:167], a[92:95], v[156:159]\n"
"v_cvt_pk_bf16_f32 v81, v82, v83\n"
"v_cvt_pk_bf16_f32 v82, v84, v85\n"
"v_mfma_f32_16x16x32_bf16 v[160:163], a[164:167], a[116:119], v[160:163]\n"
"v_cvt_pk_bf16_f32 v83, v86, v87\n"
"v_cvt_pk_bf16_f32 v212, v176, v177\n"
"v_mfma_f32_16x16x32_bf16 v[160:163], a[160:163], a[112:115], v[160:163]\n"
"v_cvt_pk_bf16_f32 v213, v178, v179\n"
"v_mul_f32 v180, s55, v180\n"
"v_mfma_f32_16x16x32_bf16 v[164:167], a[160:163], a[136:139], v[164:167]\n"
"v_permlane16_swap_b32 v80, v82\n"
"v_mfma_f32_16x16x32_bf16 v[164:167], a[164:167], a[140:143], v[164:167]\n"
"s_waitcnt lgkmcnt(2)\n"
"v_permlane16_swap_b32 v81, v83\n"
"v_mfma_f32_16x16x32_bf16 v[168:171], a[144:147], a[72:75], 0\n"
"ds_read_b128 a[160:163], v240 offset:14336\n"
"ds_read_b128 a[164:167], v240 offset:14848\n"
"v_mfma_f32_16x16x32_bf16 v[168:171], a[148:151], a[76:79], v[168:171]\n"
"v_mul_f32 v181, s55, v181\n"
"v_mul_f32 v182, s55, v182\n"
"v_mfma_f32_16x16x32_bf16 v[172:175], a[148:151], a[100:103], 0\n"
"v_mul_f32 v183, s55, v183\n"
"v_mul_f32 v184, s55, v184\n"
"v_mfma_f32_16x16x32_bf16 v[172:175], a[144:147], a[96:99], v[172:175]\n"
"v_exp_f32 v180, v180\n"
"v_mfma_f32_16x16x32_bf16 v[176:179], a[144:147], a[120:123], 0\n"
"buffer_store_dwordx4 v[80:83], v244, s[48:51], s61 offen offset:256\n"
"v_mfma_f32_16x16x32_bf16 v[176:179], a[148:151], a[124:127], v[176:179]\n"
"s_waitcnt lgkmcnt(2)\n"
"v_exp_f32 v181, v181\n"
"v_mfma_f32_16x16x32_bf16 v[168:171], a[152:155], a[80:83], v[168:171]\n"
"ds_read_b128 a[144:147], v240 offset:12544\n"
"ds_read_b128 a[148:151], v240 offset:13056\n"
"v_mfma_f32_16x16x32_bf16 v[168:171], a[156:159], a[84:87], v[168:171]\n"
"v_exp_f32 v182, v182\n"
"v_mfma_f32_16x16x32_bf16 v[172:175], a[156:159], a[108:111], v[172:175]\n"
"v_exp_f32 v183, v183\n"
"v_mfma_f32_16x16x32_bf16 v[172:175], a[152:155], a[104:107], v[172:175]\n"
"v_mul_f32 v185, s55, v185\n"
"v_mul_f32 v186, s55, v186\n"
"v_mfma_f32_16x16x32_bf16 v[176:179], a[152:155], a[128:131], v[176:179]\n"
"v_mul_f32 v187, s55, v187\n"
"v_cvt_pk_bf16_f32 v206, v180, v181\n"
"v_mfma_f32_16x16x32_bf16 v[176:179], a[156:159], a[132:135], v[176:179]\n"
"s_waitcnt lgkmcnt(2)\n"
"v_cvt_pk_bf16_f32 v207, v182, v183\n"
"v_mul_f32 v188, s55, v188\n"
"v_mfma_f32_16x16x32_bf16 v[168:171], a[160:163], a[88:91], v[168:171]\n"
"ds_read_b128 a[152:155], v240 offset:13568\n"
"ds_read_b128 a[156:159], v240 offset:14080\n"
"v_mfma_f32_16x16x32_bf16 v[168:171], a[164:167], a[92:95], v[168:171]\n"
"v_exp_f32 v184, v184\n"
"v_mfma_f32_16x16x32_bf16 v[172:175], a[164:167], a[116:119], v[172:175]\n"
"v_exp_f32 v185, v185\n"
"v_mfma_f32_16x16x32_bf16 v[172:175], a[160:163], a[112:115], v[172:175]\n"
"v_exp_f32 v186, v186\n"
"v_mfma_f32_16x16x32_bf16 v[176:179], a[160:163], a[136:139], v[176:179]\n"
"v_exp_f32 v187, v187\n"
"v_mfma_f32_16x16x32_bf16 v[176:179], a[164:167], a[140:143], v[176:179]\n"
"s_waitcnt lgkmcnt(2)\n"
"v_mul_f32 v189, s55, v189\n"
"v_mul_f32 v190, s55, v190\n"
"v_mul_f32 v191, s55, v191\n"
"v_mfma_f32_16x16x32_bf16 v[180:183], a[144:147], a[72:75], 0\n"
"ds_read_b128 a[160:163], v240 offset:14592\n"
"ds_read_b128 a[164:167], v240 offset:15104\n"
"v_mfma_f32_16x16x32_bf16 v[180:183], a[148:151], a[76:79], v[180:183]\n"
"v_cvt_pk_bf16_f32 v210, v184, v185\n"
"v_cvt_pk_bf16_f32 v211, v186, v187\n"
"v_exp_f32 v188, v188\n"
"v_exp_f32 v189, v189\n"
"v_exp_f32 v190, v190\n"
"v_exp_f32 v191, v191\n"
"v_mul_f32 v88, v233, v88\n"
"v_mul_f32 v89, v233, v89\n"
"v_mul_f32 v90, v233, v90\n"
"v_mul_f32 v91, v233, v91\n"
"v_mul_f32 v92, v233, v92\n"
"v_mul_f32 v93, v233, v93\n"
"v_mul_f32 v94, v233, v94\n"
"v_mul_f32 v95, v233, v95\n"
"v_cvt_pk_bf16_f32 v88, v88, v89\n"
"v_cvt_pk_bf16_f32 v89, v90, v91\n"
"v_cvt_pk_bf16_f32 v90, v92, v93\n"
"v_cvt_pk_bf16_f32 v91, v94, v95\n"
"v_mfma_f32_16x16x32_bf16 v[184:187], a[148:151], a[100:103], 0\n"
"v_cvt_pk_bf16_f32 v214, v188, v189\n"
"v_cvt_pk_bf16_f32 v215, v190, v191\n"
"v_mfma_f32_16x16x32_bf16 v[184:187], a[144:147], a[96:99], v[184:187]\n"
"s_waitcnt vmcnt(19)\n"
"v_permlane16_swap_b32 v88, v90\n"
"v_permlane16_swap_b32 v89, v91\n"
"v_mfma_f32_16x16x32_bf16 v[188:191], a[144:147], a[120:123], 0\n"
"s_barrier\n"
"buffer_store_dwordx4 v[88:91], v244, s[48:51], s61 offen offset:320\n"
"v_mfma_f32_16x16x32_bf16 v[188:191], a[148:151], a[124:127], v[188:191]\n"
"s_waitcnt lgkmcnt(2)\n"
"v_mul_f32 v96, v234, v96\n"
"v_mul_f32 v97, v234, v97\n"
"v_mfma_f32_16x16x32_bf16 v[180:183], a[152:155], a[80:83], v[180:183]\n"
"s_mov_b32 s53, 368640\n"
"ds_read_b64_tr_b16 a[144:145], v241 offset:33792\n"
"ds_read_b64_tr_b16 a[146:147], v241 offset:34048\n"
"v_mfma_f32_16x16x32_bf16 v[180:183], a[156:159], a[84:87], v[180:183]\n"
"s_add_u32 m0, s52, 6144\n"
"ds_read_b64_tr_b16 a[148:149], v241 offset:36864\n"
"ds_read_b64_tr_b16 a[150:151], v241 offset:37120\n"
"v_mfma_f32_16x16x32_bf16 v[184:187], a[156:159], a[108:111], v[184:187]\n"
"buffer_load_dwordx4 v242, s[36:39], s53 offen offset:0 sc0 nt lds\n"
"v_mfma_f32_16x16x32_bf16 v[184:187], a[152:155], a[104:107], v[184:187]\n"
"v_mul_f32 v98, v234, v98\n"
"v_mul_f32 v99, v234, v99\n"
"v_mfma_f32_16x16x32_bf16 v[188:191], a[152:155], a[128:131], v[188:191]\n"
"v_mul_f32 v100, v234, v100\n"
"v_mul_f32 v101, v234, v101\n"
"v_mfma_f32_16x16x32_bf16 v[188:191], a[156:159], a[132:135], v[188:191]\n"
"s_waitcnt lgkmcnt(4)\n"
"v_mul_f32 v102, v234, v102\n"
"v_mul_f32 v103, v234, v103\n"
"v_mfma_f32_16x16x32_bf16 v[180:183], a[160:163], a[88:91], v[180:183]\n"
"ds_read_b64_tr_b16 a[152:153], v241 offset:33824\n"
"ds_read_b64_tr_b16 a[154:155], v241 offset:34080\n"
"v_mfma_f32_16x16x32_bf16 v[180:183], a[164:167], a[92:95], v[180:183]\n"
"s_add_u32 m0, s52, 7040\n"
"ds_read_b64_tr_b16 a[156:157], v241 offset:36896\n"
"ds_read_b64_tr_b16 a[158:159], v241 offset:37152\n"
"v_mfma_f32_16x16x32_bf16 v[184:187], a[164:167], a[116:119], v[184:187]\n"
"buffer_load_dwordx4 v242, s[36:39], s53 offen offset:128 sc0 nt lds\n"
"v_mfma_f32_16x16x32_bf16 v[184:187], a[160:163], a[112:115], v[184:187]\n"
"v_mul_f32 v144, s55, v144\n"
"v_mul_f32 v145, s55, v145\n"
"v_mfma_f32_16x16x32_bf16 v[188:191], a[160:163], a[136:139], v[188:191]\n"
"v_mul_f32 v146, s55, v146\n"
"v_mul_f32 v147, s55, v147\n"
"v_mfma_f32_16x16x32_bf16 v[188:191], a[164:167], a[140:143], v[188:191]\n"
"v_cvt_pk_bf16_f32 v96, v96, v97\n"
"v_cvt_pk_bf16_f32 v97, v98, v99\n"
"v_mfma_f32_16x16x32_bf16 a[240:243], a[252:255], v[192:195], a[240:243]\n"
"v_cvt_pk_bf16_f32 v98, v100, v101\n"
"v_cvt_pk_bf16_f32 v99, v102, v103\n"
"v_mfma_f32_16x16x32_bf16 a[240:243], a[252:255], v[204:207], a[240:243]\n"
"s_add_u32 m0, s52, 7936\n"
"v_exp_f32 v144, v144\n"
"v_mfma_f32_16x16x32_bf16 a[244:247], a[252:255], v[196:199], a[244:247]\n"
"buffer_load_dwordx4 v242, s[36:39], s53 offen offset:256 sc0 nt lds\n"
"v_mfma_f32_16x16x32_bf16 a[244:247], a[252:255], v[208:211], a[244:247]\n"
"v_exp_f32 v145, v145\n"
"v_mfma_f32_16x16x32_bf16 a[248:251], a[252:255], v[200:203], a[248:251]\n"
"v_exp_f32 v146, v146\n"
"v_mfma_f32_16x16x32_bf16 a[248:251], a[252:255], v[212:215], a[248:251]\n"
"s_waitcnt lgkmcnt(4)\n"
"v_exp_f32 v147, v147\n"
"v_mfma_f32_16x16x32_bf16 a[0:3], a[144:147], v[192:195], a[0:3]\n"
"s_mov_b32 s54, 380928\n"
"ds_read_b64_tr_b16 a[160:161], v241 offset:34304\n"
"ds_read_b64_tr_b16 a[162:163], v241 offset:34560\n"
"v_mfma_f32_16x16x32_bf16 a[0:3], a[148:151], v[204:207], a[0:3]\n"
"s_add_u32 m0, s52, 9216\n"
"ds_read_b64_tr_b16 a[164:165], v241 offset:37376\n"
"ds_read_b64_tr_b16 a[166:167], v241 offset:37632\n"
"v_mfma_f32_16x16x32_bf16 a[48:51], a[148:151], v[208:211], a[48:51]\n"
"buffer_load_dwordx4 v242, s[36:39], s54 offen offset:0 sc0 nt lds\n"
"v_mfma_f32_16x16x32_bf16 a[48:51], a[144:147], v[196:199], a[48:51]\n"
"v_mul_f32 v148, s55, v148\n"
"v_mul_f32 v149, s55, v149\n"
"v_mfma_f32_16x16x32_bf16 a[192:195], a[144:147], v[200:203], a[192:195]\n"
"v_mul_f32 v150, s55, v150\n"
"v_mul_f32 v151, s55, v151\n"
"v_mfma_f32_16x16x32_bf16 a[192:195], a[148:151], v[212:215], a[192:195]\n"
"s_waitcnt lgkmcnt(4)\n"
"v_permlane16_swap_b32 v96, v98\n"
"v_mfma_f32_16x16x32_bf16 a[4:7], a[152:155], v[192:195], a[4:7]\n"
"ds_read_b64_tr_b16 a[144:145], v241 offset:34336\n"
"ds_read_b64_tr_b16 a[146:147], v241 offset:34592\n"
"v_mfma_f32_16x16x32_bf16 a[4:7], a[156:159], v[204:207], a[4:7]\n"
"s_add_u32 m0, s52, 10112\n"
"ds_read_b64_tr_b16 a[148:149], v241 offset:37408\n"
"ds_read_b64_tr_b16 a[150:151], v241 offset:37664\n"
"v_mfma_f32_16x16x32_bf16 a[52:55], a[156:159], v[208:211], a[52:55]\n"
"buffer_load_dwordx4 v242, s[36:39], s54 offen offset:128 sc0 nt lds\n"
"v_mfma_f32_16x16x32_bf16 a[52:55], a[152:155], v[196:199], a[52:55]\n"
"v_permlane16_swap_b32 v97, v99\n"
"v_mfma_f32_16x16x32_bf16 a[196:199], a[152:155], v[200:203], a[196:199]\n"
"v_exp_f32 v148, v148\n"
"v_mfma_f32_16x16x32_bf16 a[196:199], a[156:159], v[212:215], a[196:199]\n"
"s_waitcnt lgkmcnt(4)\n"
"v_exp_f32 v149, v149\n"
"v_mfma_f32_16x16x32_bf16 a[8:11], a[160:163], v[192:195], a[8:11]\n"
"ds_read_b64_tr_b16 a[152:153], v241 offset:34816\n"
"ds_read_b64_tr_b16 a[154:155], v241 offset:35072\n"
"v_mfma_f32_16x16x32_bf16 a[8:11], a[164:167], v[204:207], a[8:11]\n"
"s_add_u32 m0, s52, 11008\n"
"ds_read_b64_tr_b16 a[156:157], v241 offset:37888\n"
"ds_read_b64_tr_b16 a[158:159], v241 offset:38144\n"
"v_mfma_f32_16x16x32_bf16 a[56:59], a[164:167], v[208:211], a[56:59]\n"
"buffer_load_dwordx4 v242, s[36:39], s54 offen offset:256 sc0 nt lds\n"
"v_mfma_f32_16x16x32_bf16 a[56:59], a[160:163], v[196:199], a[56:59]\n"
"v_exp_f32 v150, v150\n"
"v_mfma_f32_16x16x32_bf16 a[200:203], a[160:163], v[200:203], a[200:203]\n"
"v_exp_f32 v151, v151\n"
"v_mfma_f32_16x16x32_bf16 a[200:203], a[164:167], v[212:215], a[200:203]\n"
"s_waitcnt lgkmcnt(4)\n"
"v_mul_f32 v152, s55, v152\n"
"v_mul_f32 v153, s55, v153\n"
"v_mfma_f32_16x16x32_bf16 a[12:15], a[144:147], v[192:195], a[12:15]\n"
"s_mov_b32 s59, 319488\n"
"ds_read_b64_tr_b16 a[160:161], v241 offset:34848\n"
"ds_read_b64_tr_b16 a[162:163], v241 offset:35104\n"
"v_mfma_f32_16x16x32_bf16 a[12:15], a[148:151], v[204:207], a[12:15]\n"
"s_add_u32 m0, s52, 27648\n"
"ds_read_b64_tr_b16 a[164:165], v241 offset:37920\n"
"ds_read_b64_tr_b16 a[166:167], v241 offset:38176\n"
"v_mfma_f32_16x16x32_bf16 a[60:63], a[148:151], v[208:211], a[60:63]\n"
"buffer_load_dwordx4 v242, s[40:43], s59 offen offset:0 sc0 nt lds\n"
"v_mfma_f32_16x16x32_bf16 a[60:63], a[144:147], v[196:199], a[60:63]\n"
"v_mul_f32 v154, s55, v154\n"
"v_mul_f32 v155, s55, v155\n"
"v_mfma_f32_16x16x32_bf16 a[204:207], a[144:147], v[200:203], a[204:207]\n"
"v_exp_f32 v152, v152\n"
"v_mfma_f32_16x16x32_bf16 a[204:207], a[148:151], v[212:215], a[204:207]\n"
"s_waitcnt lgkmcnt(4)\n"
"v_exp_f32 v153, v153\n"
"v_mfma_f32_16x16x32_bf16 a[16:19], a[152:155], v[192:195], a[16:19]\n"
"ds_read_b64_tr_b16 a[144:145], v241 offset:35328\n"
"ds_read_b64_tr_b16 a[146:147], v241 offset:35584\n"
"v_mfma_f32_16x16x32_bf16 a[16:19], a[156:159], v[204:207], a[16:19]\n"
"s_add_u32 m0, s52, 28544\n"
"ds_read_b64_tr_b16 a[148:149], v241 offset:38400\n"
"ds_read_b64_tr_b16 a[150:151], v241 offset:38656\n"
"v_mfma_f32_16x16x32_bf16 a[64:67], a[156:159], v[208:211], a[64:67]\n"
"buffer_load_dwordx4 v242, s[40:43], s59 offen offset:128 sc0 nt lds\n"
"v_mfma_f32_16x16x32_bf16 a[64:67], a[152:155], v[196:199], a[64:67]\n"
"v_exp_f32 v154, v154\n"
"v_mfma_f32_16x16x32_bf16 a[208:211], a[152:155], v[200:203], a[208:211]\n"
"v_exp_f32 v155, v155\n"
"v_mfma_f32_16x16x32_bf16 a[208:211], a[156:159], v[212:215], a[208:211]\n"
"s_waitcnt lgkmcnt(4)\n"
"v_mul_f32 v156, s55, v156\n"
"v_mul_f32 v157, s55, v157\n"
"v_mfma_f32_16x16x32_bf16 a[20:23], a[160:163], v[192:195], a[20:23]\n"
"ds_read_b64_tr_b16 a[152:153], v241 offset:35360\n"
"ds_read_b64_tr_b16 a[154:155], v241 offset:35616\n"
"v_mfma_f32_16x16x32_bf16 a[20:23], a[164:167], v[204:207], a[20:23]\n"
"s_add_u32 m0, s52, 29440\n"
"ds_read_b64_tr_b16 a[156:157], v241 offset:38432\n"
"ds_read_b64_tr_b16 a[158:159], v241 offset:38688\n"
"v_mfma_f32_16x16x32_bf16 a[68:71], a[164:167], v[208:211], a[68:71]\n"
"buffer_load_dwordx4 v242, s[40:43], s59 offen offset:256 sc0 nt lds\n"
"v_mfma_f32_16x16x32_bf16 a[68:71], a[160:163], v[196:199], a[68:71]\n"
"v_mul_f32 v158, s55, v158\n"
"v_mul_f32 v159, s55, v159\n"
"v_mfma_f32_16x16x32_bf16 a[212:215], a[160:163], v[200:203], a[212:215]\n"
"v_exp_f32 v156, v156\n"
"v_mfma_f32_16x16x32_bf16 a[212:215], a[164:167], v[212:215], a[212:215]\n"
"s_waitcnt lgkmcnt(4)\n"
"v_exp_f32 v157, v157\n"
"v_mfma_f32_16x16x32_bf16 a[24:27], a[144:147], v[192:195], a[24:27]\n"
"s_mov_b32 s70, 331776\n"
"ds_read_b64_tr_b16 a[160:161], v241 offset:35840\n"
"ds_read_b64_tr_b16 a[162:163], v241 offset:36096\n"
"v_mfma_f32_16x16x32_bf16 a[24:27], a[148:151], v[204:207], a[24:27]\n"
"s_add_u32 m0, s52, 30720\n"
"ds_read_b64_tr_b16 a[164:165], v241 offset:38912\n"
"ds_read_b64_tr_b16 a[166:167], v241 offset:39168\n"
"v_mfma_f32_16x16x32_bf16 a[168:171], a[148:151], v[208:211], a[168:171]\n"
"buffer_load_dwordx4 v242, s[40:43], s70 offen offset:0 sc0 nt lds\n"
"v_mfma_f32_16x16x32_bf16 a[168:171], a[144:147], v[196:199], a[168:171]\n"
"v_exp_f32 v158, v158\n"
"v_mfma_f32_16x16x32_bf16 a[216:219], a[144:147], v[200:203], a[216:219]\n"
"v_exp_f32 v159, v159\n"
"v_mfma_f32_16x16x32_bf16 a[216:219], a[148:151], v[212:215], a[216:219]\n"
"s_waitcnt lgkmcnt(4)\n"
"v_mul_f32 v160, s55, v160\n"
"v_mul_f32 v161, s55, v161\n"
"v_mfma_f32_16x16x32_bf16 a[28:31], a[152:155], v[192:195], a[28:31]\n"
"ds_read_b64_tr_b16 a[144:145], v241 offset:35872\n"
"ds_read_b64_tr_b16 a[146:147], v241 offset:36128\n"
"v_mfma_f32_16x16x32_bf16 a[28:31], a[156:159], v[204:207], a[28:31]\n"
"s_add_u32 m0, s52, 31616\n"
"ds_read_b64_tr_b16 a[148:149], v241 offset:38944\n"
"ds_read_b64_tr_b16 a[150:151], v241 offset:39200\n"
"v_mfma_f32_16x16x32_bf16 a[172:175], a[156:159], v[208:211], a[172:175]\n"
"buffer_load_dwordx4 v242, s[40:43], s70 offen offset:128 sc0 nt lds\n"
"v_mfma_f32_16x16x32_bf16 a[172:175], a[152:155], v[196:199], a[172:175]\n"
"v_mul_f32 v162, s55, v162\n"
"v_mul_f32 v163, s55, v163\n"
"v_mfma_f32_16x16x32_bf16 a[220:223], a[152:155], v[200:203], a[220:223]\n"
"v_exp_f32 v160, v160\n"
"v_mfma_f32_16x16x32_bf16 a[220:223], a[156:159], v[212:215], a[220:223]\n"
"s_waitcnt lgkmcnt(4)\n"
"v_exp_f32 v161, v161\n"
"v_mfma_f32_16x16x32_bf16 a[32:35], a[160:163], v[192:195], a[32:35]\n"
"ds_read_b64_tr_b16 a[152:153], v241 offset:36352\n"
"ds_read_b64_tr_b16 a[154:155], v241 offset:36608\n"
"v_mfma_f32_16x16x32_bf16 a[32:35], a[164:167], v[204:207], a[32:35]\n"
"s_add_u32 m0, s52, 32512\n"
"ds_read_b64_tr_b16 a[156:157], v241 offset:39424\n"
"ds_read_b64_tr_b16 a[158:159], v241 offset:39680\n"
"v_mfma_f32_16x16x32_bf16 a[176:179], a[164:167], v[208:211], a[176:179]\n"
"buffer_load_dwordx4 v242, s[40:43], s70 offen offset:256 sc0 nt lds\n"
"v_mfma_f32_16x16x32_bf16 a[176:179], a[160:163], v[196:199], a[176:179]\n"
"v_exp_f32 v162, v162\n"
"v_mfma_f32_16x16x32_bf16 a[224:227], a[160:163], v[200:203], a[224:227]\n"
"v_exp_f32 v163, v163\n"
"v_mfma_f32_16x16x32_bf16 a[224:227], a[164:167], v[212:215], a[224:227]\n"
"s_waitcnt lgkmcnt(4)\n"
"v_mul_f32 v164, s55, v164\n"
"v_mul_f32 v165, s55, v165\n"
"v_mfma_f32_16x16x32_bf16 a[36:39], a[144:147], v[192:195], a[36:39]\n"
"ds_read_b64_tr_b16 a[160:161], v241 offset:36384\n"
"ds_read_b64_tr_b16 a[162:163], v241 offset:36640\n"
"v_mfma_f32_16x16x32_bf16 a[36:39], a[148:151], v[204:207], a[36:39]\n"
"ds_read_b64_tr_b16 a[164:165], v241 offset:39456\n"
"ds_read_b64_tr_b16 a[166:167], v241 offset:39712\n"
"v_mfma_f32_16x16x32_bf16 a[180:183], a[148:151], v[208:211], a[180:183]\n"
"v_mul_f32 v166, s55, v166\n"
"v_mul_f32 v167, s55, v167\n"
"v_mfma_f32_16x16x32_bf16 a[180:183], a[144:147], v[196:199], a[180:183]\n"
"v_exp_f32 v164, v164\n"
"v_mfma_f32_16x16x32_bf16 a[228:231], a[144:147], v[200:203], a[228:231]\n"
"v_exp_f32 v165, v165\n"
"v_mfma_f32_16x16x32_bf16 a[228:231], a[148:151], v[212:215], a[228:231]\n"
"s_waitcnt lgkmcnt(4)\n"
"v_exp_f32 v166, v166\n"
"v_mfma_f32_16x16x32_bf16 a[40:43], a[152:155], v[192:195], a[40:43]\n"
"ds_read_b128 a[144:147], v240 offset:15360\n"
"ds_read_b128 a[148:151], v240 offset:15872\n"
"v_mfma_f32_16x16x32_bf16 a[40:43], a[156:159], v[204:207], a[40:43]\n"
"v_exp_f32 v167, v167\n"
"v_mfma_f32_16x16x32_bf16 a[184:187], a[156:159], v[208:211], a[184:187]\n"
"v_mul_f32 v104, v234, v104\n"
"v_mul_f32 v105, v234, v105\n"
"v_mfma_f32_16x16x32_bf16 a[184:187], a[152:155], v[196:199], a[184:187]\n"
"v_mul_f32 v106, v234, v106\n"
"v_mul_f32 v107, v234, v107\n"
"v_mfma_f32_16x16x32_bf16 a[232:235], a[152:155], v[200:203], a[232:235]\n"
"v_mul_f32 v108, v234, v108\n"
"v_mul_f32 v109, v234, v109\n"
"v_mfma_f32_16x16x32_bf16 a[232:235], a[156:159], v[212:215], a[232:235]\n"
"s_waitcnt lgkmcnt(2)\n"
"v_mul_f32 v110, v234, v110\n"
"v_mul_f32 v111, v234, v111\n"
"v_mfma_f32_16x16x32_bf16 a[44:47], a[160:163], v[192:195], a[44:47]\n"
"ds_read_b128 a[152:155], v240 offset:16384\n"
"ds_read_b128 a[156:159], v240 offset:16896\n"
"v_mfma_f32_16x16x32_bf16 a[44:47], a[164:167], v[204:207], a[44:47]\n"
"v_cvt_pk_bf16_f32 v192, v144, v145\n"
"v_cvt_pk_bf16_f32 v193, v146, v147\n"
"v_mfma_f32_16x16x32_bf16 a[188:191], a[164:167], v[208:211], a[188:191]\n"
"v_cvt_pk_bf16_f32 v194, v156, v157\n"
"v_cvt_pk_bf16_f32 v195, v158, v159\n"
"v_mfma_f32_16x16x32_bf16 a[188:191], a[160:163], v[196:199], a[188:191]\n"
"v_cvt_pk_bf16_f32 v196, v148, v149\n"
"v_cvt_pk_bf16_f32 v197, v150, v151\n"
"v_mfma_f32_16x16x32_bf16 a[236:239], a[160:163], v[200:203], a[236:239]\n"
"v_cvt_pk_bf16_f32 v200, v152, v153\n"
"v_cvt_pk_bf16_f32 v201, v154, v155\n"
"v_mfma_f32_16x16x32_bf16 a[236:239], a[164:167], v[212:215], a[236:239]\n"
"s_waitcnt lgkmcnt(2)\n"
"v_cvt_pk_bf16_f32 v198, v160, v161\n"
"v_cvt_pk_bf16_f32 v199, v162, v163\n"
"v_mfma_f32_16x16x32_bf16 v[144:147], a[144:147], a[72:75], 0\n"
"ds_read_b128 a[160:163], v240 offset:17408\n"
"ds_read_b128 a[164:167], v240 offset:17920\n"
"v_mfma_f32_16x16x32_bf16 v[144:147], a[148:151], a[76:79], v[144:147]\n"
"v_cvt_pk_bf16_f32 v104, v104, v105\n"
"v_cvt_pk_bf16_f32 v105, v106, v107\n"
"v_mfma_f32_16x16x32_bf16 v[148:151], a[148:151], a[100:103], 0\n"
"v_cvt_pk_bf16_f32 v106, v108, v109\n"
"v_cvt_pk_bf16_f32 v107, v110, v111\n"
"v_mfma_f32_16x16x32_bf16 v[148:151], a[144:147], a[96:99], v[148:151]\n"
"v_cvt_pk_bf16_f32 v202, v164, v165\n"
"v_cvt_pk_bf16_f32 v203, v166, v167\n"
"v_mfma_f32_16x16x32_bf16 v[152:155], a[144:147], a[120:123], 0\n"
"buffer_store_dwordx4 v[96:99], v244, s[48:51], s62 offen offset:0\n"
"v_mfma_f32_16x16x32_bf16 v[152:155], a[148:151], a[124:127], v[152:155]\n"
"s_waitcnt lgkmcnt(2)\n"
"v_permlane16_swap_b32 v104, v106\n"
"v_mfma_f32_16x16x32_bf16 v[144:147], a[152:155], a[80:83], v[144:147]\n"
"ds_read_b128 a[144:147], v240 offset:15616\n"
"ds_read_b128 a[148:151], v240 offset:16128\n"
"v_mfma_f32_16x16x32_bf16 v[144:147], a[156:159], a[84:87], v[144:147]\n"
"v_permlane16_swap_b32 v105, v107\n"
"v_mfma_f32_16x16x32_bf16 v[148:151], a[156:159], a[108:111], v[148:151]\n"
"v_mul_f32 v168, s55, v168\n"
"v_mul_f32 v169, s55, v169\n"
"v_mfma_f32_16x16x32_bf16 v[148:151], a[152:155], a[104:107], v[148:151]\n"
"v_mul_f32 v170, s55, v170\n"
"v_mul_f32 v171, s55, v171\n"
"v_mfma_f32_16x16x32_bf16 v[152:155], a[152:155], a[128:131], v[152:155]\n"
"v_exp_f32 v168, v168\n"
"v_mfma_f32_16x16x32_bf16 v[152:155], a[156:159], a[132:135], v[152:155]\n"
"s_waitcnt lgkmcnt(2)\n"
"v_exp_f32 v169, v169\n"
"v_mfma_f32_16x16x32_bf16 v[144:147], a[160:163], a[88:91], v[144:147]\n"
"ds_read_b128 a[152:155], v240 offset:16640\n"
"ds_read_b128 a[156:159], v240 offset:17152\n"
"v_mfma_f32_16x16x32_bf16 v[144:147], a[164:167], a[92:95], v[144:147]\n"
"v_exp_f32 v170, v170\n"
"v_mfma_f32_16x16x32_bf16 v[148:151], a[164:167], a[116:119], v[148:151]\n"
"v_exp_f32 v171, v171\n"
"v_mfma_f32_16x16x32_bf16 v[148:151], a[160:163], a[112:115], v[148:151]\n"
"v_mul_f32 v172, s55, v172\n"
"v_mul_f32 v173, s55, v173\n"
"v_mfma_f32_16x16x32_bf16 v[152:155], a[160:163], a[136:139], v[152:155]\n"
"v_mul_f32 v174, s55, v174\n"
"v_mul_f32 v175, s55, v175\n"
"v_mfma_f32_16x16x32_bf16 v[152:155], a[164:167], a[140:143], v[152:155]\n"
"s_waitcnt lgkmcnt(2)\n"
"v_cvt_pk_bf16_f32 v204, v168, v169\n"
"v_cvt_pk_bf16_f32 v205, v170, v171\n"
"v_mfma_f32_16x16x32_bf16 v[156:159], a[144:147], a[72:75], 0\n"
"ds_read_b128 a[160:163], v240 offset:17664\n"
"ds_read_b128 a[164:167], v240 offset:18176\n"
"v_mfma_f32_16x16x32_bf16 v[156:159], a[148:151], a[76:79], v[156:159]\n"
"v_exp_f32 v172, v172\n"
"v_mfma_f32_16x16x32_bf16 v[160:163], a[148:151], a[100:103], 0\n"
"v_exp_f32 v173, v173\n"
"v_mfma_f32_16x16x32_bf16 v[160:163], a[144:147], a[96:99], v[160:163]\n"
"v_exp_f32 v174, v174\n"
"v_mfma_f32_16x16x32_bf16 v[164:167], a[144:147], a[120:123], 0\n"
"buffer_store_dwordx4 v[104:107], v244, s[48:51], s62 offen offset:64\n"
"v_mfma_f32_16x16x32_bf16 v[164:167], a[148:151], a[124:127], v[164:167]\n"
"s_waitcnt lgkmcnt(2)\n"
"v_exp_f32 v175, v175\n"
"v_mfma_f32_16x16x32_bf16 v[156:159], a[152:155], a[80:83], v[156:159]\n"
"ds_read_b128 a[144:147], v240 offset:18432\n"
"ds_read_b128 a[148:151], v240 offset:18944\n"
"v_mfma_f32_16x16x32_bf16 v[156:159], a[156:159], a[84:87], v[156:159]\n"
"v_mul_f32 v176, s55, v176\n"
"v_mul_f32 v177, s55, v177\n"
"v_mfma_f32_16x16x32_bf16 v[160:163], a[156:159], a[108:111], v[160:163]\n"
"v_mul_f32 v178, s55, v178\n"
"v_mul_f32 v179, s55, v179\n"
"v_mfma_f32_16x16x32_bf16 v[160:163], a[152:155], a[104:107], v[160:163]\n"
"v_cvt_pk_bf16_f32 v208, v172, v173\n"
"v_cvt_pk_bf16_f32 v209, v174, v175\n"
"v_mfma_f32_16x16x32_bf16 v[164:167], a[152:155], a[128:131], v[164:167]\n"
"v_exp_f32 v176, v176\n"
"v_mfma_f32_16x16x32_bf16 v[164:167], a[156:159], a[132:135], v[164:167]\n"
"s_waitcnt lgkmcnt(2)\n"
"v_exp_f32 v177, v177\n"
"v_mfma_f32_16x16x32_bf16 v[156:159], a[160:163], a[88:91], v[156:159]\n"
"ds_read_b128 a[152:155], v240 offset:19456\n"
"ds_read_b128 a[156:159], v240 offset:19968\n"
"v_mfma_f32_16x16x32_bf16 v[156:159], a[164:167], a[92:95], v[156:159]\n"
"v_exp_f32 v178, v178\n"
"v_mfma_f32_16x16x32_bf16 v[160:163], a[164:167], a[116:119], v[160:163]\n"
"v_exp_f32 v179, v179\n"
"v_mfma_f32_16x16x32_bf16 v[160:163], a[160:163], a[112:115], v[160:163]\n"
"v_mul_f32 v112, v234, v112\n"
"v_mul_f32 v113, v234, v113\n"
"v_mfma_f32_16x16x32_bf16 v[164:167], a[160:163], a[136:139], v[164:167]\n"
"v_mul_f32 v114, v234, v114\n"
"v_mul_f32 v115, v234, v115\n"
"v_mfma_f32_16x16x32_bf16 v[164:167], a[164:167], a[140:143], v[164:167]\n"
"s_waitcnt lgkmcnt(2)\n"
"v_mul_f32 v116, v234, v116\n"
"v_mul_f32 v117, v234, v117\n"
"v_mfma_f32_16x16x32_bf16 v[168:171], a[144:147], a[72:75], 0\n"
"ds_read_b128 a[160:163], v240 offset:20480\n"
"ds_read_b128 a[164:167], v240 offset:20992\n"
"v_mfma_f32_16x16x32_bf16 v[168:171], a[148:151], a[76:79], v[168:171]\n"
"v_mul_f32 v118, v234, v118\n"
"v_mul_f32 v119, v234, v119\n"
"v_cvt_pk_bf16_f32 v112, v112, v113\n"
"v_cvt_pk_bf16_f32 v113, v114, v115\n"
"v_cvt_pk_bf16_f32 v114, v116, v117\n"
"v_cvt_pk_bf16_f32 v115, v118, v119\n"
"v_mfma_f32_16x16x32_bf16 v[172:175], a[148:151], a[100:103], 0\n"
"v_cvt_pk_bf16_f32 v212, v176, v177\n"
"v_cvt_pk_bf16_f32 v213, v178, v179\n"
"v_mfma_f32_16x16x32_bf16 v[172:175], a[144:147], a[96:99], v[172:175]\n"
"v_permlane16_swap_b32 v112, v114\n"
"v_permlane16_swap_b32 v113, v115\n"
"v_mfma_f32_16x16x32_bf16 v[176:179], a[144:147], a[120:123], 0\n"
"buffer_store_dwordx4 v[112:115], v244, s[48:51], s62 offen offset:128\n"
"v_mfma_f32_16x16x32_bf16 v[176:179], a[148:151], a[124:127], v[176:179]\n"
"s_waitcnt lgkmcnt(2)\n"
"v_mul_f32 v180, s55, v180\n"
"v_mul_f32 v181, s55, v181\n"
"v_mfma_f32_16x16x32_bf16 v[168:171], a[152:155], a[80:83], v[168:171]\n"
"ds_read_b128 a[144:147], v240 offset:18688\n"
"ds_read_b128 a[148:151], v240 offset:19200\n"
"v_mfma_f32_16x16x32_bf16 v[168:171], a[156:159], a[84:87], v[168:171]\n"
"v_mul_f32 v182, s55, v182\n"
"v_mul_f32 v183, s55, v183\n"
"v_mfma_f32_16x16x32_bf16 v[172:175], a[156:159], a[108:111], v[172:175]\n"
"v_exp_f32 v180, v180\n"
"v_mfma_f32_16x16x32_bf16 v[172:175], a[152:155], a[104:107], v[172:175]\n"
"v_exp_f32 v181, v181\n"
"v_mfma_f32_16x16x32_bf16 v[176:179], a[152:155], a[128:131], v[176:179]\n"
"v_exp_f32 v182, v182\n"
"v_mfma_f32_16x16x32_bf16 v[176:179], a[156:159], a[132:135], v[176:179]\n"
"s_waitcnt lgkmcnt(2)\n"
"v_exp_f32 v183, v183\n"
"v_mfma_f32_16x16x32_bf16 v[168:171], a[160:163], a[88:91], v[168:171]\n"
"ds_read_b128 a[152:155], v240 offset:19712\n"
"ds_read_b128 a[156:159], v240 offset:20224\n"
"v_mfma_f32_16x16x32_bf16 v[168:171], a[164:167], a[92:95], v[168:171]\n"
"v_mul_f32 v184, s55, v184\n"
"v_mul_f32 v185, s55, v185\n"
"v_mfma_f32_16x16x32_bf16 v[172:175], a[164:167], a[116:119], v[172:175]\n"
"v_mul_f32 v186, s55, v186\n"
"v_mul_f32 v187, s55, v187\n"
"v_mfma_f32_16x16x32_bf16 v[172:175], a[160:163], a[112:115], v[172:175]\n"
"v_cvt_pk_bf16_f32 v206, v180, v181\n"
"v_cvt_pk_bf16_f32 v207, v182, v183\n"
"v_mfma_f32_16x16x32_bf16 v[176:179], a[160:163], a[136:139], v[176:179]\n"
"v_exp_f32 v184, v184\n"
"v_mfma_f32_16x16x32_bf16 v[176:179], a[164:167], a[140:143], v[176:179]\n"
"s_waitcnt lgkmcnt(2)\n"
"v_exp_f32 v185, v185\n"
"v_exp_f32 v186, v186\n"
"v_exp_f32 v187, v187\n"
"v_mul_f32 v188, s55, v188\n"
"v_mul_f32 v189, s55, v189\n"
"v_mul_f32 v190, s55, v190\n"
"v_mul_f32 v191, s55, v191\n"
"v_mfma_f32_16x16x32_bf16 v[180:183], a[144:147], a[72:75], 0\n"
"ds_read_b128 a[160:163], v240 offset:20736\n"
"ds_read_b128 a[164:167], v240 offset:21248\n"
"v_mfma_f32_16x16x32_bf16 v[180:183], a[148:151], a[76:79], v[180:183]\n"
"v_cvt_pk_bf16_f32 v210, v184, v185\n"
"v_cvt_pk_bf16_f32 v211, v186, v187\n"
"v_exp_f32 v188, v188\n"
"v_exp_f32 v189, v189\n"
"v_exp_f32 v190, v190\n"
"v_exp_f32 v191, v191\n"
"v_mul_f32 v120, v234, v120\n"
"v_mul_f32 v121, v234, v121\n"
"v_mul_f32 v122, v234, v122\n"
"v_mul_f32 v123, v234, v123\n"
"v_mul_f32 v124, v234, v124\n"
"v_mul_f32 v125, v234, v125\n"
"v_mul_f32 v126, v234, v126\n"
"v_mul_f32 v127, v234, v127\n"
"v_cvt_pk_bf16_f32 v120, v120, v121\n"
"v_cvt_pk_bf16_f32 v121, v122, v123\n"
"v_cvt_pk_bf16_f32 v122, v124, v125\n"
"v_cvt_pk_bf16_f32 v123, v126, v127\n"
"v_mfma_f32_16x16x32_bf16 v[184:187], a[148:151], a[100:103], 0\n"
"v_cvt_pk_bf16_f32 v214, v188, v189\n"
"v_cvt_pk_bf16_f32 v215, v190, v191\n"
"v_mfma_f32_16x16x32_bf16 v[184:187], a[144:147], a[96:99], v[184:187]\n"
"s_waitcnt vmcnt(19)\n"
"v_permlane16_swap_b32 v120, v122\n"
"v_permlane16_swap_b32 v121, v123\n"
"v_mfma_f32_16x16x32_bf16 v[188:191], a[144:147], a[120:123], 0\n"
"s_barrier\n"
"buffer_store_dwordx4 v[120:123], v244, s[48:51], s62 offen offset:192\n"
"v_mfma_f32_16x16x32_bf16 v[188:191], a[148:151], a[124:127], v[188:191]\n"
"s_waitcnt lgkmcnt(2)\n"
"v_mul_f32 v144, s55, v144\n"
"v_mul_f32 v145, s55, v145\n"
"v_mfma_f32_16x16x32_bf16 v[180:183], a[152:155], a[80:83], v[180:183]\n"
"s_mov_b32 s59, 344064\n"
"ds_read_b64_tr_b16 a[144:145], v241 offset:21504\n"
"ds_read_b64_tr_b16 a[146:147], v241 offset:21760\n"
"v_mfma_f32_16x16x32_bf16 v[180:183], a[156:159], a[84:87], v[180:183]\n"
"s_add_u32 m0, s52, 33792\n"
"ds_read_b64_tr_b16 a[148:149], v241 offset:24576\n"
"ds_read_b64_tr_b16 a[150:151], v241 offset:24832\n"
"v_mfma_f32_16x16x32_bf16 v[184:187], a[156:159], a[108:111], v[184:187]\n"
"buffer_load_dwordx4 v242, s[40:43], s59 offen offset:0 sc0 nt lds\n"
"v_mfma_f32_16x16x32_bf16 v[184:187], a[152:155], a[104:107], v[184:187]\n"
"v_mul_f32 v146, s55, v146\n"
"v_mul_f32 v147, s55, v147\n"
"v_mfma_f32_16x16x32_bf16 v[188:191], a[152:155], a[128:131], v[188:191]\n"
"v_exp_f32 v144, v144\n"
"v_mfma_f32_16x16x32_bf16 v[188:191], a[156:159], a[132:135], v[188:191]\n"
"s_waitcnt lgkmcnt(4)\n"
"v_exp_f32 v145, v145\n"
"v_mfma_f32_16x16x32_bf16 v[180:183], a[160:163], a[88:91], v[180:183]\n"
"ds_read_b64_tr_b16 a[152:153], v241 offset:21536\n"
"ds_read_b64_tr_b16 a[154:155], v241 offset:21792\n"
"v_mfma_f32_16x16x32_bf16 v[180:183], a[164:167], a[92:95], v[180:183]\n"
"s_add_u32 m0, s52, 34688\n"
"ds_read_b64_tr_b16 a[156:157], v241 offset:24608\n"
"ds_read_b64_tr_b16 a[158:159], v241 offset:24864\n"
"v_mfma_f32_16x16x32_bf16 v[184:187], a[164:167], a[116:119], v[184:187]\n"
"buffer_load_dwordx4 v242, s[40:43], s59 offen offset:128 sc0 nt lds\n"
"v_mfma_f32_16x16x32_bf16 v[184:187], a[160:163], a[112:115], v[184:187]\n"
"v_exp_f32 v146, v146\n"
"v_mfma_f32_16x16x32_bf16 v[188:191], a[160:163], a[136:139], v[188:191]\n"
"v_exp_f32 v147, v147\n"
"v_mfma_f32_16x16x32_bf16 v[188:191], a[164:167], a[140:143], v[188:191]\n"
"v_mul_f32 v148, s55, v148\n"
"v_mul_f32 v149, s55, v149\n"
"v_mfma_f32_16x16x32_bf16 a[240:243], a[252:255], v[192:195], a[240:243]\n"
"v_mul_f32 v150, s55, v150\n"
"v_mul_f32 v151, s55, v151\n"
"v_mfma_f32_16x16x32_bf16 a[240:243], a[252:255], v[204:207], a[240:243]\n"
"s_add_u32 m0, s52, 35584\n"
"v_exp_f32 v148, v148\n"
"v_mfma_f32_16x16x32_bf16 a[244:247], a[252:255], v[196:199], a[244:247]\n"
"buffer_load_dwordx4 v242, s[40:43], s59 offen offset:256 sc0 nt lds\n"
"v_mfma_f32_16x16x32_bf16 a[244:247], a[252:255], v[208:211], a[244:247]\n"
"v_exp_f32 v149, v149\n"
"v_mfma_f32_16x16x32_bf16 a[248:251], a[252:255], v[200:203], a[248:251]\n"
"v_exp_f32 v150, v150\n"
"v_mfma_f32_16x16x32_bf16 a[248:251], a[252:255], v[212:215], a[248:251]\n"
"s_waitcnt lgkmcnt(4)\n"
"v_exp_f32 v151, v151\n"
"v_mfma_f32_16x16x32_bf16 a[0:3], a[144:147], v[192:195], a[0:3]\n"
"s_mov_b32 s70, 356352\n"
"ds_read_b64_tr_b16 a[160:161], v241 offset:22016\n"
"ds_read_b64_tr_b16 a[162:163], v241 offset:22272\n"
"v_mfma_f32_16x16x32_bf16 a[0:3], a[148:151], v[204:207], a[0:3]\n"
"s_add_u32 m0, s52, 36864\n"
"ds_read_b64_tr_b16 a[164:165], v241 offset:25088\n"
"ds_read_b64_tr_b16 a[166:167], v241 offset:25344\n"
"v_mfma_f32_16x16x32_bf16 a[48:51], a[148:151], v[208:211], a[48:51]\n"
"buffer_load_dwordx4 v242, s[40:43], s70 offen offset:0 sc0 nt lds\n"
"v_mfma_f32_16x16x32_bf16 a[48:51], a[144:147], v[196:199], a[48:51]\n"
"v_mul_f32 v152, s55, v152\n"
"v_mul_f32 v153, s55, v153\n"
"v_mfma_f32_16x16x32_bf16 a[192:195], a[144:147], v[200:203], a[192:195]\n"
"v_mul_f32 v154, s55, v154\n"
"v_mul_f32 v155, s55, v155\n"
"v_mfma_f32_16x16x32_bf16 a[192:195], a[148:151], v[212:215], a[192:195]\n"
"s_waitcnt lgkmcnt(4)\n"
"v_exp_f32 v152, v152\n"
"v_mfma_f32_16x16x32_bf16 a[4:7], a[152:155], v[192:195], a[4:7]\n"
"ds_read_b64_tr_b16 a[144:145], v241 offset:22048\n"
"ds_read_b64_tr_b16 a[146:147], v241 offset:22304\n"
"v_mfma_f32_16x16x32_bf16 a[4:7], a[156:159], v[204:207], a[4:7]\n"
"s_add_u32 m0, s52, 37760\n"
"ds_read_b64_tr_b16 a[148:149], v241 offset:25120\n"
"ds_read_b64_tr_b16 a[150:151], v241 offset:25376\n"
"v_mfma_f32_16x16x32_bf16 a[52:55], a[156:159], v[208:211], a[52:55]\n"
"buffer_load_dwordx4 v242, s[40:43], s70 offen offset:128 sc0 nt lds\n"
"v_mfma_f32_16x16x32_bf16 a[52:55], a[152:155], v[196:199], a[52:55]\n"
"v_exp_f32 v153, v153\n"
"v_mfma_f32_16x16x32_bf16 a[196:199], a[152:155], v[200:203], a[196:199]\n"
"v_exp_f32 v154, v154\n"
"v_mfma_f32_16x16x32_bf16 a[196:199], a[156:159], v[212:215], a[196:199]\n"
"s_waitcnt lgkmcnt(4)\n"
"v_exp_f32 v155, v155\n"
"v_mfma_f32_16x16x32_bf16 a[8:11], a[160:163], v[192:195], a[8:11]\n"
"ds_read_b64_tr_b16 a[152:153], v241 offset:22528\n"
"ds_read_b64_tr_b16 a[154:155], v241 offset:22784\n"
"v_mfma_f32_16x16x32_bf16 a[8:11], a[164:167], v[204:207], a[8:11]\n"
"s_add_u32 m0, s52, 38656\n"
"ds_read_b64_tr_b16 a[156:157], v241 offset:25600\n"
"ds_read_b64_tr_b16 a[158:159], v241 offset:25856\n"
"v_mfma_f32_16x16x32_bf16 a[56:59], a[164:167], v[208:211], a[56:59]\n"
"buffer_load_dwordx4 v242, s[40:43], s70 offen offset:256 sc0 nt lds\n"
"v_mfma_f32_16x16x32_bf16 a[56:59], a[160:163], v[196:199], a[56:59]\n"
"v_mul_f32 v128, v234, v128\n"
"v_mul_f32 v129, v234, v129\n"
"v_mfma_f32_16x16x32_bf16 a[200:203], a[160:163], v[200:203], a[200:203]\n"
"v_mul_f32 v130, v234, v130\n"
"v_mul_f32 v131, v234, v131\n"
"v_mfma_f32_16x16x32_bf16 a[200:203], a[164:167], v[212:215], a[200:203]\n"
"s_waitcnt lgkmcnt(4)\n"
"v_mul_f32 v132, v234, v132\n"
"v_mul_f32 v133, v234, v133\n"
"v_mfma_f32_16x16x32_bf16 a[12:15], a[144:147], v[192:195], a[12:15]\n"
"ds_read_b64_tr_b16 a[160:161], v241 offset:22560\n"
"ds_read_b64_tr_b16 a[162:163], v241 offset:22816\n"
"v_mfma_f32_16x16x32_bf16 a[12:15], a[148:151], v[204:207], a[12:15]\n"
"ds_read_b64_tr_b16 a[164:165], v241 offset:25632\n"
"ds_read_b64_tr_b16 a[166:167], v241 offset:25888\n"
"v_mfma_f32_16x16x32_bf16 a[60:63], a[148:151], v[208:211], a[60:63]\n"
"v_mul_f32 v134, v234, v134\n"
"v_mul_f32 v135, v234, v135\n"
"v_mfma_f32_16x16x32_bf16 a[60:63], a[144:147], v[196:199], a[60:63]\n"
"v_cvt_pk_bf16_f32 v128, v128, v129\n"
"v_cvt_pk_bf16_f32 v129, v130, v131\n"
"v_mfma_f32_16x16x32_bf16 a[204:207], a[144:147], v[200:203], a[204:207]\n"
"v_cvt_pk_bf16_f32 v130, v132, v133\n"
"v_cvt_pk_bf16_f32 v131, v134, v135\n"
"v_mfma_f32_16x16x32_bf16 a[204:207], a[148:151], v[212:215], a[204:207]\n"
"s_waitcnt lgkmcnt(4)\n"
"v_mul_f32 v156, s55, v156\n"
"v_mul_f32 v157, s55, v157\n"
"v_mfma_f32_16x16x32_bf16 a[16:19], a[152:155], v[192:195], a[16:19]\n"
"ds_read_b64_tr_b16 a[144:145], v241 offset:23040\n"
"ds_read_b64_tr_b16 a[146:147], v241 offset:23296\n"
"v_mfma_f32_16x16x32_bf16 a[16:19], a[156:159], v[204:207], a[16:19]\n"
"ds_read_b64_tr_b16 a[148:149], v241 offset:26112\n"
"ds_read_b64_tr_b16 a[150:151], v241 offset:26368\n"
"v_mfma_f32_16x16x32_bf16 a[64:67], a[156:159], v[208:211], a[64:67]\n"
"v_permlane16_swap_b32 v128, v130\n"
"v_mfma_f32_16x16x32_bf16 a[64:67], a[152:155], v[196:199], a[64:67]\n"
"v_permlane16_swap_b32 v129, v131\n"
"v_mfma_f32_16x16x32_bf16 a[208:211], a[152:155], v[200:203], a[208:211]\n"
"v_mul_f32 v158, s55, v158\n"
"v_mul_f32 v159, s55, v159\n"
"v_mfma_f32_16x16x32_bf16 a[208:211], a[156:159], v[212:215], a[208:211]\n"
"s_waitcnt lgkmcnt(4)\n"
"v_exp_f32 v156, v156\n"
"v_mfma_f32_16x16x32_bf16 a[20:23], a[160:163], v[192:195], a[20:23]\n"
"ds_read_b64_tr_b16 a[152:153], v241 offset:23072\n"
"ds_read_b64_tr_b16 a[154:155], v241 offset:23328\n"
"v_mfma_f32_16x16x32_bf16 a[20:23], a[164:167], v[204:207], a[20:23]\n"
"ds_read_b64_tr_b16 a[156:157], v241 offset:26144\n"
"ds_read_b64_tr_b16 a[158:159], v241 offset:26400\n"
"v_mfma_f32_16x16x32_bf16 a[68:71], a[164:167], v[208:211], a[68:71]\n"
"v_exp_f32 v157, v157\n"
"v_mfma_f32_16x16x32_bf16 a[68:71], a[160:163], v[196:199], a[68:71]\n"
"v_exp_f32 v158, v158\n"
"v_mfma_f32_16x16x32_bf16 a[212:215], a[160:163], v[200:203], a[212:215]\n"
"v_exp_f32 v159, v159\n"
"v_mfma_f32_16x16x32_bf16 a[212:215], a[164:167], v[212:215], a[212:215]\n"
"s_waitcnt lgkmcnt(4)\n"
"v_mul_f32 v160, s55, v160\n"
"v_mul_f32 v161, s55, v161\n"
"v_mfma_f32_16x16x32_bf16 a[24:27], a[144:147], v[192:195], a[24:27]\n"
"ds_read_b64_tr_b16 a[160:161], v241 offset:23552\n"
"ds_read_b64_tr_b16 a[162:163], v241 offset:23808\n"
"v_mfma_f32_16x16x32_bf16 a[24:27], a[148:151], v[204:207], a[24:27]\n"
"ds_read_b64_tr_b16 a[164:165], v241 offset:26624\n"
"ds_read_b64_tr_b16 a[166:167], v241 offset:26880\n"
"v_mfma_f32_16x16x32_bf16 a[168:171], a[148:151], v[208:211], a[168:171]\n"
"v_mul_f32 v162, s55, v162\n"
"v_mul_f32 v163, s55, v163\n"
"v_mfma_f32_16x16x32_bf16 a[168:171], a[144:147], v[196:199], a[168:171]\n"
"v_exp_f32 v160, v160\n"
"v_mfma_f32_16x16x32_bf16 a[216:219], a[144:147], v[200:203], a[216:219]\n"
"v_exp_f32 v161, v161\n"
"v_mfma_f32_16x16x32_bf16 a[216:219], a[148:151], v[212:215], a[216:219]\n"
"s_waitcnt lgkmcnt(4)\n"
"v_exp_f32 v162, v162\n"
"v_mfma_f32_16x16x32_bf16 a[28:31], a[152:155], v[192:195], a[28:31]\n"
"ds_read_b64_tr_b16 a[144:145], v241 offset:23584\n"
"ds_read_b64_tr_b16 a[146:147], v241 offset:23840\n"
"v_mfma_f32_16x16x32_bf16 a[28:31], a[156:159], v[204:207], a[28:31]\n"
"ds_read_b64_tr_b16 a[148:149], v241 offset:26656\n"
"ds_read_b64_tr_b16 a[150:151], v241 offset:26912\n"
"v_mfma_f32_16x16x32_bf16 a[172:175], a[156:159], v[208:211], a[172:175]\n"
"v_exp_f32 v163, v163\n"
"v_mfma_f32_16x16x32_bf16 a[172:175], a[152:155], v[196:199], a[172:175]\n"
"v_mul_f32 v164, s55, v164\n"
"v_mul_f32 v165, s55, v165\n"
"v_mfma_f32_16x16x32_bf16 a[220:223], a[152:155], v[200:203], a[220:223]\n"
"v_mul_f32 v166, s55, v166\n"
"v_mul_f32 v167, s55, v167\n"
"v_mfma_f32_16x16x32_bf16 a[220:223], a[156:159], v[212:215], a[220:223]\n"
"s_waitcnt lgkmcnt(4)\n"
"v_exp_f32 v164, v164\n"
"v_mfma_f32_16x16x32_bf16 a[32:35], a[160:163], v[192:195], a[32:35]\n"
"ds_read_b64_tr_b16 a[152:153], v241 offset:24064\n"
"ds_read_b64_tr_b16 a[154:155], v241 offset:24320\n"
"v_mfma_f32_16x16x32_bf16 a[32:35], a[164:167], v[204:207], a[32:35]\n"
"ds_read_b64_tr_b16 a[156:157], v241 offset:27136\n"
"ds_read_b64_tr_b16 a[158:159], v241 offset:27392\n"
"v_mfma_f32_16x16x32_bf16 a[176:179], a[164:167], v[208:211], a[176:179]\n"
"v_exp_f32 v165, v165\n"
"v_mfma_f32_16x16x32_bf16 a[176:179], a[160:163], v[196:199], a[176:179]\n"
"v_exp_f32 v166, v166\n"
"v_mfma_f32_16x16x32_bf16 a[224:227], a[160:163], v[200:203], a[224:227]\n"
"v_exp_f32 v167, v167\n"
"v_mfma_f32_16x16x32_bf16 a[224:227], a[164:167], v[212:215], a[224:227]\n"
"s_waitcnt lgkmcnt(4)\n"
"v_mul_f32 v136, v234, v136\n"
"v_mul_f32 v137, v234, v137\n"
"v_mfma_f32_16x16x32_bf16 a[36:39], a[144:147], v[192:195], a[36:39]\n"
"ds_read_b64_tr_b16 a[160:161], v241 offset:24096\n"
"ds_read_b64_tr_b16 a[162:163], v241 offset:24352\n"
"v_mfma_f32_16x16x32_bf16 a[36:39], a[148:151], v[204:207], a[36:39]\n"
"ds_read_b64_tr_b16 a[164:165], v241 offset:27168\n"
"ds_read_b64_tr_b16 a[166:167], v241 offset:27424\n"
"v_mfma_f32_16x16x32_bf16 a[180:183], a[148:151], v[208:211], a[180:183]\n"
"v_mul_f32 v138, v234, v138\n"
"v_mul_f32 v139, v234, v139\n"
"v_mfma_f32_16x16x32_bf16 a[180:183], a[144:147], v[196:199], a[180:183]\n"
"v_mul_f32 v140, v234, v140\n"
"v_mul_f32 v141, v234, v141\n"
"v_mfma_f32_16x16x32_bf16 a[228:231], a[144:147], v[200:203], a[228:231]\n"
"v_mul_f32 v142, v234, v142\n"
"v_mul_f32 v143, v234, v143\n"
"v_mfma_f32_16x16x32_bf16 a[228:231], a[148:151], v[212:215], a[228:231]\n"
"s_waitcnt lgkmcnt(4)\n"
"v_cvt_pk_bf16_f32 v136, v136, v137\n"
"v_cvt_pk_bf16_f32 v137, v138, v139\n"
"v_mfma_f32_16x16x32_bf16 a[40:43], a[152:155], v[192:195], a[40:43]\n"
"ds_read_b128 a[144:147], v240 offset:0\n"
"ds_read_b128 a[148:151], v240 offset:512\n"
"v_mfma_f32_16x16x32_bf16 a[40:43], a[156:159], v[204:207], a[40:43]\n"
"v_cvt_pk_bf16_f32 v138, v140, v141\n"
"v_cvt_pk_bf16_f32 v139, v142, v143\n"
"v_mfma_f32_16x16x32_bf16 a[184:187], a[156:159], v[208:211], a[184:187]\n"
"v_mul_f32 v168, s55, v168\n"
"v_mul_f32 v169, s55, v169\n"
"v_mfma_f32_16x16x32_bf16 a[184:187], a[152:155], v[196:199], a[184:187]\n"
"v_permlane16_swap_b32 v136, v138\n"
"v_mfma_f32_16x16x32_bf16 a[232:235], a[152:155], v[200:203], a[232:235]\n"
"v_permlane16_swap_b32 v137, v139\n"
"v_mfma_f32_16x16x32_bf16 a[232:235], a[156:159], v[212:215], a[232:235]\n"
"s_waitcnt lgkmcnt(2)\n"
"v_mul_f32 v170, s55, v170\n"
"v_mul_f32 v171, s55, v171\n"
"v_mfma_f32_16x16x32_bf16 a[44:47], a[160:163], v[192:195], a[44:47]\n"
"ds_read_b128 a[152:155], v240 offset:1024\n"
"ds_read_b128 a[156:159], v240 offset:1536\n"
"v_mfma_f32_16x16x32_bf16 a[44:47], a[164:167], v[204:207], a[44:47]\n"
"v_cvt_pk_bf16_f32 v192, v144, v145\n"
"v_cvt_pk_bf16_f32 v193, v146, v147\n"
"v_mfma_f32_16x16x32_bf16 a[188:191], a[164:167], v[208:211], a[188:191]\n"
"v_cvt_pk_bf16_f32 v194, v156, v157\n"
"v_cvt_pk_bf16_f32 v195, v158, v159\n"
"v_mfma_f32_16x16x32_bf16 a[188:191], a[160:163], v[196:199], a[188:191]\n"
"v_cvt_pk_bf16_f32 v196, v148, v149\n"
"v_cvt_pk_bf16_f32 v197, v150, v151\n"
"v_mfma_f32_16x16x32_bf16 a[236:239], a[160:163], v[200:203], a[236:239]\n"
"v_cvt_pk_bf16_f32 v200, v152, v153\n"
"v_cvt_pk_bf16_f32 v201, v154, v155\n"
"v_mfma_f32_16x16x32_bf16 a[236:239], a[164:167], v[212:215], a[236:239]\n"
"s_waitcnt lgkmcnt(2)\n"
"v_cvt_pk_bf16_f32 v198, v160, v161\n"
"v_cvt_pk_bf16_f32 v199, v162, v163\n"
"v_mfma_f32_16x16x32_bf16 v[144:147], a[144:147], a[72:75], 0\n"
"ds_read_b128 a[160:163], v240 offset:2048\n"
"ds_read_b128 a[164:167], v240 offset:2560\n"
"v_mfma_f32_16x16x32_bf16 v[144:147], a[148:151], a[76:79], v[144:147]\n"
"v_cvt_pk_bf16_f32 v202, v164, v165\n"
"v_cvt_pk_bf16_f32 v203, v166, v167\n"
"v_mfma_f32_16x16x32_bf16 v[148:151], a[148:151], a[100:103], 0\n"
"v_exp_f32 v168, v168\n"
"v_mfma_f32_16x16x32_bf16 v[148:151], a[144:147], a[96:99], v[148:151]\n"
"v_exp_f32 v169, v169\n"
"v_mfma_f32_16x16x32_bf16 v[152:155], a[144:147], a[120:123], 0\n"
"buffer_store_dwordx4 v[128:131], v244, s[48:51], s62 offen offset:256\n"
"v_mfma_f32_16x16x32_bf16 v[152:155], a[148:151], a[124:127], v[152:155]\n"
"s_waitcnt lgkmcnt(2)\n"
"v_exp_f32 v170, v170\n"
"v_mfma_f32_16x16x32_bf16 v[144:147], a[152:155], a[80:83], v[144:147]\n"
"ds_read_b128 a[144:147], v240 offset:256\n"
"ds_read_b128 a[148:151], v240 offset:768\n"
"v_mfma_f32_16x16x32_bf16 v[144:147], a[156:159], a[84:87], v[144:147]\n"
"v_exp_f32 v171, v171\n"
"v_mfma_f32_16x16x32_bf16 v[148:151], a[156:159], a[108:111], v[148:151]\n"
"v_mul_f32 v172, s55, v172\n"
"v_mul_f32 v173, s55, v173\n"
"v_mfma_f32_16x16x32_bf16 v[148:151], a[152:155], a[104:107], v[148:151]\n"
"v_mul_f32 v174, s55, v174\n"
"v_mul_f32 v175, s55, v175\n"
"v_mfma_f32_16x16x32_bf16 v[152:155], a[152:155], a[128:131], v[152:155]\n"
"v_cvt_pk_bf16_f32 v204, v168, v169\n"
"v_cvt_pk_bf16_f32 v205, v170, v171\n"
"v_mfma_f32_16x16x32_bf16 v[152:155], a[156:159], a[132:135], v[152:155]\n"
"s_waitcnt lgkmcnt(2)\n"
"v_exp_f32 v172, v172\n"
"v_mfma_f32_16x16x32_bf16 v[144:147], a[160:163], a[88:91], v[144:147]\n"
"ds_read_b128 a[152:155], v240 offset:1280\n"
"ds_read_b128 a[156:159], v240 offset:1792\n"
"v_mfma_f32_16x16x32_bf16 v[144:147], a[164:167], a[92:95], v[144:147]\n"
"v_exp_f32 v173, v173\n"
"v_mfma_f32_16x16x32_bf16 v[148:151], a[164:167], a[116:119], v[148:151]\n"
"v_exp_f32 v174, v174\n"
"v_mfma_f32_16x16x32_bf16 v[148:151], a[160:163], a[112:115], v[148:151]\n"
"v_exp_f32 v175, v175\n"
"v_mfma_f32_16x16x32_bf16 v[152:155], a[160:163], a[136:139], v[152:155]\n"
"v_mul_f32 v176, s55, v176\n"
"v_mul_f32 v177, s55, v177\n"
"v_mfma_f32_16x16x32_bf16 v[152:155], a[164:167], a[140:143], v[152:155]\n"
"s_waitcnt lgkmcnt(2)\n"
"v_mul_f32 v178, s55, v178\n"
"v_mul_f32 v179, s55, v179\n"
"v_mfma_f32_16x16x32_bf16 v[156:159], a[144:147], a[72:75], 0\n"
"ds_read_b128 a[160:163], v240 offset:2304\n"
"ds_read_b128 a[164:167], v240 offset:2816\n"
"v_mfma_f32_16x16x32_bf16 v[156:159], a[148:151], a[76:79], v[156:159]\n"
"v_cvt_pk_bf16_f32 v208, v172, v173\n"
"v_cvt_pk_bf16_f32 v209, v174, v175\n"
"v_mfma_f32_16x16x32_bf16 v[160:163], a[148:151], a[100:103], 0\n"
"v_exp_f32 v176, v176\n"
"v_mfma_f32_16x16x32_bf16 v[160:163], a[144:147], a[96:99], v[160:163]\n"
"v_exp_f32 v177, v177\n"
"v_mfma_f32_16x16x32_bf16 v[164:167], a[144:147], a[120:123], 0\n"
"buffer_store_dwordx4 v[136:139], v244, s[48:51], s62 offen offset:320\n"
"v_mfma_f32_16x16x32_bf16 v[164:167], a[148:151], a[124:127], v[164:167]\n"
"s_waitcnt lgkmcnt(2)\n"
"v_exp_f32 v178, v178\n"
"v_mfma_f32_16x16x32_bf16 v[156:159], a[152:155], a[80:83], v[156:159]\n"
"ds_read_b128 a[144:147], v240 offset:3072\n"
"ds_read_b128 a[148:151], v240 offset:3584\n"
"v_mfma_f32_16x16x32_bf16 v[156:159], a[156:159], a[84:87], v[156:159]\n"
"v_exp_f32 v179, v179\n"
"v_mfma_f32_16x16x32_bf16 v[160:163], a[156:159], a[108:111], v[160:163]\n"
"v_cvt_pk_bf16_f32 v212, v176, v177\n"
"v_cvt_pk_bf16_f32 v213, v178, v179\n"
"v_mfma_f32_16x16x32_bf16 v[160:163], a[152:155], a[104:107], v[160:163]\n"
"v_mul_f32 v180, s55, v180\n"
"v_mul_f32 v181, s55, v181\n"
"v_mfma_f32_16x16x32_bf16 v[164:167], a[152:155], a[128:131], v[164:167]\n"
"v_mul_f32 v182, s55, v182\n"
"v_mul_f32 v183, s55, v183\n"
"v_mfma_f32_16x16x32_bf16 v[164:167], a[156:159], a[132:135], v[164:167]\n"
"s_waitcnt lgkmcnt(2)\n"
"v_exp_f32 v180, v180\n"
"v_mfma_f32_16x16x32_bf16 v[156:159], a[160:163], a[88:91], v[156:159]\n"
"ds_read_b128 a[152:155], v240 offset:4096\n"
"ds_read_b128 a[156:159], v240 offset:4608\n"
"v_mfma_f32_16x16x32_bf16 v[156:159], a[164:167], a[92:95], v[156:159]\n"
"v_exp_f32 v181, v181\n"
"v_mfma_f32_16x16x32_bf16 v[160:163], a[164:167], a[116:119], v[160:163]\n"
"v_exp_f32 v182, v182\n"
"v_mfma_f32_16x16x32_bf16 v[160:163], a[160:163], a[112:115], v[160:163]\n"
"v_exp_f32 v183, v183\n"
"v_mfma_f32_16x16x32_bf16 v[164:167], a[160:163], a[136:139], v[164:167]\n"
"v_mul_f32 v184, s55, v184\n"
"v_mul_f32 v185, s55, v185\n"
"v_mfma_f32_16x16x32_bf16 v[164:167], a[164:167], a[140:143], v[164:167]\n"
"s_waitcnt lgkmcnt(2)\n"
"v_mul_f32 v186, s55, v186\n"
"v_mul_f32 v187, s55, v187\n"
"v_mfma_f32_16x16x32_bf16 v[168:171], a[144:147], a[72:75], 0\n"
"ds_read_b128 a[160:163], v240 offset:5120\n"
"ds_read_b128 a[164:167], v240 offset:5632\n"
"v_mfma_f32_16x16x32_bf16 v[168:171], a[148:151], a[76:79], v[168:171]\n"
"v_cvt_pk_bf16_f32 v206, v180, v181\n"
"v_cvt_pk_bf16_f32 v207, v182, v183\n"
"v_mfma_f32_16x16x32_bf16 v[172:175], a[148:151], a[100:103], 0\n"
"v_exp_f32 v184, v184\n"
"v_mfma_f32_16x16x32_bf16 v[172:175], a[144:147], a[96:99], v[172:175]\n"
"v_exp_f32 v185, v185\n"
"v_mfma_f32_16x16x32_bf16 v[176:179], a[144:147], a[120:123], 0\n"
"v_exp_f32 v186, v186\n"
"v_mfma_f32_16x16x32_bf16 v[176:179], a[148:151], a[124:127], v[176:179]\n"
"s_waitcnt lgkmcnt(2)\n"
"v_exp_f32 v187, v187\n"
"v_mfma_f32_16x16x32_bf16 v[168:171], a[152:155], a[80:83], v[168:171]\n"
"ds_read_b128 a[144:147], v240 offset:3328\n"
"ds_read_b128 a[148:151], v240 offset:3840\n"
"v_mfma_f32_16x16x32_bf16 v[168:171], a[156:159], a[84:87], v[168:171]\n"
"v_mul_f32 v188, s55, v188\n"
"v_mul_f32 v189, s55, v189\n"
"v_mfma_f32_16x16x32_bf16 v[172:175], a[156:159], a[108:111], v[172:175]\n"
"v_mul_f32 v190, s55, v190\n"
"v_mul_f32 v191, s55, v191\n"
"v_mfma_f32_16x16x32_bf16 v[172:175], a[152:155], a[104:107], v[172:175]\n"
"v_cvt_pk_bf16_f32 v210, v184, v185\n"
"v_cvt_pk_bf16_f32 v211, v186, v187\n"
"v_mfma_f32_16x16x32_bf16 v[176:179], a[152:155], a[128:131], v[176:179]\n"
"v_exp_f32 v188, v188\n"
"v_mfma_f32_16x16x32_bf16 v[176:179], a[156:159], a[132:135], v[176:179]\n"
"s_waitcnt lgkmcnt(2)\n"
"v_exp_f32 v189, v189\n"
"v_mfma_f32_16x16x32_bf16 v[168:171], a[160:163], a[88:91], v[168:171]\n"
"ds_read_b128 a[152:155], v240 offset:4352\n"
"ds_read_b128 a[156:159], v240 offset:4864\n"
"v_mfma_f32_16x16x32_bf16 v[168:171], a[164:167], a[92:95], v[168:171]\n"
"v_exp_f32 v190, v190\n"
"v_mfma_f32_16x16x32_bf16 v[172:175], a[164:167], a[116:119], v[172:175]\n"
"v_exp_f32 v191, v191\n"
"v_mfma_f32_16x16x32_bf16 v[172:175], a[160:163], a[112:115], v[172:175]\n"
"v_cvt_pk_bf16_f32 v214, v188, v189\n"
"v_cvt_pk_bf16_f32 v215, v190, v191\n"
"v_mfma_f32_16x16x32_bf16 v[176:179], a[160:163], a[136:139], v[176:179]\n"
"v_mul_f32 v144, s55, v144\n"
"v_mul_f32 v145, s55, v145\n"
"v_mfma_f32_16x16x32_bf16 v[176:179], a[164:167], a[140:143], v[176:179]\n"
"s_waitcnt lgkmcnt(2)\n"
"v_mul_f32 v146, s55, v146\n"
"v_mul_f32 v147, s55, v147\n"
"v_mfma_f32_16x16x32_bf16 v[180:183], a[144:147], a[72:75], 0\n"
"ds_read_b128 a[160:163], v240 offset:5376\n"
"ds_read_b128 a[164:167], v240 offset:5888\n"
"v_mfma_f32_16x16x32_bf16 v[180:183], a[148:151], a[76:79], v[180:183]\n"
"v_exp_f32 v144, v144\n"
"v_mfma_f32_16x16x32_bf16 v[184:187], a[148:151], a[100:103], 0\n"
"v_exp_f32 v145, v145\n"
"v_mfma_f32_16x16x32_bf16 v[184:187], a[144:147], a[96:99], v[184:187]\n"
"s_waitcnt vmcnt(12)\n"
"v_exp_f32 v146, v146\n"
"v_mfma_f32_16x16x32_bf16 v[188:191], a[144:147], a[120:123], 0\n"
"s_barrier\n"
"v_exp_f32 v147, v147\n"
"v_mfma_f32_16x16x32_bf16 v[188:191], a[148:151], a[124:127], v[188:191]\n"
"s_waitcnt lgkmcnt(2)\n"
"v_mul_f32 v148, s55, v148\n"
"v_mul_f32 v149, s55, v149\n"
"v_mfma_f32_16x16x32_bf16 v[180:183], a[152:155], a[80:83], v[180:183]\n"
"s_mov_b32 s59, 368640\n"
"ds_read_b64_tr_b16 a[144:145], v241 offset:27648\n"
"ds_read_b64_tr_b16 a[146:147], v241 offset:27904\n"
"v_mfma_f32_16x16x32_bf16 v[180:183], a[156:159], a[84:87], v[180:183]\n"
"s_add_u32 m0, s52, 21504\n"
"ds_read_b64_tr_b16 a[148:149], v241 offset:30720\n"
"ds_read_b64_tr_b16 a[150:151], v241 offset:30976\n"
"v_mfma_f32_16x16x32_bf16 v[184:187], a[156:159], a[108:111], v[184:187]\n"
"buffer_load_dwordx4 v242, s[40:43], s59 offen offset:0 sc0 nt lds\n"
"v_mfma_f32_16x16x32_bf16 v[184:187], a[152:155], a[104:107], v[184:187]\n"
"v_mul_f32 v150, s55, v150\n"
"v_mul_f32 v151, s55, v151\n"
"v_mfma_f32_16x16x32_bf16 v[188:191], a[152:155], a[128:131], v[188:191]\n"
"v_exp_f32 v148, v148\n"
"v_mfma_f32_16x16x32_bf16 v[188:191], a[156:159], a[132:135], v[188:191]\n"
"s_waitcnt lgkmcnt(4)\n"
"v_exp_f32 v149, v149\n"
"v_mfma_f32_16x16x32_bf16 v[180:183], a[160:163], a[88:91], v[180:183]\n"
"ds_read_b64_tr_b16 a[152:153], v241 offset:27680\n"
"ds_read_b64_tr_b16 a[154:155], v241 offset:27936\n"
"v_mfma_f32_16x16x32_bf16 v[180:183], a[164:167], a[92:95], v[180:183]\n"
"s_add_u32 m0, s52, 22400\n"
"ds_read_b64_tr_b16 a[156:157], v241 offset:30752\n"
"ds_read_b64_tr_b16 a[158:159], v241 offset:31008\n"
"v_mfma_f32_16x16x32_bf16 v[184:187], a[164:167], a[116:119], v[184:187]\n"
"buffer_load_dwordx4 v242, s[40:43], s59 offen offset:128 sc0 nt lds\n"
"v_mfma_f32_16x16x32_bf16 v[184:187], a[160:163], a[112:115], v[184:187]\n"
"v_exp_f32 v150, v150\n"
"v_mfma_f32_16x16x32_bf16 v[188:191], a[160:163], a[136:139], v[188:191]\n"
"v_exp_f32 v151, v151\n"
"v_mfma_f32_16x16x32_bf16 v[188:191], a[164:167], a[140:143], v[188:191]\n"
"v_mul_f32 v152, s55, v152\n"
"v_mul_f32 v153, s55, v153\n"
"v_mfma_f32_16x16x32_bf16 a[240:243], a[252:255], v[192:195], a[240:243]\n"
"v_mul_f32 v154, s55, v154\n"
"v_mul_f32 v155, s55, v155\n"
"v_mfma_f32_16x16x32_bf16 a[240:243], a[252:255], v[204:207], a[240:243]\n"
"s_add_u32 m0, s52, 23296\n"
"v_exp_f32 v152, v152\n"
"v_mfma_f32_16x16x32_bf16 a[244:247], a[252:255], v[196:199], a[244:247]\n"
"buffer_load_dwordx4 v242, s[40:43], s59 offen offset:256 sc0 nt lds\n"
"v_mfma_f32_16x16x32_bf16 a[244:247], a[252:255], v[208:211], a[244:247]\n"
"v_exp_f32 v153, v153\n"
"v_mfma_f32_16x16x32_bf16 a[248:251], a[252:255], v[200:203], a[248:251]\n"
"v_exp_f32 v154, v154\n"
"v_mfma_f32_16x16x32_bf16 a[248:251], a[252:255], v[212:215], a[248:251]\n"
"s_waitcnt lgkmcnt(4)\n"
"v_exp_f32 v155, v155\n"
"v_mfma_f32_16x16x32_bf16 a[0:3], a[144:147], v[192:195], a[0:3]\n"
"s_mov_b32 s70, 380928\n"
"ds_read_b64_tr_b16 a[160:161], v241 offset:28160\n"
"ds_read_b64_tr_b16 a[162:163], v241 offset:28416\n"
"v_mfma_f32_16x16x32_bf16 a[0:3], a[148:151], v[204:207], a[0:3]\n"
"s_add_u32 m0, s52, 24576\n"
"ds_read_b64_tr_b16 a[164:165], v241 offset:31232\n"
"ds_read_b64_tr_b16 a[166:167], v241 offset:31488\n"
"v_mfma_f32_16x16x32_bf16 a[48:51], a[148:151], v[208:211], a[48:51]\n"
"buffer_load_dwordx4 v242, s[40:43], s70 offen offset:0 sc0 nt lds\n"
"v_mfma_f32_16x16x32_bf16 a[48:51], a[144:147], v[196:199], a[48:51]\n"
"v_mul_f32 v156, s55, v156\n"
"v_mul_f32 v157, s55, v157\n"
"v_mfma_f32_16x16x32_bf16 a[192:195], a[144:147], v[200:203], a[192:195]\n"
"v_mul_f32 v158, s55, v158\n"
"v_mul_f32 v159, s55, v159\n"
"v_mfma_f32_16x16x32_bf16 a[192:195], a[148:151], v[212:215], a[192:195]\n"
"s_waitcnt lgkmcnt(4)\n"
"v_exp_f32 v156, v156\n"
"v_mfma_f32_16x16x32_bf16 a[4:7], a[152:155], v[192:195], a[4:7]\n"
"ds_read_b64_tr_b16 a[144:145], v241 offset:28192\n"
"ds_read_b64_tr_b16 a[146:147], v241 offset:28448\n"
"v_mfma_f32_16x16x32_bf16 a[4:7], a[156:159], v[204:207], a[4:7]\n"
"s_add_u32 m0, s52, 25472\n"
"ds_read_b64_tr_b16 a[148:149], v241 offset:31264\n"
"ds_read_b64_tr_b16 a[150:151], v241 offset:31520\n"
"v_mfma_f32_16x16x32_bf16 a[52:55], a[156:159], v[208:211], a[52:55]\n"
"buffer_load_dwordx4 v242, s[40:43], s70 offen offset:128 sc0 nt lds\n"
"v_mfma_f32_16x16x32_bf16 a[52:55], a[152:155], v[196:199], a[52:55]\n"
"v_exp_f32 v157, v157\n"
"v_mfma_f32_16x16x32_bf16 a[196:199], a[152:155], v[200:203], a[196:199]\n"
"v_exp_f32 v158, v158\n"
"v_mfma_f32_16x16x32_bf16 a[196:199], a[156:159], v[212:215], a[196:199]\n"
"s_waitcnt lgkmcnt(4)\n"
"v_exp_f32 v159, v159\n"
"v_mfma_f32_16x16x32_bf16 a[8:11], a[160:163], v[192:195], a[8:11]\n"
"ds_read_b64_tr_b16 a[152:153], v241 offset:28672\n"
"ds_read_b64_tr_b16 a[154:155], v241 offset:28928\n"
"v_mfma_f32_16x16x32_bf16 a[8:11], a[164:167], v[204:207], a[8:11]\n"
"s_add_u32 m0, s52, 26368\n"
"ds_read_b64_tr_b16 a[156:157], v241 offset:31744\n"
"ds_read_b64_tr_b16 a[158:159], v241 offset:32000\n"
"v_mfma_f32_16x16x32_bf16 a[56:59], a[164:167], v[208:211], a[56:59]\n"
"buffer_load_dwordx4 v242, s[40:43], s70 offen offset:256 sc0 nt lds\n"
"v_mfma_f32_16x16x32_bf16 a[56:59], a[160:163], v[196:199], a[56:59]\n"
"v_mul_f32 v160, s55, v160\n"
"v_mul_f32 v161, s55, v161\n"
"v_mfma_f32_16x16x32_bf16 a[200:203], a[160:163], v[200:203], a[200:203]\n"
"v_mul_f32 v162, s55, v162\n"
"v_mul_f32 v163, s55, v163\n"
"v_mfma_f32_16x16x32_bf16 a[200:203], a[164:167], v[212:215], a[200:203]\n"
"s_waitcnt lgkmcnt(4)\n"
"v_exp_f32 v160, v160\n"
"v_mfma_f32_16x16x32_bf16 a[12:15], a[144:147], v[192:195], a[12:15]\n"
"ds_read_b64_tr_b16 a[160:161], v241 offset:28704\n"
"ds_read_b64_tr_b16 a[162:163], v241 offset:28960\n"
"v_mfma_f32_16x16x32_bf16 a[12:15], a[148:151], v[204:207], a[12:15]\n"
"ds_read_b64_tr_b16 a[164:165], v241 offset:31776\n"
"ds_read_b64_tr_b16 a[166:167], v241 offset:32032\n"
"v_mfma_f32_16x16x32_bf16 a[60:63], a[148:151], v[208:211], a[60:63]\n"
"v_exp_f32 v161, v161\n"
"v_mfma_f32_16x16x32_bf16 a[60:63], a[144:147], v[196:199], a[60:63]\n"
"v_exp_f32 v162, v162\n"
"v_mfma_f32_16x16x32_bf16 a[204:207], a[144:147], v[200:203], a[204:207]\n"
"v_exp_f32 v163, v163\n"
"v_mfma_f32_16x16x32_bf16 a[204:207], a[148:151], v[212:215], a[204:207]\n"
"s_waitcnt lgkmcnt(4)\n"
"v_mul_f32 v164, s55, v164\n"
"v_mul_f32 v165, s55, v165\n"
"v_mfma_f32_16x16x32_bf16 a[16:19], a[152:155], v[192:195], a[16:19]\n"
"ds_read_b64_tr_b16 a[144:145], v241 offset:29184\n"
"ds_read_b64_tr_b16 a[146:147], v241 offset:29440\n"
"v_mfma_f32_16x16x32_bf16 a[16:19], a[156:159], v[204:207], a[16:19]\n"
"ds_read_b64_tr_b16 a[148:149], v241 offset:32256\n"
"ds_read_b64_tr_b16 a[150:151], v241 offset:32512\n"
"v_mfma_f32_16x16x32_bf16 a[64:67], a[156:159], v[208:211], a[64:67]\n"
"v_mul_f32 v166, s55, v166\n"
"v_mul_f32 v167, s55, v167\n"
"v_mfma_f32_16x16x32_bf16 a[64:67], a[152:155], v[196:199], a[64:67]\n"
"v_exp_f32 v164, v164\n"
"v_mfma_f32_16x16x32_bf16 a[208:211], a[152:155], v[200:203], a[208:211]\n"
"v_exp_f32 v165, v165\n"
"v_mfma_f32_16x16x32_bf16 a[208:211], a[156:159], v[212:215], a[208:211]\n"
"s_waitcnt lgkmcnt(4)\n"
"v_exp_f32 v166, v166\n"
"v_mfma_f32_16x16x32_bf16 a[20:23], a[160:163], v[192:195], a[20:23]\n"
"ds_read_b64_tr_b16 a[152:153], v241 offset:29216\n"
"ds_read_b64_tr_b16 a[154:155], v241 offset:29472\n"
"v_mfma_f32_16x16x32_bf16 a[20:23], a[164:167], v[204:207], a[20:23]\n"
"ds_read_b64_tr_b16 a[156:157], v241 offset:32288\n"
"ds_read_b64_tr_b16 a[158:159], v241 offset:32544\n"
"v_mfma_f32_16x16x32_bf16 a[68:71], a[164:167], v[208:211], a[68:71]\n"
"v_exp_f32 v167, v167\n"
"v_mfma_f32_16x16x32_bf16 a[68:71], a[160:163], v[196:199], a[68:71]\n"
"v_mul_f32 v168, s55, v168\n"
"v_mul_f32 v169, s55, v169\n"
"v_mfma_f32_16x16x32_bf16 a[212:215], a[160:163], v[200:203], a[212:215]\n"
"v_mul_f32 v170, s55, v170\n"
"v_mul_f32 v171, s55, v171\n"
"v_mfma_f32_16x16x32_bf16 a[212:215], a[164:167], v[212:215], a[212:215]\n"
"s_waitcnt lgkmcnt(4)\n"
"v_exp_f32 v168, v168\n"
"v_mfma_f32_16x16x32_bf16 a[24:27], a[144:147], v[192:195], a[24:27]\n"
"ds_read_b64_tr_b16 a[160:161], v241 offset:29696\n"
"ds_read_b64_tr_b16 a[162:163], v241 offset:29952\n"
"v_mfma_f32_16x16x32_bf16 a[24:27], a[148:151], v[204:207], a[24:27]\n"
"ds_read_b64_tr_b16 a[164:165], v241 offset:32768\n"
"ds_read_b64_tr_b16 a[166:167], v241 offset:33024\n"
"v_mfma_f32_16x16x32_bf16 a[168:171], a[148:151], v[208:211], a[168:171]\n"
"v_exp_f32 v169, v169\n"
"v_mfma_f32_16x16x32_bf16 a[168:171], a[144:147], v[196:199], a[168:171]\n"
"v_exp_f32 v170, v170\n"
"v_mfma_f32_16x16x32_bf16 a[216:219], a[144:147], v[200:203], a[216:219]\n"
"v_exp_f32 v171, v171\n"
"v_mfma_f32_16x16x32_bf16 a[216:219], a[148:151], v[212:215], a[216:219]\n"
"s_waitcnt lgkmcnt(4)\n"
"v_mul_f32 v172, s55, v172\n"
"v_mul_f32 v173, s55, v173\n"
"v_mfma_f32_16x16x32_bf16 a[28:31], a[152:155], v[192:195], a[28:31]\n"
"ds_read_b64_tr_b16 a[144:145], v241 offset:29728\n"
"ds_read_b64_tr_b16 a[146:147], v241 offset:29984\n"
"v_mfma_f32_16x16x32_bf16 a[28:31], a[156:159], v[204:207], a[28:31]\n"
"ds_read_b64_tr_b16 a[148:149], v241 offset:32800\n"
"ds_read_b64_tr_b16 a[150:151], v241 offset:33056\n"
"v_mfma_f32_16x16x32_bf16 a[172:175], a[156:159], v[208:211], a[172:175]\n"
"v_mul_f32 v174, s55, v174\n"
"v_mul_f32 v175, s55, v175\n"
"v_mfma_f32_16x16x32_bf16 a[172:175], a[152:155], v[196:199], a[172:175]\n"
"v_exp_f32 v172, v172\n"
"v_mfma_f32_16x16x32_bf16 a[220:223], a[152:155], v[200:203], a[220:223]\n"
"v_exp_f32 v173, v173\n"
"v_mfma_f32_16x16x32_bf16 a[220:223], a[156:159], v[212:215], a[220:223]\n"
"s_waitcnt lgkmcnt(4)\n"
"v_exp_f32 v174, v174\n"
"v_mfma_f32_16x16x32_bf16 a[32:35], a[160:163], v[192:195], a[32:35]\n"
"ds_read_b64_tr_b16 a[152:153], v241 offset:30208\n"
"ds_read_b64_tr_b16 a[154:155], v241 offset:30464\n"
"v_mfma_f32_16x16x32_bf16 a[32:35], a[164:167], v[204:207], a[32:35]\n"
"ds_read_b64_tr_b16 a[156:157], v241 offset:33280\n"
"ds_read_b64_tr_b16 a[158:159], v241 offset:33536\n"
"v_mfma_f32_16x16x32_bf16 a[176:179], a[164:167], v[208:211], a[176:179]\n"
"v_exp_f32 v175, v175\n"
"v_mfma_f32_16x16x32_bf16 a[176:179], a[160:163], v[196:199], a[176:179]\n"
"v_mul_f32 v176, s55, v176\n"
"v_mul_f32 v177, s55, v177\n"
"v_mfma_f32_16x16x32_bf16 a[224:227], a[160:163], v[200:203], a[224:227]\n"
"v_mul_f32 v178, s55, v178\n"
"v_mul_f32 v179, s55, v179\n"
"v_mfma_f32_16x16x32_bf16 a[224:227], a[164:167], v[212:215], a[224:227]\n"
"s_waitcnt lgkmcnt(4)\n"
"v_exp_f32 v176, v176\n"
"v_mfma_f32_16x16x32_bf16 a[36:39], a[144:147], v[192:195], a[36:39]\n"
"ds_read_b64_tr_b16 a[160:161], v241 offset:30240\n"
"ds_read_b64_tr_b16 a[162:163], v241 offset:30496\n"
"v_mfma_f32_16x16x32_bf16 a[36:39], a[148:151], v[204:207], a[36:39]\n"
"ds_read_b64_tr_b16 a[164:165], v241 offset:33312\n"
"ds_read_b64_tr_b16 a[166:167], v241 offset:33568\n"
"v_mfma_f32_16x16x32_bf16 a[180:183], a[148:151], v[208:211], a[180:183]\n"
"v_exp_f32 v177, v177\n"
"v_mfma_f32_16x16x32_bf16 a[180:183], a[144:147], v[196:199], a[180:183]\n"
"v_exp_f32 v178, v178\n"
"v_mfma_f32_16x16x32_bf16 a[228:231], a[144:147], v[200:203], a[228:231]\n"
"v_exp_f32 v179, v179\n"
"v_mfma_f32_16x16x32_bf16 a[228:231], a[148:151], v[212:215], a[228:231]\n"
"s_waitcnt lgkmcnt(4)\n"
"v_mul_f32 v180, s55, v180\n"
"v_mul_f32 v181, s55, v181\n"
"v_mfma_f32_16x16x32_bf16 a[40:43], a[152:155], v[192:195], a[40:43]\n"
"ds_read_b128 a[144:147], v240 offset:6144\n"
"ds_read_b128 a[148:151], v240 offset:6656\n"
"v_mfma_f32_16x16x32_bf16 a[40:43], a[156:159], v[204:207], a[40:43]\n"
"v_mul_f32 v182, s55, v182\n"
"v_mul_f32 v183, s55, v183\n"
"v_mfma_f32_16x16x32_bf16 a[184:187], a[156:159], v[208:211], a[184:187]\n"
"v_exp_f32 v180, v180\n"
"v_mfma_f32_16x16x32_bf16 a[184:187], a[152:155], v[196:199], a[184:187]\n"
"v_exp_f32 v181, v181\n"
"v_mfma_f32_16x16x32_bf16 a[232:235], a[152:155], v[200:203], a[232:235]\n"
"v_exp_f32 v182, v182\n"
"v_mfma_f32_16x16x32_bf16 a[232:235], a[156:159], v[212:215], a[232:235]\n"
"s_waitcnt lgkmcnt(2)\n"
"v_exp_f32 v183, v183\n"
"v_mfma_f32_16x16x32_bf16 a[44:47], a[160:163], v[192:195], a[44:47]\n"
"ds_read_b128 a[152:155], v240 offset:7168\n"
"ds_read_b128 a[156:159], v240 offset:7680\n"
"v_mfma_f32_16x16x32_bf16 a[44:47], a[164:167], v[204:207], a[44:47]\n"
"v_cvt_pk_bf16_f32 v192, v144, v145\n"
"v_cvt_pk_bf16_f32 v193, v146, v147\n"
"v_mfma_f32_16x16x32_bf16 a[188:191], a[164:167], v[208:211], a[188:191]\n"
"v_cvt_pk_bf16_f32 v194, v156, v157\n"
"v_cvt_pk_bf16_f32 v195, v158, v159\n"
"v_mfma_f32_16x16x32_bf16 a[188:191], a[160:163], v[196:199], a[188:191]\n"
"v_cvt_pk_bf16_f32 v196, v148, v149\n"
"v_cvt_pk_bf16_f32 v197, v150, v151\n"
"v_mfma_f32_16x16x32_bf16 a[236:239], a[160:163], v[200:203], a[236:239]\n"
"v_cvt_pk_bf16_f32 v200, v152, v153\n"
"v_cvt_pk_bf16_f32 v201, v154, v155\n"
"v_mfma_f32_16x16x32_bf16 a[236:239], a[164:167], v[212:215], a[236:239]\n"
"s_waitcnt lgkmcnt(2)\n"
"v_cvt_pk_bf16_f32 v198, v160, v161\n"
"v_cvt_pk_bf16_f32 v199, v162, v163\n"
"v_mfma_f32_16x16x32_bf16 v[144:147], a[144:147], a[72:75], 0\n"
"ds_read_b128 a[160:163], v240 offset:8192\n"
"ds_read_b128 a[164:167], v240 offset:8704\n"
"v_mfma_f32_16x16x32_bf16 v[144:147], a[148:151], a[76:79], v[144:147]\n"
"v_cvt_pk_bf16_f32 v202, v164, v165\n"
"v_cvt_pk_bf16_f32 v203, v166, v167\n"
"v_mfma_f32_16x16x32_bf16 v[148:151], a[148:151], a[100:103], 0\n"
"v_cvt_pk_bf16_f32 v204, v168, v169\n"
"v_cvt_pk_bf16_f32 v205, v170, v171\n"
"v_mfma_f32_16x16x32_bf16 v[148:151], a[144:147], a[96:99], v[148:151]\n"
"v_cvt_pk_bf16_f32 v208, v172, v173\n"
"v_cvt_pk_bf16_f32 v209, v174, v175\n"
"v_mfma_f32_16x16x32_bf16 v[152:155], a[144:147], a[120:123], 0\n"
"v_cvt_pk_bf16_f32 v212, v176, v177\n"
"v_cvt_pk_bf16_f32 v213, v178, v179\n"
"v_mfma_f32_16x16x32_bf16 v[152:155], a[148:151], a[124:127], v[152:155]\n"
"s_waitcnt lgkmcnt(2)\n"
"v_mul_f32 v184, s55, v184\n"
"v_mul_f32 v185, s55, v185\n"
"v_mfma_f32_16x16x32_bf16 v[144:147], a[152:155], a[80:83], v[144:147]\n"
"ds_read_b128 a[144:147], v240 offset:6400\n"
"ds_read_b128 a[148:151], v240 offset:6912\n"
"v_mfma_f32_16x16x32_bf16 v[144:147], a[156:159], a[84:87], v[144:147]\n"
"v_mul_f32 v186, s55, v186\n"
"v_mul_f32 v187, s55, v187\n"
"v_mfma_f32_16x16x32_bf16 v[148:151], a[156:159], a[108:111], v[148:151]\n"
"v_cvt_pk_bf16_f32 v206, v180, v181\n"
"v_cvt_pk_bf16_f32 v207, v182, v183\n"
"v_mfma_f32_16x16x32_bf16 v[148:151], a[152:155], a[104:107], v[148:151]\n"
"v_exp_f32 v184, v184\n"
"v_mfma_f32_16x16x32_bf16 v[152:155], a[152:155], a[128:131], v[152:155]\n"
"v_exp_f32 v185, v185\n"
"v_mfma_f32_16x16x32_bf16 v[152:155], a[156:159], a[132:135], v[152:155]\n"
"s_waitcnt lgkmcnt(2)\n"
"v_exp_f32 v186, v186\n"
"v_mfma_f32_16x16x32_bf16 v[144:147], a[160:163], a[88:91], v[144:147]\n"
"ds_read_b128 a[152:155], v240 offset:7424\n"
"ds_read_b128 a[156:159], v240 offset:7936\n"
"v_mfma_f32_16x16x32_bf16 v[144:147], a[164:167], a[92:95], v[144:147]\n"
"v_exp_f32 v187, v187\n"
"v_mfma_f32_16x16x32_bf16 v[148:151], a[164:167], a[116:119], v[148:151]\n"
"v_mul_f32 v188, s55, v188\n"
"v_mul_f32 v189, s55, v189\n"
"v_mfma_f32_16x16x32_bf16 v[148:151], a[160:163], a[112:115], v[148:151]\n"
"v_mul_f32 v190, s55, v190\n"
"v_mul_f32 v191, s55, v191\n"
"v_mfma_f32_16x16x32_bf16 v[152:155], a[160:163], a[136:139], v[152:155]\n"
"v_cvt_pk_bf16_f32 v210, v184, v185\n"
"v_cvt_pk_bf16_f32 v211, v186, v187\n"
"v_mfma_f32_16x16x32_bf16 v[152:155], a[164:167], a[140:143], v[152:155]\n"
"s_waitcnt lgkmcnt(2)\n"
"v_exp_f32 v188, v188\n"
"v_mfma_f32_16x16x32_bf16 v[156:159], a[144:147], a[72:75], 0\n"
"ds_read_b128 a[160:163], v240 offset:8448\n"
"ds_read_b128 a[164:167], v240 offset:8960\n"
"v_mfma_f32_16x16x32_bf16 v[156:159], a[148:151], a[76:79], v[156:159]\n"
"v_exp_f32 v189, v189\n"
"v_mfma_f32_16x16x32_bf16 v[160:163], a[148:151], a[100:103], 0\n"
"v_exp_f32 v190, v190\n"
"v_mfma_f32_16x16x32_bf16 v[160:163], a[144:147], a[96:99], v[160:163]\n"
"v_exp_f32 v191, v191\n"
"v_mfma_f32_16x16x32_bf16 v[164:167], a[144:147], a[120:123], 0\n"
"v_cvt_pk_bf16_f32 v214, v188, v189\n"
"v_cvt_pk_bf16_f32 v215, v190, v191\n"
"v_mfma_f32_16x16x32_bf16 v[164:167], a[148:151], a[124:127], v[164:167]\n"
"s_waitcnt lgkmcnt(2)\n"
"v_mul_f32 v144, s55, v144\n"
"v_mul_f32 v145, s55, v145\n"
"v_mfma_f32_16x16x32_bf16 v[156:159], a[152:155], a[80:83], v[156:159]\n"
"ds_read_b128 a[144:147], v240 offset:9216\n"
"ds_read_b128 a[148:151], v240 offset:9728\n"
"v_mfma_f32_16x16x32_bf16 v[156:159], a[156:159], a[84:87], v[156:159]\n"
"v_mul_f32 v146, s55, v146\n"
"v_mul_f32 v147, s55, v147\n"
"v_mfma_f32_16x16x32_bf16 v[160:163], a[156:159], a[108:111], v[160:163]\n"
"v_exp_f32 v144, v144\n"
"v_mfma_f32_16x16x32_bf16 v[160:163], a[152:155], a[104:107], v[160:163]\n"
"v_exp_f32 v145, v145\n"
"v_mfma_f32_16x16x32_bf16 v[164:167], a[152:155], a[128:131], v[164:167]\n"
"v_exp_f32 v146, v146\n"
"v_mfma_f32_16x16x32_bf16 v[164:167], a[156:159], a[132:135], v[164:167]\n"
"s_waitcnt lgkmcnt(2)\n"
"v_exp_f32 v147, v147\n"
"v_mfma_f32_16x16x32_bf16 v[156:159], a[160:163], a[88:91], v[156:159]\n"
"ds_read_b128 a[152:155], v240 offset:10240\n"
"ds_read_b128 a[156:159], v240 offset:10752\n"
"v_mfma_f32_16x16x32_bf16 v[156:159], a[164:167], a[92:95], v[156:159]\n"
"v_mul_f32 v148, s55, v148\n"
"v_mul_f32 v149, s55, v149\n"
"v_mfma_f32_16x16x32_bf16 v[160:163], a[164:167], a[116:119], v[160:163]\n"
"v_mul_f32 v150, s55, v150\n"
"v_mul_f32 v151, s55, v151\n"
"v_mfma_f32_16x16x32_bf16 v[160:163], a[160:163], a[112:115], v[160:163]\n"
"v_cvt_pk_bf16_f32 v144, v144, v145\n"
"v_cvt_pk_bf16_f32 v145, v146, v147\n"
"v_mfma_f32_16x16x32_bf16 v[164:167], a[160:163], a[136:139], v[164:167]\n"
"v_exp_f32 v148, v148\n"
"v_mfma_f32_16x16x32_bf16 v[164:167], a[164:167], a[140:143], v[164:167]\n"
"s_waitcnt lgkmcnt(2)\n"
"v_exp_f32 v149, v149\n"
"v_mfma_f32_16x16x32_bf16 v[168:171], a[144:147], a[72:75], 0\n"
"ds_read_b128 a[160:163], v240 offset:11264\n"
"ds_read_b128 a[164:167], v240 offset:11776\n"
"v_mfma_f32_16x16x32_bf16 v[168:171], a[148:151], a[76:79], v[168:171]\n"
"v_exp_f32 v150, v150\n"
"v_mfma_f32_16x16x32_bf16 v[172:175], a[148:151], a[100:103], 0\n"
"v_exp_f32 v151, v151\n"
"v_mfma_f32_16x16x32_bf16 v[172:175], a[144:147], a[96:99], v[172:175]\n"
"v_mul_f32 v156, s55, v156\n"
"v_mul_f32 v157, s55, v157\n"
"v_mfma_f32_16x16x32_bf16 v[176:179], a[144:147], a[120:123], 0\n"
"v_mul_f32 v158, s55, v158\n"
"v_mul_f32 v159, s55, v159\n"
"v_mfma_f32_16x16x32_bf16 v[176:179], a[148:151], a[124:127], v[176:179]\n"
"s_waitcnt lgkmcnt(2)\n"
"v_exp_f32 v156, v156\n"
"v_mfma_f32_16x16x32_bf16 v[168:171], a[152:155], a[80:83], v[168:171]\n"
"ds_read_b128 a[144:147], v240 offset:9472\n"
"ds_read_b128 a[148:151], v240 offset:9984\n"
"v_mfma_f32_16x16x32_bf16 v[168:171], a[156:159], a[84:87], v[168:171]\n"
"v_exp_f32 v157, v157\n"
"v_mfma_f32_16x16x32_bf16 v[172:175], a[156:159], a[108:111], v[172:175]\n"
"v_exp_f32 v158, v158\n"
"v_mfma_f32_16x16x32_bf16 v[172:175], a[152:155], a[104:107], v[172:175]\n"
"v_exp_f32 v159, v159\n"
"v_mfma_f32_16x16x32_bf16 v[176:179], a[152:155], a[128:131], v[176:179]\n"
"v_mul_f32 v160, s55, v160\n"
"v_mul_f32 v161, s55, v161\n"
"v_mfma_f32_16x16x32_bf16 v[176:179], a[156:159], a[132:135], v[176:179]\n"
"s_waitcnt lgkmcnt(2)\n"
"v_mul_f32 v162, s55, v162\n"
"v_mul_f32 v163, s55, v163\n"
"v_mfma_f32_16x16x32_bf16 v[168:171], a[160:163], a[88:91], v[168:171]\n"
"ds_read_b128 a[152:155], v240 offset:10496\n"
"ds_read_b128 a[156:159], v240 offset:11008\n"
"v_mfma_f32_16x16x32_bf16 v[168:171], a[164:167], a[92:95], v[168:171]\n"
"v_mul_f32 v152, s55, v152\n"
"v_mul_f32 v153, s55, v153\n"
"v_mfma_f32_16x16x32_bf16 v[172:175], a[164:167], a[116:119], v[172:175]\n"
"v_mul_f32 v154, s55, v154\n"
"v_mul_f32 v155, s55, v155\n"
"v_mfma_f32_16x16x32_bf16 v[172:175], a[160:163], a[112:115], v[172:175]\n"
"v_cvt_pk_bf16_f32 v146, v156, v157\n"
"v_cvt_pk_bf16_f32 v147, v158, v159\n"
"v_mfma_f32_16x16x32_bf16 v[176:179], a[160:163], a[136:139], v[176:179]\n"
"v_exp_f32 v160, v160\n"
"v_mfma_f32_16x16x32_bf16 v[176:179], a[164:167], a[140:143], v[176:179]\n"
"s_waitcnt lgkmcnt(2)\n"
"v_exp_f32 v161, v161\n"
"v_mfma_f32_16x16x32_bf16 v[180:183], a[144:147], a[72:75], 0\n"
"ds_read_b128 a[160:163], v240 offset:11520\n"
"ds_read_b128 a[164:167], v240 offset:12032\n"
"v_mfma_f32_16x16x32_bf16 v[180:183], a[148:151], a[76:79], v[180:183]\n"
"v_exp_f32 v162, v162\n"
"v_mfma_f32_16x16x32_bf16 v[184:187], a[148:151], a[100:103], 0\n"
"v_exp_f32 v163, v163\n"
"v_mfma_f32_16x16x32_bf16 v[184:187], a[144:147], a[96:99], v[184:187]\n"
"s_waitcnt vmcnt(8)\n"
"v_mul_f32 v168, s55, v168\n"
"v_mul_f32 v169, s55, v169\n"
"v_mfma_f32_16x16x32_bf16 v[188:191], a[144:147], a[120:123], 0\n"
"s_barrier\n"
"v_mul_f32 v170, s55, v170\n"
"v_mul_f32 v171, s55, v171\n"
"v_mfma_f32_16x16x32_bf16 v[188:191], a[148:151], a[124:127], v[188:191]\n"
"s_waitcnt lgkmcnt(2)\n"
"v_exp_f32 v168, v168\n"
"v_mfma_f32_16x16x32_bf16 v[180:183], a[152:155], a[80:83], v[180:183]\n"
"ds_read_b64_tr_b16 a[144:145], v241 offset:33792\n"
"ds_read_b64_tr_b16 a[146:147], v241 offset:34048\n"
"v_mfma_f32_16x16x32_bf16 v[180:183], a[156:159], a[84:87], v[180:183]\n"
"ds_read_b64_tr_b16 a[148:149], v241 offset:36864\n"
"ds_read_b64_tr_b16 a[150:151], v241 offset:37120\n"
"v_mfma_f32_16x16x32_bf16 v[184:187], a[156:159], a[108:111], v[184:187]\n"
"v_exp_f32 v169, v169\n"
"v_mfma_f32_16x16x32_bf16 v[184:187], a[152:155], a[104:107], v[184:187]\n"
"v_exp_f32 v170, v170\n"
"v_mfma_f32_16x16x32_bf16 v[188:191], a[152:155], a[128:131], v[188:191]\n"
"v_exp_f32 v171, v171\n"
"v_mfma_f32_16x16x32_bf16 v[188:191], a[156:159], a[132:135], v[188:191]\n"
"s_waitcnt lgkmcnt(4)\n"
"v_mul_f32 v172, s55, v172\n"
"v_mul_f32 v173, s55, v173\n"
"v_mfma_f32_16x16x32_bf16 v[180:183], a[160:163], a[88:91], v[180:183]\n"
"ds_read_b64_tr_b16 a[152:153], v241 offset:33824\n"
"ds_read_b64_tr_b16 a[154:155], v241 offset:34080\n"
"v_mfma_f32_16x16x32_bf16 v[180:183], a[164:167], a[92:95], v[180:183]\n"
"ds_read_b64_tr_b16 a[156:157], v241 offset:36896\n"
"ds_read_b64_tr_b16 a[158:159], v241 offset:37152\n"
"v_mfma_f32_16x16x32_bf16 v[184:187], a[164:167], a[116:119], v[184:187]\n"
"v_mul_f32 v174, s55, v174\n"
"v_mul_f32 v175, s55, v175\n"
"v_mfma_f32_16x16x32_bf16 v[184:187], a[160:163], a[112:115], v[184:187]\n"
"v_mul_f32 v164, s55, v164\n"
"v_mul_f32 v165, s55, v165\n"
"v_mfma_f32_16x16x32_bf16 v[188:191], a[160:163], a[136:139], v[188:191]\n"
"v_mul_f32 v166, s55, v166\n"
"v_mul_f32 v167, s55, v167\n"
"v_mfma_f32_16x16x32_bf16 v[188:191], a[164:167], a[140:143], v[188:191]\n"
"v_cvt_pk_bf16_f32 v168, v168, v169\n"
"v_cvt_pk_bf16_f32 v169, v170, v171\n"
"v_mfma_f32_16x16x32_bf16 a[240:243], a[252:255], v[192:195], a[240:243]\n"
"v_exp_f32 v172, v172\n"
"v_mfma_f32_16x16x32_bf16 a[240:243], a[252:255], v[204:207], a[240:243]\n"
"v_exp_f32 v173, v173\n"
"v_mfma_f32_16x16x32_bf16 a[244:247], a[252:255], v[196:199], a[244:247]\n"
"v_exp_f32 v174, v174\n"
"v_mfma_f32_16x16x32_bf16 a[244:247], a[252:255], v[208:211], a[244:247]\n"
"v_mul_f32 v180, s55, v180\n"
"v_mul_f32 v181, s55, v181\n"
"v_mfma_f32_16x16x32_bf16 a[248:251], a[252:255], v[200:203], a[248:251]\n"
"v_mul_f32 v182, s55, v182\n"
"v_mul_f32 v183, s55, v183\n"
"v_mfma_f32_16x16x32_bf16 a[248:251], a[252:255], v[212:215], a[248:251]\n"
"s_waitcnt lgkmcnt(4)\n"
"v_exp_f32 v180, v180\n"
"v_mfma_f32_16x16x32_bf16 a[0:3], a[144:147], v[192:195], a[0:3]\n"
"ds_read_b64_tr_b16 a[160:161], v241 offset:34304\n"
"ds_read_b64_tr_b16 a[162:163], v241 offset:34560\n"
"v_mfma_f32_16x16x32_bf16 a[0:3], a[148:151], v[204:207], a[0:3]\n"
"ds_read_b64_tr_b16 a[164:165], v241 offset:37376\n"
"ds_read_b64_tr_b16 a[166:167], v241 offset:37632\n"
"v_mfma_f32_16x16x32_bf16 a[48:51], a[148:151], v[208:211], a[48:51]\n"
"v_exp_f32 v181, v181\n"
"v_mfma_f32_16x16x32_bf16 a[48:51], a[144:147], v[196:199], a[48:51]\n"
"v_exp_f32 v182, v182\n"
"v_mfma_f32_16x16x32_bf16 a[192:195], a[144:147], v[200:203], a[192:195]\n"
"v_exp_f32 v183, v183\n"
"v_mfma_f32_16x16x32_bf16 a[192:195], a[148:151], v[212:215], a[192:195]\n"
"s_waitcnt lgkmcnt(4)\n"
"v_mul_f32 v184, s55, v184\n"
"v_mul_f32 v185, s55, v185\n"
"v_mfma_f32_16x16x32_bf16 a[4:7], a[152:155], v[192:195], a[4:7]\n"
"ds_read_b64_tr_b16 a[144:145], v241 offset:34336\n"
"ds_read_b64_tr_b16 a[146:147], v241 offset:34592\n"
"v_mfma_f32_16x16x32_bf16 a[4:7], a[156:159], v[204:207], a[4:7]\n"
"ds_read_b64_tr_b16 a[148:149], v241 offset:37408\n"
"ds_read_b64_tr_b16 a[150:151], v241 offset:37664\n"
"v_mfma_f32_16x16x32_bf16 a[52:55], a[156:159], v[208:211], a[52:55]\n"
"v_mul_f32 v186, s55, v186\n"
"v_mul_f32 v187, s55, v187\n"
"v_mfma_f32_16x16x32_bf16 a[52:55], a[152:155], v[196:199], a[52:55]\n"
"v_exp_f32 v175, v175\n"
"v_mfma_f32_16x16x32_bf16 a[196:199], a[152:155], v[200:203], a[196:199]\n"
"v_mul_f32 v176, s55, v176\n"
"v_mul_f32 v177, s55, v177\n"
"v_mfma_f32_16x16x32_bf16 a[196:199], a[156:159], v[212:215], a[196:199]\n"
"s_waitcnt lgkmcnt(4)\n"
"v_mul_f32 v178, s55, v178\n"
"v_mul_f32 v179, s55, v179\n"
"v_mfma_f32_16x16x32_bf16 a[8:11], a[160:163], v[192:195], a[8:11]\n"
"ds_read_b64_tr_b16 a[152:153], v241 offset:34816\n"
"ds_read_b64_tr_b16 a[154:155], v241 offset:35072\n"
"v_mfma_f32_16x16x32_bf16 a[8:11], a[164:167], v[204:207], a[8:11]\n"
"ds_read_b64_tr_b16 a[156:157], v241 offset:37888\n"
"ds_read_b64_tr_b16 a[158:159], v241 offset:38144\n"
"v_mfma_f32_16x16x32_bf16 a[56:59], a[164:167], v[208:211], a[56:59]\n"
"v_cvt_pk_bf16_f32 v170, v180, v181\n"
"v_cvt_pk_bf16_f32 v171, v182, v183\n"
"v_mfma_f32_16x16x32_bf16 a[56:59], a[160:163], v[196:199], a[56:59]\n"
"v_exp_f32 v184, v184\n"
"v_mfma_f32_16x16x32_bf16 a[200:203], a[160:163], v[200:203], a[200:203]\n"
"v_exp_f32 v185, v185\n"
"v_mfma_f32_16x16x32_bf16 a[200:203], a[164:167], v[212:215], a[200:203]\n"
"s_waitcnt lgkmcnt(4)\n"
"v_exp_f32 v186, v186\n"
"v_mfma_f32_16x16x32_bf16 a[12:15], a[144:147], v[192:195], a[12:15]\n"
"ds_read_b64_tr_b16 a[160:161], v241 offset:34848\n"
"ds_read_b64_tr_b16 a[162:163], v241 offset:35104\n"
"v_mfma_f32_16x16x32_bf16 a[12:15], a[148:151], v[204:207], a[12:15]\n"
"ds_read_b64_tr_b16 a[164:165], v241 offset:37920\n"
"ds_read_b64_tr_b16 a[166:167], v241 offset:38176\n"
"v_mfma_f32_16x16x32_bf16 a[60:63], a[148:151], v[208:211], a[60:63]\n"
"v_exp_f32 v187, v187\n"
"v_mfma_f32_16x16x32_bf16 a[60:63], a[144:147], v[196:199], a[60:63]\n"
"v_mul_f32 v188, s55, v188\n"
"v_mul_f32 v189, s55, v189\n"
"v_mfma_f32_16x16x32_bf16 a[204:207], a[144:147], v[200:203], a[204:207]\n"
"v_mul_f32 v190, s55, v190\n"
"v_mul_f32 v191, s55, v191\n"
"v_mfma_f32_16x16x32_bf16 a[204:207], a[148:151], v[212:215], a[204:207]\n"
"s_waitcnt lgkmcnt(4)\n"
"v_cvt_pk_bf16_f32 v148, v148, v149\n"
"v_cvt_pk_bf16_f32 v149, v150, v151\n"
"v_mfma_f32_16x16x32_bf16 a[16:19], a[152:155], v[192:195], a[16:19]\n"
"ds_read_b64_tr_b16 a[144:145], v241 offset:35328\n"
"ds_read_b64_tr_b16 a[146:147], v241 offset:35584\n"
"v_mfma_f32_16x16x32_bf16 a[16:19], a[156:159], v[204:207], a[16:19]\n"
"ds_read_b64_tr_b16 a[148:149], v241 offset:38400\n"
"ds_read_b64_tr_b16 a[150:151], v241 offset:38656\n"
"v_mfma_f32_16x16x32_bf16 a[64:67], a[156:159], v[208:211], a[64:67]\n"
"v_exp_f32 v152, v152\n"
"v_mfma_f32_16x16x32_bf16 a[64:67], a[152:155], v[196:199], a[64:67]\n"
"v_exp_f32 v153, v153\n"
"v_mfma_f32_16x16x32_bf16 a[208:211], a[152:155], v[200:203], a[208:211]\n"
"v_exp_f32 v154, v154\n"
"v_mfma_f32_16x16x32_bf16 a[208:211], a[156:159], v[212:215], a[208:211]\n"
"s_waitcnt lgkmcnt(4)\n"
"v_exp_f32 v155, v155\n"
"v_mfma_f32_16x16x32_bf16 a[20:23], a[160:163], v[192:195], a[20:23]\n"
"ds_read_b64_tr_b16 a[152:153], v241 offset:35360\n"
"ds_read_b64_tr_b16 a[154:155], v241 offset:35616\n"
"v_mfma_f32_16x16x32_bf16 a[20:23], a[164:167], v[204:207], a[20:23]\n"
"ds_read_b64_tr_b16 a[156:157], v241 offset:38432\n"
"ds_read_b64_tr_b16 a[158:159], v241 offset:38688\n"
"v_mfma_f32_16x16x32_bf16 a[68:71], a[164:167], v[208:211], a[68:71]\n"
"v_cvt_pk_bf16_f32 v150, v160, v161\n"
"v_cvt_pk_bf16_f32 v151, v162, v163\n"
"v_mfma_f32_16x16x32_bf16 a[68:71], a[160:163], v[196:199], a[68:71]\n"
"v_exp_f32 v164, v164\n"
"v_mfma_f32_16x16x32_bf16 a[212:215], a[160:163], v[200:203], a[212:215]\n"
"v_exp_f32 v165, v165\n"
"v_mfma_f32_16x16x32_bf16 a[212:215], a[164:167], v[212:215], a[212:215]\n"
"s_waitcnt lgkmcnt(4)\n"
"v_exp_f32 v166, v166\n"
"v_mfma_f32_16x16x32_bf16 a[24:27], a[144:147], v[192:195], a[24:27]\n"
"ds_read_b64_tr_b16 a[160:161], v241 offset:35840\n"
"ds_read_b64_tr_b16 a[162:163], v241 offset:36096\n"
"v_mfma_f32_16x16x32_bf16 a[24:27], a[148:151], v[204:207], a[24:27]\n"
"ds_read_b64_tr_b16 a[164:165], v241 offset:38912\n"
"ds_read_b64_tr_b16 a[166:167], v241 offset:39168\n"
"v_mfma_f32_16x16x32_bf16 a[168:171], a[148:151], v[208:211], a[168:171]\n"
"v_exp_f32 v167, v167\n"
"v_mfma_f32_16x16x32_bf16 a[168:171], a[144:147], v[196:199], a[168:171]\n"
"v_cvt_pk_bf16_f32 v172, v172, v173\n"
"v_cvt_pk_bf16_f32 v173, v174, v175\n"
"v_mfma_f32_16x16x32_bf16 a[216:219], a[144:147], v[200:203], a[216:219]\n"
"v_exp_f32 v176, v176\n"
"v_mfma_f32_16x16x32_bf16 a[216:219], a[148:151], v[212:215], a[216:219]\n"
"s_waitcnt lgkmcnt(4)\n"
"v_exp_f32 v177, v177\n"
"v_mfma_f32_16x16x32_bf16 a[28:31], a[152:155], v[192:195], a[28:31]\n"
"ds_read_b64_tr_b16 a[144:145], v241 offset:35872\n"
"ds_read_b64_tr_b16 a[146:147], v241 offset:36128\n"
"v_mfma_f32_16x16x32_bf16 a[28:31], a[156:159], v[204:207], a[28:31]\n"
"ds_read_b64_tr_b16 a[148:149], v241 offset:38944\n"
"ds_read_b64_tr_b16 a[150:151], v241 offset:39200\n"
"v_mfma_f32_16x16x32_bf16 a[172:175], a[156:159], v[208:211], a[172:175]\n"
"v_exp_f32 v178, v178\n"
"v_mfma_f32_16x16x32_bf16 a[172:175], a[152:155], v[196:199], a[172:175]\n"
"v_exp_f32 v179, v179\n"
"v_mfma_f32_16x16x32_bf16 a[220:223], a[152:155], v[200:203], a[220:223]\n"
"v_cvt_pk_bf16_f32 v174, v184, v185\n"
"v_cvt_pk_bf16_f32 v175, v186, v187\n"
"v_mfma_f32_16x16x32_bf16 a[220:223], a[156:159], v[212:215], a[220:223]\n"
"s_waitcnt lgkmcnt(4)\n"
"v_exp_f32 v188, v188\n"
"v_mfma_f32_16x16x32_bf16 a[32:35], a[160:163], v[192:195], a[32:35]\n"
"ds_read_b64_tr_b16 a[152:153], v241 offset:36352\n"
"ds_read_b64_tr_b16 a[154:155], v241 offset:36608\n"
"v_mfma_f32_16x16x32_bf16 a[32:35], a[164:167], v[204:207], a[32:35]\n"
"ds_read_b64_tr_b16 a[156:157], v241 offset:39424\n"
"ds_read_b64_tr_b16 a[158:159], v241 offset:39680\n"
"v_mfma_f32_16x16x32_bf16 a[176:179], a[164:167], v[208:211], a[176:179]\n"
"v_exp_f32 v189, v189\n"
"v_mfma_f32_16x16x32_bf16 a[176:179], a[160:163], v[196:199], a[176:179]\n"
"v_exp_f32 v190, v190\n"
"v_mfma_f32_16x16x32_bf16 a[224:227], a[160:163], v[200:203], a[224:227]\n"
"v_exp_f32 v191, v191\n"
"v_mfma_f32_16x16x32_bf16 a[224:227], a[164:167], v[212:215], a[224:227]\n"
"s_waitcnt lgkmcnt(4)\n"
"v_cvt_pk_bf16_f32 v152, v152, v153\n"
"v_cvt_pk_bf16_f32 v153, v154, v155\n"
"v_mfma_f32_16x16x32_bf16 a[36:39], a[144:147], v[192:195], a[36:39]\n"
"ds_read_b64_tr_b16 a[160:161], v241 offset:36384\n"
"ds_read_b64_tr_b16 a[162:163], v241 offset:36640\n"
"v_mfma_f32_16x16x32_bf16 a[36:39], a[148:151], v[204:207], a[36:39]\n"
"ds_read_b64_tr_b16 a[164:165], v241 offset:39456\n"
"ds_read_b64_tr_b16 a[166:167], v241 offset:39712\n"
"v_mfma_f32_16x16x32_bf16 a[180:183], a[148:151], v[208:211], a[180:183]\n"
"v_cvt_pk_bf16_f32 v154, v164, v165\n"
"v_cvt_pk_bf16_f32 v155, v166, v167\n"
"v_mfma_f32_16x16x32_bf16 a[180:183], a[144:147], v[196:199], a[180:183]\n"
"s_waitcnt vmcnt(0)\n"
"v_cvt_pk_bf16_f32 v176, v176, v177\n"
"v_cvt_pk_bf16_f32 v177, v178, v179\n"
"v_mfma_f32_16x16x32_bf16 a[228:231], a[144:147], v[200:203], a[228:231]\n"
"s_barrier\n"
"v_cvt_pk_bf16_f32 v178, v188, v189\n"
"v_cvt_pk_bf16_f32 v179, v190, v191\n"
"v_mfma_f32_16x16x32_bf16 a[228:231], a[148:151], v[212:215], a[228:231]\n"
"s_waitcnt lgkmcnt(4)\n"
"v_mfma_f32_16x16x32_bf16 a[40:43], a[152:155], v[192:195], a[40:43]\n"
"ds_read_b64_tr_b16 a[144:145], v241 offset:21504\n"
"ds_read_b64_tr_b16 a[146:147], v241 offset:21760\n"
"v_mfma_f32_16x16x32_bf16 a[40:43], a[156:159], v[204:207], a[40:43]\n"
"ds_read_b64_tr_b16 a[148:149], v241 offset:24576\n"
"ds_read_b64_tr_b16 a[150:151], v241 offset:24832\n"
"v_mfma_f32_16x16x32_bf16 a[184:187], a[156:159], v[208:211], a[184:187]\n"
"v_mfma_f32_16x16x32_bf16 a[184:187], a[152:155], v[196:199], a[184:187]\n"
"v_mfma_f32_16x16x32_bf16 a[232:235], a[152:155], v[200:203], a[232:235]\n"
"v_mfma_f32_16x16x32_bf16 a[232:235], a[156:159], v[212:215], a[232:235]\n"
"s_waitcnt lgkmcnt(4)\n"
"v_mfma_f32_16x16x32_bf16 a[44:47], a[160:163], v[192:195], a[44:47]\n"
"ds_read_b64_tr_b16 a[152:153], v241 offset:21536\n"
"ds_read_b64_tr_b16 a[154:155], v241 offset:21792\n"
"v_mfma_f32_16x16x32_bf16 a[44:47], a[164:167], v[204:207], a[44:47]\n"
"ds_read_b64_tr_b16 a[156:157], v241 offset:24608\n"
"ds_read_b64_tr_b16 a[158:159], v241 offset:24864\n"
"v_mfma_f32_16x16x32_bf16 a[188:191], a[164:167], v[208:211], a[188:191]\n"
"v_mfma_f32_16x16x32_bf16 a[188:191], a[160:163], v[196:199], a[188:191]\n"
"v_mfma_f32_16x16x32_bf16 a[236:239], a[160:163], v[200:203], a[236:239]\n"
"v_mfma_f32_16x16x32_bf16 a[236:239], a[164:167], v[212:215], a[236:239]\n"
"v_mfma_f32_16x16x32_bf16 a[240:243], a[252:255], v[144:147], a[240:243]\n"
"v_mfma_f32_16x16x32_bf16 a[240:243], a[252:255], v[168:171], a[240:243]\n"
"v_mfma_f32_16x16x32_bf16 a[244:247], a[252:255], v[148:151], a[244:247]\n"
"v_mfma_f32_16x16x32_bf16 a[244:247], a[252:255], v[172:175], a[244:247]\n"
"v_mfma_f32_16x16x32_bf16 a[248:251], a[252:255], v[152:155], a[248:251]\n"
"v_mfma_f32_16x16x32_bf16 a[248:251], a[252:255], v[176:179], a[248:251]\n"
"s_waitcnt lgkmcnt(4)\n"
"v_mfma_f32_16x16x32_bf16 a[0:3], a[144:147], v[144:147], a[0:3]\n"
"ds_read_b64_tr_b16 a[160:161], v241 offset:22016\n"
"ds_read_b64_tr_b16 a[162:163], v241 offset:22272\n"
"v_mfma_f32_16x16x32_bf16 a[0:3], a[148:151], v[168:171], a[0:3]\n"
"ds_read_b64_tr_b16 a[164:165], v241 offset:25088\n"
"ds_read_b64_tr_b16 a[166:167], v241 offset:25344\n"
"v_mfma_f32_16x16x32_bf16 a[48:51], a[148:151], v[172:175], a[48:51]\n"
"v_mfma_f32_16x16x32_bf16 a[48:51], a[144:147], v[148:151], a[48:51]\n"
"v_accvgpr_read_b32 v235, a240\n"
"v_subrev_u32 v246, s57, v235\n"
"v_mfma_f32_16x16x32_bf16 a[192:195], a[144:147], v[152:155], a[192:195]\n"
"v_max_u32 v238, v238, v246\n"
"v_mfma_f32_16x16x32_bf16 a[192:195], a[148:151], v[176:179], a[192:195]\n"
"s_waitcnt lgkmcnt(4)\n"
"v_rcp_f32 v235, v235\n"
"v_mfma_f32_16x16x32_bf16 a[4:7], a[152:155], v[144:147], a[4:7]\n"
"ds_read_b64_tr_b16 a[144:145], v241 offset:22048\n"
"ds_read_b64_tr_b16 a[146:147], v241 offset:22304\n"
"v_mfma_f32_16x16x32_bf16 a[4:7], a[156:159], v[168:171], a[4:7]\n"
"ds_read_b64_tr_b16 a[148:149], v241 offset:25120\n"
"ds_read_b64_tr_b16 a[150:151], v241 offset:25376\n"
"v_mfma_f32_16x16x32_bf16 a[52:55], a[156:159], v[172:175], a[52:55]\n"
"v_accvgpr_read_b32 v236, a244\n"
"v_subrev_u32 v247, s57, v236\n"
"v_mfma_f32_16x16x32_bf16 a[52:55], a[152:155], v[148:151], a[52:55]\n"
"v_max_u32 v238, v238, v247\n"
"v_accvgpr_read_b32 v237, a248\n"
"v_mfma_f32_16x16x32_bf16 a[196:199], a[152:155], v[152:155], a[196:199]\n"
"v_rcp_f32 v236, v236\n"
"v_mfma_f32_16x16x32_bf16 a[196:199], a[156:159], v[176:179], a[196:199]\n"
"s_waitcnt lgkmcnt(4)\n"
"v_subrev_u32 v248, s57, v237\n"
"v_max_u32 v238, v238, v248\n"
"v_mfma_f32_16x16x32_bf16 a[8:11], a[160:163], v[144:147], a[8:11]\n"
"ds_read_b64_tr_b16 a[152:153], v241 offset:22528\n"
"ds_read_b64_tr_b16 a[154:155], v241 offset:22784\n"
"v_mfma_f32_16x16x32_bf16 a[8:11], a[164:167], v[168:171], a[8:11]\n"
"ds_read_b64_tr_b16 a[156:157], v241 offset:25600\n"
"ds_read_b64_tr_b16 a[158:159], v241 offset:25856\n"
"v_mfma_f32_16x16x32_bf16 a[56:59], a[164:167], v[172:175], a[56:59]\n"
"v_rcp_f32 v237, v237\n"
"v_mfma_f32_16x16x32_bf16 a[56:59], a[160:163], v[148:151], a[56:59]\n"
"v_mfma_f32_16x16x32_bf16 a[200:203], a[160:163], v[152:155], a[200:203]\n"
"v_mfma_f32_16x16x32_bf16 a[200:203], a[164:167], v[176:179], a[200:203]\n"
"s_waitcnt lgkmcnt(4)\n"
"v_mfma_f32_16x16x32_bf16 a[12:15], a[144:147], v[144:147], a[12:15]\n"
"ds_read_b64_tr_b16 a[160:161], v241 offset:22560\n"
"ds_read_b64_tr_b16 a[162:163], v241 offset:22816\n"
"v_mfma_f32_16x16x32_bf16 a[12:15], a[148:151], v[168:171], a[12:15]\n"
"ds_read_b64_tr_b16 a[164:165], v241 offset:25632\n"
"ds_read_b64_tr_b16 a[166:167], v241 offset:25888\n"
"v_mfma_f32_16x16x32_bf16 a[60:63], a[148:151], v[172:175], a[60:63]\n"
"v_mfma_f32_16x16x32_bf16 a[60:63], a[144:147], v[148:151], a[60:63]\n"
"v_mfma_f32_16x16x32_bf16 a[204:207], a[144:147], v[152:155], a[204:207]\n"
"v_mfma_f32_16x16x32_bf16 a[204:207], a[148:151], v[176:179], a[204:207]\n"
"s_waitcnt lgkmcnt(4)\n"
"v_mfma_f32_16x16x32_bf16 a[16:19], a[152:155], v[144:147], a[16:19]\n"
"ds_read_b64_tr_b16 a[144:145], v241 offset:23040\n"
"ds_read_b64_tr_b16 a[146:147], v241 offset:23296\n"
"v_mfma_f32_16x16x32_bf16 a[16:19], a[156:159], v[168:171], a[16:19]\n"
"ds_read_b64_tr_b16 a[148:149], v241 offset:26112\n"
"ds_read_b64_tr_b16 a[150:151], v241 offset:26368\n"
"v_mfma_f32_16x16x32_bf16 a[64:67], a[156:159], v[172:175], a[64:67]\n"
"v_mfma_f32_16x16x32_bf16 a[64:67], a[152:155], v[148:151], a[64:67]\n"
"v_accvgpr_read_b32 v0, a0\n"
"v_accvgpr_read_b32 v1, a1\n"
"v_mfma_f32_16x16x32_bf16 a[208:211], a[152:155], v[152:155], a[208:211]\n"
"v_accvgpr_read_b32 v2, a2\n"
"v_accvgpr_read_b32 v3, a3\n"
"v_mfma_f32_16x16x32_bf16 a[208:211], a[156:159], v[176:179], a[208:211]\n"
"s_waitcnt lgkmcnt(4)\n"
"v_accvgpr_read_b32 v4, a4\n"
"v_accvgpr_read_b32 v5, a5\n"
"v_mfma_f32_16x16x32_bf16 a[20:23], a[160:163], v[144:147], a[20:23]\n"
"ds_read_b64_tr_b16 a[152:153], v241 offset:23072\n"
"ds_read_b64_tr_b16 a[154:155], v241 offset:23328\n"
"v_mfma_f32_16x16x32_bf16 a[20:23], a[164:167], v[168:171], a[20:23]\n"
"ds_read_b64_tr_b16 a[156:157], v241 offset:26144\n"
"ds_read_b64_tr_b16 a[158:159], v241 offset:26400\n"
"v_mfma_f32_16x16x32_bf16 a[68:71], a[164:167], v[172:175], a[68:71]\n"
"v_accvgpr_read_b32 v6, a6\n"
"v_accvgpr_read_b32 v7, a7\n"
"v_mfma_f32_16x16x32_bf16 a[68:71], a[160:163], v[148:151], a[68:71]\n"
"v_accvgpr_read_b32 v8, a8\n"
"v_accvgpr_read_b32 v9, a9\n"
"v_mfma_f32_16x16x32_bf16 a[212:215], a[160:163], v[152:155], a[212:215]\n"
"v_accvgpr_read_b32 v10, a10\n"
"v_accvgpr_read_b32 v11, a11\n"
"v_mfma_f32_16x16x32_bf16 a[212:215], a[164:167], v[176:179], a[212:215]\n"
"s_waitcnt lgkmcnt(4)\n"
"v_accvgpr_read_b32 v12, a12\n"
"v_accvgpr_read_b32 v13, a13\n"
"v_mfma_f32_16x16x32_bf16 a[24:27], a[144:147], v[144:147], a[24:27]\n"
"ds_read_b64_tr_b16 a[160:161], v241 offset:23552\n"
"ds_read_b64_tr_b16 a[162:163], v241 offset:23808\n"
"v_mfma_f32_16x16x32_bf16 a[24:27], a[148:151], v[168:171], a[24:27]\n"
"ds_read_b64_tr_b16 a[164:165], v241 offset:26624\n"
"ds_read_b64_tr_b16 a[166:167], v241 offset:26880\n"
"v_mfma_f32_16x16x32_bf16 a[168:171], a[148:151], v[172:175], a[168:171]\n"
"v_accvgpr_read_b32 v14, a14\n"
"v_accvgpr_read_b32 v15, a15\n"
"v_mfma_f32_16x16x32_bf16 a[168:171], a[144:147], v[148:151], a[168:171]\n"
"v_mul_f32 v0, v235, v0\n"
"v_mul_f32 v1, v235, v1\n"
"v_mfma_f32_16x16x32_bf16 a[216:219], a[144:147], v[152:155], a[216:219]\n"
"v_mul_f32 v2, v235, v2\n"
"v_mul_f32 v3, v235, v3\n"
"v_mfma_f32_16x16x32_bf16 a[216:219], a[148:151], v[176:179], a[216:219]\n"
"s_waitcnt lgkmcnt(4)\n"
"v_cvt_pk_bf16_f32 v0, v0, v1\n"
"v_cvt_pk_bf16_f32 v1, v2, v3\n"
"v_mfma_f32_16x16x32_bf16 a[28:31], a[152:155], v[144:147], a[28:31]\n"
"ds_read_b64_tr_b16 a[144:145], v241 offset:23584\n"
"ds_read_b64_tr_b16 a[146:147], v241 offset:23840\n"
"v_mfma_f32_16x16x32_bf16 a[28:31], a[156:159], v[168:171], a[28:31]\n"
"ds_read_b64_tr_b16 a[148:149], v241 offset:26656\n"
"ds_read_b64_tr_b16 a[150:151], v241 offset:26912\n"
"v_mfma_f32_16x16x32_bf16 a[172:175], a[156:159], v[172:175], a[172:175]\n"
"ds_write_b64 v218, v[0:1] offset:12288\n"
"v_mul_f32 v4, v235, v4\n"
"v_mfma_f32_16x16x32_bf16 a[172:175], a[152:155], v[148:151], a[172:175]\n"
"v_mul_f32 v5, v235, v5\n"
"v_mul_f32 v6, v235, v6\n"
"v_mfma_f32_16x16x32_bf16 a[220:223], a[152:155], v[152:155], a[220:223]\n"
"v_mul_f32 v7, v235, v7\n"
"v_cvt_pk_bf16_f32 v4, v4, v5\n"
"v_mfma_f32_16x16x32_bf16 a[220:223], a[156:159], v[176:179], a[220:223]\n"
"s_waitcnt lgkmcnt(5)\n"
"v_cvt_pk_bf16_f32 v5, v6, v7\n"
"v_mul_f32 v8, v235, v8\n"
"v_mfma_f32_16x16x32_bf16 a[32:35], a[160:163], v[144:147], a[32:35]\n"
"ds_read_b64_tr_b16 a[152:153], v241 offset:24064\n"
"ds_read_b64_tr_b16 a[154:155], v241 offset:24320\n"
"v_mfma_f32_16x16x32_bf16 a[32:35], a[164:167], v[168:171], a[32:35]\n"
"ds_read_b64_tr_b16 a[156:157], v241 offset:27136\n"
"ds_read_b64_tr_b16 a[158:159], v241 offset:27392\n"
"v_mfma_f32_16x16x32_bf16 a[176:179], a[164:167], v[172:175], a[176:179]\n"
"ds_write_b64 v218, v[4:5] offset:12320\n"
"v_mul_f32 v9, v235, v9\n"
"v_mfma_f32_16x16x32_bf16 a[176:179], a[160:163], v[148:151], a[176:179]\n"
"v_mul_f32 v10, v235, v10\n"
"v_mul_f32 v11, v235, v11\n"
"v_mfma_f32_16x16x32_bf16 a[224:227], a[160:163], v[152:155], a[224:227]\n"
"v_cvt_pk_bf16_f32 v8, v8, v9\n"
"v_cvt_pk_bf16_f32 v9, v10, v11\n"
"v_mfma_f32_16x16x32_bf16 a[224:227], a[164:167], v[176:179], a[224:227]\n"
"s_waitcnt lgkmcnt(6)\n"
"ds_write_b64 v218, v[8:9] offset:12352\n"
"v_mul_f32 v12, v235, v12\n"
"v_mfma_f32_16x16x32_bf16 a[36:39], a[144:147], v[144:147], a[36:39]\n"
"ds_read_b64_tr_b16 a[160:161], v241 offset:24096\n"
"ds_read_b64_tr_b16 a[162:163], v241 offset:24352\n"
"v_mfma_f32_16x16x32_bf16 a[36:39], a[148:151], v[168:171], a[36:39]\n"
"ds_read_b64_tr_b16 a[164:165], v241 offset:27168\n"
"ds_read_b64_tr_b16 a[166:167], v241 offset:27424\n"
"v_mfma_f32_16x16x32_bf16 a[180:183], a[148:151], v[172:175], a[180:183]\n"
"v_mul_f32 v13, v235, v13\n"
"v_mul_f32 v14, v235, v14\n"
"v_mfma_f32_16x16x32_bf16 a[180:183], a[144:147], v[148:151], a[180:183]\n"
"v_mul_f32 v15, v235, v15\n"
"v_cvt_pk_bf16_f32 v12, v12, v13\n"
"v_mfma_f32_16x16x32_bf16 a[228:231], a[144:147], v[152:155], a[228:231]\n"
"v_cvt_pk_bf16_f32 v13, v14, v15\n"
"v_accvgpr_read_b32 v48, a48\n"
"v_mfma_f32_16x16x32_bf16 a[228:231], a[148:151], v[176:179], a[228:231]\n"
"s_waitcnt lgkmcnt(6)\n"
"ds_write_b64 v218, v[12:13] offset:12384\n"
"v_accvgpr_read_b32 v49, a49\n"
"v_mfma_f32_16x16x32_bf16 a[40:43], a[152:155], v[144:147], a[40:43]\n"
"ds_read_b128 v[0:3], v219 offset:12288\n"
"ds_read_b128 v[4:7], v219 offset:13440\n"
"v_mfma_f32_16x16x32_bf16 a[40:43], a[156:159], v[168:171], a[40:43]\n"
"v_accvgpr_read_b32 v50, a50\n"
"v_accvgpr_read_b32 v51, a51\n"
"v_mfma_f32_16x16x32_bf16 a[184:187], a[156:159], v[172:175], a[184:187]\n"
"v_accvgpr_read_b32 v52, a52\n"
"v_accvgpr_read_b32 v53, a53\n"
"v_mfma_f32_16x16x32_bf16 a[184:187], a[152:155], v[148:151], a[184:187]\n"
"s_waitcnt lgkmcnt(0)\n"
"buffer_store_dwordx4 v[0:3], v220, s[48:51], s63 offen offset:0 sc1\n"
"v_mfma_f32_16x16x32_bf16 a[232:235], a[152:155], v[152:155], a[232:235]\n"
"buffer_store_dwordx4 v[4:7], v220, s[48:51], s63 offen offset:3072 sc1\n"
"v_mfma_f32_16x16x32_bf16 a[232:235], a[156:159], v[176:179], a[232:235]\n"
"v_accvgpr_read_b32 v54, a54\n"
"v_accvgpr_read_b32 v55, a55\n"
"v_mfma_f32_16x16x32_bf16 a[44:47], a[160:163], v[144:147], a[44:47]\n"
"v_accvgpr_read_b32 v56, a56\n"
"v_accvgpr_read_b32 v57, a57\n"
"v_mfma_f32_16x16x32_bf16 a[44:47], a[164:167], v[168:171], a[44:47]\n"
"v_accvgpr_read_b32 v58, a58\n"
"v_accvgpr_read_b32 v59, a59\n"
"v_mfma_f32_16x16x32_bf16 a[188:191], a[164:167], v[172:175], a[188:191]\n"
"v_accvgpr_read_b32 v60, a60\n"
"v_accvgpr_read_b32 v61, a61\n"
"v_mfma_f32_16x16x32_bf16 a[188:191], a[160:163], v[148:151], a[188:191]\n"
"v_accvgpr_read_b32 v62, a62\n"
"v_accvgpr_read_b32 v63, a63\n"
"v_mfma_f32_16x16x32_bf16 a[236:239], a[160:163], v[152:155], a[236:239]\n"
"v_mul_f32 v48, v236, v48\n"
"v_mul_f32 v49, v236, v49\n"
"v_mfma_f32_16x16x32_bf16 a[236:239], a[164:167], v[176:179], a[236:239]\n"
"v_mul_f32 v50, v236, v50\n"
"v_mul_f32 v51, v236, v51\n"
"v_cvt_pk_bf16_f32 v48, v48, v49\n"
"v_cvt_pk_bf16_f32 v49, v50, v51\n"
"v_mul_f32 v52, v236, v52\n"
"v_mul_f32 v53, v236, v53\n"
"v_mul_f32 v54, v236, v54\n"
"v_mul_f32 v55, v236, v55\n"
"v_cvt_pk_bf16_f32 v52, v52, v53\n"
"v_cvt_pk_bf16_f32 v53, v54, v55\n"
"v_mul_f32 v56, v236, v56\n"
"v_mul_f32 v57, v236, v57\n"
"v_mul_f32 v58, v236, v58\n"
"v_mul_f32 v59, v236, v59\n"
"v_cvt_pk_bf16_f32 v56, v56, v57\n"
"v_cvt_pk_bf16_f32 v57, v58, v59\n"
"v_mul_f32 v60, v236, v60\n"
"v_mul_f32 v61, v236, v61\n"
"v_mul_f32 v62, v236, v62\n"
"v_mul_f32 v63, v236, v63\n"
"v_cvt_pk_bf16_f32 v60, v60, v61\n"
"v_cvt_pk_bf16_f32 v61, v62, v63\n"
"ds_write_b64 v218, v[48:49] offset:15360\n"
"ds_write_b64 v218, v[52:53] offset:15392\n"
"ds_write_b64 v218, v[56:57] offset:15424\n"
"ds_write_b64 v218, v[60:61] offset:15456\n"
"v_accvgpr_read_b32 v96, a192\n"
"ds_read_b128 v[48:51], v219 offset:15360\n"
"ds_read_b128 v[52:55], v219 offset:16512\n"
"v_accvgpr_read_b32 v97, a193\n"
"v_accvgpr_read_b32 v98, a194\n"
"v_accvgpr_read_b32 v99, a195\n"
"s_waitcnt lgkmcnt(0)\n"
"buffer_store_dwordx4 v[48:51], v220, s[48:51], s64 offen offset:0 sc1\n"
"buffer_store_dwordx4 v[52:55], v220, s[48:51], s64 offen offset:3072 sc1\n"
"v_accvgpr_read_b32 v100, a196\n"
"v_accvgpr_read_b32 v101, a197\n"
"v_accvgpr_read_b32 v102, a198\n"
"v_accvgpr_read_b32 v103, a199\n"
"v_accvgpr_read_b32 v104, a200\n"
"v_accvgpr_read_b32 v105, a201\n"
"v_accvgpr_read_b32 v106, a202\n"
"v_accvgpr_read_b32 v107, a203\n"
"v_accvgpr_read_b32 v108, a204\n"
"v_accvgpr_read_b32 v109, a205\n"
"v_accvgpr_read_b32 v110, a206\n"
"v_accvgpr_read_b32 v111, a207\n"
"v_mul_f32 v96, v237, v96\n"
"v_mul_f32 v97, v237, v97\n"
"v_mul_f32 v98, v237, v98\n"
"v_mul_f32 v99, v237, v99\n"
"v_cvt_pk_bf16_f32 v96, v96, v97\n"
"v_cvt_pk_bf16_f32 v97, v98, v99\n"
"v_mul_f32 v100, v237, v100\n"
"v_mul_f32 v101, v237, v101\n"
"v_mul_f32 v102, v237, v102\n"
"v_mul_f32 v103, v237, v103\n"
"v_cvt_pk_bf16_f32 v100, v100, v101\n"
"v_cvt_pk_bf16_f32 v101, v102, v103\n"
"v_mul_f32 v104, v237, v104\n"
"v_mul_f32 v105, v237, v105\n"
"v_mul_f32 v106, v237, v106\n"
"v_mul_f32 v107, v237, v107\n"
"v_cvt_pk_bf16_f32 v104, v104, v105\n"
"v_cvt_pk_bf16_f32 v105, v106, v107\n"
"v_mul_f32 v108, v237, v108\n"
"v_mul_f32 v109, v237, v109\n"
"v_mul_f32 v110, v237, v110\n"
"v_mul_f32 v111, v237, v111\n"
"v_cvt_pk_bf16_f32 v108, v108, v109\n"
"v_cvt_pk_bf16_f32 v109, v110, v111\n"
"ds_write_b64 v218, v[96:97] offset:18432\n"
"ds_write_b64 v218, v[100:101] offset:18464\n"
"ds_write_b64 v218, v[104:105] offset:18496\n"
"ds_write_b64 v218, v[108:109] offset:18528\n"
"v_accvgpr_read_b32 v16, a16\n"
"ds_read_b128 v[96:99], v219 offset:18432\n"
"ds_read_b128 v[100:103], v219 offset:19584\n"
"v_accvgpr_read_b32 v17, a17\n"
"v_accvgpr_read_b32 v18, a18\n"
"v_accvgpr_read_b32 v19, a19\n"
"s_waitcnt lgkmcnt(0)\n"
"buffer_store_dwordx4 v[96:99], v220, s[48:51], s65 offen offset:0 sc1\n"
"buffer_store_dwordx4 v[100:103], v220, s[48:51], s65 offen offset:3072 sc1\n"
"v_accvgpr_read_b32 v20, a20\n"
"v_accvgpr_read_b32 v21, a21\n"
"v_accvgpr_read_b32 v22, a22\n"
"v_accvgpr_read_b32 v23, a23\n"
"v_accvgpr_read_b32 v24, a24\n"
"v_accvgpr_read_b32 v25, a25\n"
"v_accvgpr_read_b32 v26, a26\n"
"v_accvgpr_read_b32 v27, a27\n"
"v_accvgpr_read_b32 v28, a28\n"
"v_accvgpr_read_b32 v29, a29\n"
"v_accvgpr_read_b32 v30, a30\n"
"v_accvgpr_read_b32 v31, a31\n"
"v_mul_f32 v16, v235, v16\n"
"v_mul_f32 v17, v235, v17\n"
"v_mul_f32 v18, v235, v18\n"
"v_mul_f32 v19, v235, v19\n"
"v_cvt_pk_bf16_f32 v16, v16, v17\n"
"v_cvt_pk_bf16_f32 v17, v18, v19\n"
"v_mul_f32 v20, v235, v20\n"
"v_mul_f32 v21, v235, v21\n"
"v_mul_f32 v22, v235, v22\n"
"v_mul_f32 v23, v235, v23\n"
"v_cvt_pk_bf16_f32 v20, v20, v21\n"
"v_cvt_pk_bf16_f32 v21, v22, v23\n"
"v_mul_f32 v24, v235, v24\n"
"v_mul_f32 v25, v235, v25\n"
"v_mul_f32 v26, v235, v26\n"
"v_mul_f32 v27, v235, v27\n"
"v_cvt_pk_bf16_f32 v24, v24, v25\n"
"v_cvt_pk_bf16_f32 v25, v26, v27\n"
"v_mul_f32 v28, v235, v28\n"
"v_mul_f32 v29, v235, v29\n"
"v_mul_f32 v30, v235, v30\n"
"v_mul_f32 v31, v235, v31\n"
"v_cvt_pk_bf16_f32 v28, v28, v29\n"
"v_cvt_pk_bf16_f32 v29, v30, v31\n"
"ds_write_b64 v218, v[16:17] offset:12288\n"
"ds_write_b64 v218, v[20:21] offset:12320\n"
"ds_write_b64 v218, v[24:25] offset:12352\n"
"ds_write_b64 v218, v[28:29] offset:12384\n"
"v_accvgpr_read_b32 v64, a64\n"
"ds_read_b128 v[16:19], v219 offset:12288\n"
"ds_read_b128 v[20:23], v219 offset:13440\n"
"v_accvgpr_read_b32 v65, a65\n"
"v_accvgpr_read_b32 v66, a66\n"
"v_accvgpr_read_b32 v67, a67\n"
"s_waitcnt lgkmcnt(0)\n"
"buffer_store_dwordx4 v[16:19], v220, s[48:51], s63 offen offset:128 sc1\n"
"buffer_store_dwordx4 v[20:23], v220, s[48:51], s63 offen offset:3200 sc1\n"
"v_accvgpr_read_b32 v68, a68\n"
"v_accvgpr_read_b32 v69, a69\n"
"v_accvgpr_read_b32 v70, a70\n"
"v_accvgpr_read_b32 v71, a71\n"
"v_accvgpr_read_b32 v72, a168\n"
"v_accvgpr_read_b32 v73, a169\n"
"v_accvgpr_read_b32 v74, a170\n"
"v_accvgpr_read_b32 v75, a171\n"
"v_accvgpr_read_b32 v76, a172\n"
"v_accvgpr_read_b32 v77, a173\n"
"v_accvgpr_read_b32 v78, a174\n"
"v_accvgpr_read_b32 v79, a175\n"
"v_mul_f32 v64, v236, v64\n"
"v_mul_f32 v65, v236, v65\n"
"v_mul_f32 v66, v236, v66\n"
"v_mul_f32 v67, v236, v67\n"
"v_cvt_pk_bf16_f32 v64, v64, v65\n"
"v_cvt_pk_bf16_f32 v65, v66, v67\n"
"v_mul_f32 v68, v236, v68\n"
"v_mul_f32 v69, v236, v69\n"
"v_mul_f32 v70, v236, v70\n"
"v_mul_f32 v71, v236, v71\n"
"v_cvt_pk_bf16_f32 v68, v68, v69\n"
"v_cvt_pk_bf16_f32 v69, v70, v71\n"
"v_mul_f32 v72, v236, v72\n"
"v_mul_f32 v73, v236, v73\n"
"v_mul_f32 v74, v236, v74\n"
"v_mul_f32 v75, v236, v75\n"
"v_cvt_pk_bf16_f32 v72, v72, v73\n"
"v_cvt_pk_bf16_f32 v73, v74, v75\n"
"v_mul_f32 v76, v236, v76\n"
"v_mul_f32 v77, v236, v77\n"
"v_mul_f32 v78, v236, v78\n"
"v_mul_f32 v79, v236, v79\n"
"v_cvt_pk_bf16_f32 v76, v76, v77\n"
"v_cvt_pk_bf16_f32 v77, v78, v79\n"
"ds_write_b64 v218, v[64:65] offset:15360\n"
"ds_write_b64 v218, v[68:69] offset:15392\n"
"ds_write_b64 v218, v[72:73] offset:15424\n"
"ds_write_b64 v218, v[76:77] offset:15456\n"
"v_accvgpr_read_b32 v112, a208\n"
"ds_read_b128 v[64:67], v219 offset:15360\n"
"ds_read_b128 v[68:71], v219 offset:16512\n"
"v_accvgpr_read_b32 v113, a209\n"
"v_accvgpr_read_b32 v114, a210\n"
"v_accvgpr_read_b32 v115, a211\n"
"s_waitcnt lgkmcnt(0)\n"
"buffer_store_dwordx4 v[64:67], v220, s[48:51], s64 offen offset:128 sc1\n"
"buffer_store_dwordx4 v[68:71], v220, s[48:51], s64 offen offset:3200 sc1\n"
"v_accvgpr_read_b32 v116, a212\n"
"v_accvgpr_read_b32 v117, a213\n"
"v_accvgpr_read_b32 v118, a214\n"
"v_accvgpr_read_b32 v119, a215\n"
"v_accvgpr_read_b32 v120, a216\n"
"v_accvgpr_read_b32 v121, a217\n"
"v_accvgpr_read_b32 v122, a218\n"
"v_accvgpr_read_b32 v123, a219\n"
"v_accvgpr_read_b32 v124, a220\n"
"v_accvgpr_read_b32 v125, a221\n"
"v_accvgpr_read_b32 v126, a222\n"
"v_accvgpr_read_b32 v127, a223\n"
"v_mul_f32 v112, v237, v112\n"
"v_mul_f32 v113, v237, v113\n"
"v_mul_f32 v114, v237, v114\n"
"v_mul_f32 v115, v237, v115\n"
"v_cvt_pk_bf16_f32 v112, v112, v113\n"
"v_cvt_pk_bf16_f32 v113, v114, v115\n"
"v_mul_f32 v116, v237, v116\n"
"v_mul_f32 v117, v237, v117\n"
"v_mul_f32 v118, v237, v118\n"
"v_mul_f32 v119, v237, v119\n"
"v_cvt_pk_bf16_f32 v116, v116, v117\n"
"v_cvt_pk_bf16_f32 v117, v118, v119\n"
"v_mul_f32 v120, v237, v120\n"
"v_mul_f32 v121, v237, v121\n"
"v_mul_f32 v122, v237, v122\n"
"v_mul_f32 v123, v237, v123\n"
"v_cvt_pk_bf16_f32 v120, v120, v121\n"
"v_cvt_pk_bf16_f32 v121, v122, v123\n"
"v_mul_f32 v124, v237, v124\n"
"v_mul_f32 v125, v237, v125\n"
"v_mul_f32 v126, v237, v126\n"
"v_mul_f32 v127, v237, v127\n"
"v_cvt_pk_bf16_f32 v124, v124, v125\n"
"v_cvt_pk_bf16_f32 v125, v126, v127\n"
"ds_write_b64 v218, v[112:113] offset:18432\n"
"ds_write_b64 v218, v[116:117] offset:18464\n"
"ds_write_b64 v218, v[120:121] offset:18496\n"
"ds_write_b64 v218, v[124:125] offset:18528\n"
"v_accvgpr_read_b32 v32, a32\n"
"ds_read_b128 v[112:115], v219 offset:18432\n"
"ds_read_b128 v[116:119], v219 offset:19584\n"
"v_accvgpr_read_b32 v33, a33\n"
"v_accvgpr_read_b32 v34, a34\n"
"v_accvgpr_read_b32 v35, a35\n"
"s_waitcnt lgkmcnt(0)\n"
"buffer_store_dwordx4 v[112:115], v220, s[48:51], s65 offen offset:128 sc1\n"
"buffer_store_dwordx4 v[116:119], v220, s[48:51], s65 offen offset:3200 sc1\n"
"v_accvgpr_read_b32 v36, a36\n"
"v_accvgpr_read_b32 v37, a37\n"
"v_accvgpr_read_b32 v38, a38\n"
"v_accvgpr_read_b32 v39, a39\n"
"v_accvgpr_read_b32 v40, a40\n"
"v_accvgpr_read_b32 v41, a41\n"
"v_accvgpr_read_b32 v42, a42\n"
"v_accvgpr_read_b32 v43, a43\n"
"v_accvgpr_read_b32 v44, a44\n"
"v_accvgpr_read_b32 v45, a45\n"
"v_accvgpr_read_b32 v46, a46\n"
"v_accvgpr_read_b32 v47, a47\n"
"v_mul_f32 v32, v235, v32\n"
"v_mul_f32 v33, v235, v33\n"
"v_mul_f32 v34, v235, v34\n"
"v_mul_f32 v35, v235, v35\n"
"v_cvt_pk_bf16_f32 v32, v32, v33\n"
"v_cvt_pk_bf16_f32 v33, v34, v35\n"
"v_mul_f32 v36, v235, v36\n"
"v_mul_f32 v37, v235, v37\n"
"v_mul_f32 v38, v235, v38\n"
"v_mul_f32 v39, v235, v39\n"
"v_cvt_pk_bf16_f32 v36, v36, v37\n"
"v_cvt_pk_bf16_f32 v37, v38, v39\n"
"v_mul_f32 v40, v235, v40\n"
"v_mul_f32 v41, v235, v41\n"
"v_mul_f32 v42, v235, v42\n"
"v_mul_f32 v43, v235, v43\n"
"v_cvt_pk_bf16_f32 v40, v40, v41\n"
"v_cvt_pk_bf16_f32 v41, v42, v43\n"
"v_mul_f32 v44, v235, v44\n"
"v_mul_f32 v45, v235, v45\n"
"v_mul_f32 v46, v235, v46\n"
"v_mul_f32 v47, v235, v47\n"
"v_cvt_pk_bf16_f32 v44, v44, v45\n"
"v_cvt_pk_bf16_f32 v45, v46, v47\n"
"ds_write_b64 v218, v[32:33] offset:12288\n"
"ds_write_b64 v218, v[36:37] offset:12320\n"
"ds_write_b64 v218, v[40:41] offset:12352\n"
"ds_write_b64 v218, v[44:45] offset:12384\n"
"v_accvgpr_read_b32 v80, a176\n"
"ds_read_b128 v[32:35], v219 offset:12288\n"
"ds_read_b128 v[36:39], v219 offset:13440\n"
"v_accvgpr_read_b32 v81, a177\n"
"v_accvgpr_read_b32 v82, a178\n"
"v_accvgpr_read_b32 v83, a179\n"
"s_waitcnt lgkmcnt(0)\n"
"buffer_store_dwordx4 v[32:35], v220, s[48:51], s63 offen offset:256 sc1\n"
"buffer_store_dwordx4 v[36:39], v220, s[48:51], s63 offen offset:3328 sc1\n"
"v_accvgpr_read_b32 v84, a180\n"
"v_accvgpr_read_b32 v85, a181\n"
"v_accvgpr_read_b32 v86, a182\n"
"v_accvgpr_read_b32 v87, a183\n"
"v_accvgpr_read_b32 v88, a184\n"
"v_accvgpr_read_b32 v89, a185\n"
"v_accvgpr_read_b32 v90, a186\n"
"v_accvgpr_read_b32 v91, a187\n"
"v_accvgpr_read_b32 v92, a188\n"
"v_accvgpr_read_b32 v93, a189\n"
"v_accvgpr_read_b32 v94, a190\n"
"v_accvgpr_read_b32 v95, a191\n"
"v_mul_f32 v80, v236, v80\n"
"v_mul_f32 v81, v236, v81\n"
"v_mul_f32 v82, v236, v82\n"
"v_mul_f32 v83, v236, v83\n"
"v_cvt_pk_bf16_f32 v80, v80, v81\n"
"v_cvt_pk_bf16_f32 v81, v82, v83\n"
"v_mul_f32 v84, v236, v84\n"
"v_mul_f32 v85, v236, v85\n"
"v_mul_f32 v86, v236, v86\n"
"v_mul_f32 v87, v236, v87\n"
"v_cvt_pk_bf16_f32 v84, v84, v85\n"
"v_cvt_pk_bf16_f32 v85, v86, v87\n"
"v_mul_f32 v88, v236, v88\n"
"v_mul_f32 v89, v236, v89\n"
"v_mul_f32 v90, v236, v90\n"
"v_mul_f32 v91, v236, v91\n"
"v_cvt_pk_bf16_f32 v88, v88, v89\n"
"v_cvt_pk_bf16_f32 v89, v90, v91\n"
"v_mul_f32 v92, v236, v92\n"
"v_mul_f32 v93, v236, v93\n"
"v_mul_f32 v94, v236, v94\n"
"v_mul_f32 v95, v236, v95\n"
"v_cvt_pk_bf16_f32 v92, v92, v93\n"
"v_cvt_pk_bf16_f32 v93, v94, v95\n"
"ds_write_b64 v218, v[80:81] offset:15360\n"
"ds_write_b64 v218, v[84:85] offset:15392\n"
"ds_write_b64 v218, v[88:89] offset:15424\n"
"ds_write_b64 v218, v[92:93] offset:15456\n"
"v_accvgpr_read_b32 v128, a224\n"
"ds_read_b128 v[80:83], v219 offset:15360\n"
"ds_read_b128 v[84:87], v219 offset:16512\n"
"v_accvgpr_read_b32 v129, a225\n"
"v_accvgpr_read_b32 v130, a226\n"
"v_accvgpr_read_b32 v131, a227\n"
"s_waitcnt lgkmcnt(0)\n"
"buffer_store_dwordx4 v[80:83], v220, s[48:51], s64 offen offset:256 sc1\n"
"buffer_store_dwordx4 v[84:87], v220, s[48:51], s64 offen offset:3328 sc1\n"
"v_accvgpr_read_b32 v132, a228\n"
"v_accvgpr_read_b32 v133, a229\n"
"v_accvgpr_read_b32 v134, a230\n"
"v_accvgpr_read_b32 v135, a231\n"
"v_accvgpr_read_b32 v136, a232\n"
"v_accvgpr_read_b32 v137, a233\n"
"v_accvgpr_read_b32 v138, a234\n"
"v_accvgpr_read_b32 v139, a235\n"
"v_accvgpr_read_b32 v140, a236\n"
"v_accvgpr_read_b32 v141, a237\n"
"v_accvgpr_read_b32 v142, a238\n"
"v_accvgpr_read_b32 v143, a239\n"
"v_mul_f32 v128, v237, v128\n"
"v_mul_f32 v129, v237, v129\n"
"v_mul_f32 v130, v237, v130\n"
"v_mul_f32 v131, v237, v131\n"
"v_cvt_pk_bf16_f32 v128, v128, v129\n"
"v_cvt_pk_bf16_f32 v129, v130, v131\n"
"v_mul_f32 v132, v237, v132\n"
"v_mul_f32 v133, v237, v133\n"
"v_mul_f32 v134, v237, v134\n"
"v_mul_f32 v135, v237, v135\n"
"v_cvt_pk_bf16_f32 v132, v132, v133\n"
"v_cvt_pk_bf16_f32 v133, v134, v135\n"
"v_mul_f32 v136, v237, v136\n"
"v_mul_f32 v137, v237, v137\n"
"v_mul_f32 v138, v237, v138\n"
"v_mul_f32 v139, v237, v139\n"
"v_cvt_pk_bf16_f32 v136, v136, v137\n"
"v_cvt_pk_bf16_f32 v137, v138, v139\n"
"v_mul_f32 v140, v237, v140\n"
"v_mul_f32 v141, v237, v141\n"
"v_mul_f32 v142, v237, v142\n"
"v_mul_f32 v143, v237, v143\n"
"v_cvt_pk_bf16_f32 v140, v140, v141\n"
"v_cvt_pk_bf16_f32 v141, v142, v143\n"
"ds_write_b64 v218, v[128:129] offset:18432\n"
"ds_write_b64 v218, v[132:133] offset:18464\n"
"ds_write_b64 v218, v[136:137] offset:18496\n"
"ds_write_b64 v218, v[140:141] offset:18528\n"
"ds_read_b128 v[128:131], v219 offset:18432\n"
"ds_read_b128 v[132:135], v219 offset:19584\n"
"s_waitcnt lgkmcnt(0)\n"
"buffer_store_dwordx4 v[128:131], v220, s[48:51], s65 offen offset:256 sc1\n"
"buffer_store_dwordx4 v[132:135], v220, s[48:51], s65 offen offset:3328 sc1\n"
"L_ce_%=:\n"
"v_cmp_lt_u32 vcc, s58, v238\n"
"s_nop 4\n"
"s_cmp_lg_u64 vcc, 0\n"
"s_cselect_b32 %0, 1, 0\n"
"s_barrier\n"
      : "=&s"(bad)
      : "s"(q), "s"(k), "s"(v), "s"(o), "s"(hb), "s"(w), "s"(half), "s"(idx)
      : "v0","v1","v2","v3","v4","v5","v6","v7","v8","v9","v10","v11","v12","v13","v14","v15","v16","v17","v18","v19","v20","v21","v22","v23","v24","v25",
      "v26","v27","v28","v29","v30","v31","v32","v33","v34","v35","v36","v37","v38","v39","v40","v41","v42","v43","v44","v45","v46","v47","v48","v49","v50",
      "v51","v52","v53","v54","v55","v56","v57","v58","v59","v60","v61","v62","v63","v64","v65","v66","v67","v68","v69","v70","v71","v72","v73","v74","v75",
      "v76","v77","v78","v79","v80","v81","v82","v83","v84","v85","v86","v87","v88","v89","v90","v91","v92","v93","v94","v95","v96","v97","v98","v99","v100",
      "v101","v102","v103","v104","v105","v106","v107","v108","v109","v110","v111","v112","v113","v114","v115","v116","v117","v118","v119","v120","v121",
      "v122","v123","v124","v125","v126","v127","v128","v129","v130","v131","v132","v133","v134","v135","v136","v137","v138","v139","v140","v141","v142",
      "v143","v144","v145","v146","v147","v148","v149","v150","v151","v152","v153","v154","v155","v156","v157","v158","v159","v160","v161","v162","v163",
      "v164","v165","v166","v167","v168","v169","v170","v171","v172","v173","v174","v175","v176","v177","v178","v179","v180","v181","v182","v183","v184",
      "v185","v186","v187","v188","v189","v190","v191","v192","v193","v194","v195","v196","v197","v198","v199","v200","v201","v202","v203","v204","v205",
      "v206","v207","v208","v209","v210","v211","v212","v213","v214","v215","v216","v217","v218","v219","v220","v221","v222","v223","v224","v225","v226",
      "v227","v228","v229","v230","v231","v232","v233","v234","v235","v236","v237","v238","v239","v240","v241","v242","v243","v244","v245","v246","v247",
      "v248","v249","v250","v251","v252","v253","v254","v255","a0","a1","a2","a3","a4","a5","a6","a7","a8","a9","a10","a11","a12","a13","a14","a15","a16",
      "a17","a18","a19","a20","a21","a22","a23","a24","a25","a26","a27","a28","a29","a30","a31","a32","a33","a34","a35","a36","a37","a38","a39","a40","a41",
      "a42","a43","a44","a45","a46","a47","a48","a49","a50","a51","a52","a53","a54","a55","a56","a57","a58","a59","a60","a61","a62","a63","a64","a65","a66",
      "a67","a68","a69","a70","a71","a72","a73","a74","a75","a76","a77","a78","a79","a80","a81","a82","a83","a84","a85","a86","a87","a88","a89","a90","a91",
      "a92","a93","a94","a95","a96","a97","a98","a99","a100","a101","a102","a103","a104","a105","a106","a107","a108","a109","a110","a111","a112","a113",
      "a114","a115","a116","a117","a118","a119","a120","a121","a122","a123","a124","a125","a126","a127","a128","a129","a130","a131","a132","a133","a134",
      "a135","a136","a137","a138","a139","a140","a141","a142","a143","a144","a145","a146","a147","a148","a149","a150","a151","a152","a153","a154","a155",
      "a156","a157","a158","a159","a160","a161","a162","a163","a164","a165","a166","a167","a168","a169","a170","a171","a172","a173","a174","a175","a176",
      "a177","a178","a179","a180","a181","a182","a183","a184","a185","a186","a187","a188","a189","a190","a191","a192","a193","a194","a195","a196","a197",
      "a198","a199","a200","a201","a202","a203","a204","a205","a206","a207","a208","a209","a210","a211","a212","a213","a214","a215","a216","a217","a218",
      "a219","a220","a221","a222","a223","a224","a225","a226","a227","a228","a229","a230","a231","a232","a233","a234","a235","a236","a237","a238","a239",
      "a240","a241","a242","a243","a244","a245","a246","a247","a248","a249","a250","a251","a252","a253","a254","a255","s36","s37","s38","s39","s40","s41",
      "s42","s43","s44","s45","s46","s47","s48","s49","s50","s51","s52","s53","s54","s55","s56","s57","s58","s59","s60","s61","s62","s63","s64","s65","s66",
      "s67","s68","s69","s70","s71","s72","s73","s74","s75","s76","s77","s78","s79","s80","s81","s82","s83","s84","s85","s86","s87","s88","s89","s90","s91",
      "s92","s93","s94","s95","s96","s97","s98","s99","vcc","scc","memory");
  if (bad) {
    asm volatile("s_waitcnt vmcnt(0)" ::: "memory");
    char* vt = lds + 40000u * w + 32u * (w & 1u);
#pragma unroll 1
    for (unsigned u = 0; u < 2; ++u) rows_true_max(q, k, v, o, hb + u, half * 192u + 48u * w, vt);
  }
}

extern "C" void kernel_launch(void* const* d_in, const int* in_sizes, int n_in, void* d_out, int out_size, void* d_ws, size_t ws_size,
                              hipStream_t stream) {
  (void)hipFuncSetAttribute((const void*)attn_fwd_d192, hipFuncAttributeMaxDynamicSharedMemorySize, 160000);
  attn_fwd_d192<<<256, 256, 160000, stream>>>((const char*)d_in[0], (const char*)d_in[1], (const char*)d_in[2], (char*)d_out);
}
```
